```python
import torch

def _softmax(x, axis=-1):
    return torch.softmax(x, dim=axis)
def _gelu(x):
    return torch.nn.functional.gelu(x, approximate="tanh")
def _silu(x):
    return torch.nn.functional.silu(x)
def _sigmoid(x):
    return torch.sigmoid(x)
def _layernorm(x, axis=-1, eps=1e-5):
    m = x.mean(dim=axis, keepdim=True)
    v = x.var(dim=axis, keepdim=True, unbiased=False)
    return (x - m) / torch.sqrt(v + eps)
def _scatter_add(tgt, idx, src, axis=0):
    return tgt.clone().index_add_(axis, idx, src)
def _segment_sum(src, seg, n):
    out = torch.zeros((n,) + src.shape[1:], dtype=src.dtype)
    return out.index_add_(0, seg, src)
def _pad(x, pw):
    flat = tuple(v for pair in reversed(pw) for v in pair)
    return torch.nn.functional.pad(x, flat)

kernel_name = 'recurrent_scan__ssm_conv_h3__geglu_ffn__prompt'

def setup_inputs():

    _w = (lambda a: (lambda t: t.to(torch.bfloat16) if t.is_floating_point() else t)(torch.as_tensor(a)))
    def _A_setup():
        B, S, D = 1, 2048, 512
        ks = 4
        return {
            "q": _w((lambda *s: torch.randn(*s, dtype=torch.bfloat16) * 0.05)(B, S, D)),
            "k": _w((lambda *s: torch.randn(*s, dtype=torch.bfloat16) * 0.2)(B, S, D)),
            "v": _w((lambda *s: torch.randn(*s, dtype=torch.bfloat16) * 0.2)(B, S, D)),
            "shift_k": _w((lambda *s: torch.randn(*s, dtype=torch.bfloat16) * 0.2)(D, ks) * 0.5),
            "ssm_k": _w((lambda *s: torch.randn(*s, dtype=torch.bfloat16) * 0.2)(D, S) * (1.0 / S ** 0.5)),
            "ssm_D": _w((lambda *s: torch.randn(*s, dtype=torch.bfloat16) * 0.2)(D)),
        }
    _A_in = _A_setup()

    def _B_setup():
        B, S, D, F = 1, 2048, 512, 1792
        return {
            "x": _w((lambda *s: torch.randn(*s, dtype=torch.bfloat16) * 0.05)(B, S, D)),
            "w_gate": _w((lambda *s: torch.randn(*s, dtype=torch.bfloat16) * 0.1)(D, F)),
            "w_up": _w((lambda *s: torch.randn(*s, dtype=torch.bfloat16) * 0.1)(D, F)),
            "w_down": _w((lambda *s: torch.randn(*s, dtype=torch.bfloat16) * 0.1)(F, D)),
        }
    _B_in = _B_setup()

    return {
        "A_q": _A_in["q"],
        "A_k": _A_in["k"],
        "A_v": _A_in["v"],
        "A_shift_k": _A_in["shift_k"],
        "A_ssm_k": _A_in["ssm_k"],
        "A_ssm_D": _A_in["ssm_D"],
        "B_x": _B_in["x"],
        "B_w_gate": _B_in["w_gate"],
        "B_w_up": _B_in["w_up"],
        "B_w_down": _B_in["w_down"],
    }

def reference(A_q, A_k, A_v, A_shift_k, A_ssm_k, A_ssm_D, B_x, B_w_gate, B_w_up, B_w_down):
    def _A_ref(q, k, v, shift_k, ssm_k, ssm_D):
        B, S, D = q.shape
        ks = shift_k.shape[-1]
        k_pad = _pad(k, ((0, 0), (ks - 1, 0), (0, 0)))
        k_shift = torch.zeros_like(k)
        for t in range(ks):
            k_shift = k_shift + k_pad[:, t:t + S, :] * shift_k[:, ks - 1 - t][None, None, :]
        kv = k_shift * v
        L = 2 * S
        kv_pad = _pad(kv, ((0, 0), (0, S), (0, 0)))
        kf_pad = _pad(ssm_k, ((0, 0), (0, S)))
        kv_f = (lambda x, axes=(-2,-1): torch.fft.rfft2(x.float(), dim=axes))(kv_pad[:, None, :, :], axes=(1, 2))
        kf_f = (lambda x, axes=(-2,-1): torch.fft.rfft2(x.float(), dim=axes))(kf_pad[:, None, :], axes=(1, 2))
        kf_f = (lambda x, *d: x.permute(*d))(kf_f, 1, 2, 0)
        y_f = kv_f * kf_f[None, :, :, :]
        y = (lambda x, s, axes=(-2,-1): torch.fft.irfft2(x, s=s, dim=axes).to(torch.bfloat16))(y_f, (1, L), axes=(1, 2))[:, 0, :S, :]
        y = y + kv * ssm_D[None, None, :]
        return q * y

    def _B_ref(x, w_gate, w_up, w_down):
        return (_gelu(x @ w_gate) * (x @ w_up)) @ w_down

    x = A_q
    xn = _layernorm(x, axis=-1)
    s = _A_ref(xn, A_k, A_v, A_shift_k, A_ssm_k, A_ssm_D)
    g = _silu(_B_ref(xn, B_w_gate, B_w_up, B_w_down))
    return x + s * g
```

```cpp
#include <hip/hip_runtime.h>
#include <stdint.h>

#define LAS __attribute__((address_space(3)))
#define GAS __attribute__((address_space(1)))
#define DI __device__ __forceinline__
typedef __bf16 bf8 __attribute__((ext_vector_type(8)));
typedef __bf16 bf2 __attribute__((ext_vector_type(2)));
typedef short s4 __attribute__((ext_vector_type(4)));
typedef float f4 __attribute__((ext_vector_type(4)));
typedef float f2 __attribute__((ext_vector_type(2)));
typedef unsigned u4 __attribute__((ext_vector_type(4)));
typedef unsigned u2 __attribute__((ext_vector_type(2)));
typedef unsigned long long u64;

namespace {
constexpr unsigned WS_EPOCH = 0;
constexpr unsigned WS_FK = 4096;
constexpr unsigned WS_FS = WS_FK + 256 * 128;
constexpr unsigned WS_FH = WS_FS + 256 * 128;
constexpr unsigned WS_FY = WS_FH + 256 * 128;
constexpr unsigned WS_ROW = WS_FY + 256 * 128;
constexpr unsigned WS_COL = WS_ROW + 2048 * 8;
constexpr unsigned WS_KVT = 1u << 20;
constexpr unsigned WS_YT = 3u << 20;
constexpr unsigned WS_H = 5u << 20;
constexpr unsigned WS_PROF = 32u << 20;
constexpr unsigned SLOT = 49152;
constexpr unsigned L_MISC = 3 * SLOT;
constexpr unsigned L_RS = L_MISC + 12288;
constexpr unsigned L_CS = L_RS + 1024;
constexpr unsigned L_CTL = L_CS + 1024;
constexpr unsigned C_LAND = L_CTL, C_CONS = L_CTL + 40, C_BSYNC = L_CTL + 72, C_WSYNC = L_CTL + 76, C_CVIN = L_CTL + 80,
                   C_EPI = L_CTL + 84, C_HPUB = L_CTL + 88, C_GO = L_CTL + 92, C_WD = L_CTL + 96, L_TAG = L_CTL + 128;
constexpr unsigned L_CV = 2 * SLOT;
constexpr unsigned CV_COPY = 4160, CV_UR = 4 * CV_COPY, CV_CH = CV_UR + 4608;
constexpr unsigned L_YI = L_CV, L_SI = L_CV + 8192, L_XI = L_CV + 16384;
constexpr unsigned LDS_BYTES = 163840;
constexpr int SPIN_CAP = 1 << 18;

DI float bflo(unsigned u) { return __uint_as_float(u << 16); }
DI float bfhi(unsigned u) { return __uint_as_float(u & 0xffff0000u); }
DI unsigned pk2(float a, float b) { f2 v = {a, b}; return __builtin_bit_cast(unsigned, __builtin_convertvector(v, bf2)); }
DI float dot2(unsigned a, bf2 b, float c) { return __builtin_amdgcn_fdot2_f32_bf16(__builtin_bit_cast(bf2, a), b, c, false); }
DI float rbf(float a) { return bflo(pk2(a, a)); }
DI f4 mfma(bf8 a, bf8 b, f4 c) { return __builtin_amdgcn_mfma_f32_16x16x32_bf16(a, b, c, 0, 0, 0); }
DI bf8 lds128(unsigned a) { return *(const LAS bf8*)(size_t)a; }
DI u4 lds128u(unsigned a) { return *(const LAS u4*)(size_t)a; }
DI f4 lds128f(unsigned a) { return *(const LAS f4*)(size_t)a; }
DI void sts128(unsigned a, u4 v) { *(LAS u4*)(size_t)a = v; }
DI void sts128f(unsigned a, f4 v) { *(LAS f4*)(size_t)a = v; }
DI bf8 ldtr(unsigned a0, unsigned a1) {
  s4 lo = __builtin_amdgcn_ds_read_tr16_b64_v4i16((LAS s4*)(size_t)a0);
  s4 hi = __builtin_amdgcn_ds_read_tr16_b64_v4i16((LAS s4*)(size_t)a1);
  return __builtin_bit_cast(bf8, __builtin_shufflevector(lo, hi, 0, 1, 2, 3, 4, 5, 6, 7));
}
#define WAITV(n) asm volatile("s_waitcnt vmcnt(%0)" ::"i"(n) : "memory")
#define WAITL(n) asm volatile("s_waitcnt lgkmcnt(%0)" ::"i"(n) : "memory")
#define SB() __builtin_amdgcn_sched_barrier(0)
#define OPAQUE(x) asm volatile("" : "+v"(x))
#define BAR() do { asm volatile("s_waitcnt lgkmcnt(0)" ::: "memory"); __builtin_amdgcn_s_barrier(); asm volatile("" ::: "memory"); } while (0)

DI void glds2(unsigned v0, unsigned v1, const char* sb, unsigned l) {
  asm volatile("s_mov_b32 m0, %3\n\ts_nop 0\n\tglobal_load_lds_dwordx4 %0, %2\n\tglobal_load_lds_dwordx4 %1, %2 offset:1024"
               ::"v"(v0), "v"(v1), "s"(sb), "s"(l) : "memory");
}
DI void glds4(unsigned v0, unsigned v1, unsigned v2, unsigned v3, const char* sb, unsigned l) {
  asm volatile("s_mov_b32 m0, %5\n\ts_nop 0\n\tglobal_load_lds_dwordx4 %0, %4\n\tglobal_load_lds_dwordx4 %1, %4 offset:1024\n\t"
               "global_load_lds_dwordx4 %2, %4 offset:2048\n\tglobal_load_lds_dwordx4 %3, %4 offset:3072"
               ::"v"(v0), "v"(v1), "v"(v2), "v"(v3), "s"(sb), "s"(l) : "memory");
}
DI void flag_set(char* ws, unsigned off, u64 tag) {
  __hip_atomic_store((GAS u64*)(ws + off), tag, __ATOMIC_RELAXED, __HIP_MEMORY_SCOPE_AGENT);
}
DI void poll(const char* ws, unsigned off, u64 tag) {
#pragma unroll 1
  for (int it = 0; it < SPIN_CAP; ++it) {
    u64 x = __hip_atomic_load((GAS u64*)(ws + off), __ATOMIC_RELAXED, __HIP_MEMORY_SCOPE_AGENT);
    if (__all(x == tag)) break;
    __builtin_amdgcn_s_sleep(4);
  }
  asm volatile("" ::: "memory");
}
DI float fadd(float a, float b) { float r; asm("v_add_f32 %0, %1, %2" : "=v"(r) : "v"(a), "v"(b)); return r; }
DI float fsub(float a, float b) { float r; asm("v_sub_f32 %0, %1, %2" : "=v"(r) : "v"(a), "v"(b)); return r; }
DI float fmul(float a, float b) { float r; asm("v_mul_f32 %0, %1, %2" : "=v"(r) : "v"(a), "v"(b)); return r; }
DI float ffma(float a, float b, float c) { float r; asm("v_fma_f32 %0, %1, %2, %3" : "=v"(r) : "v"(a), "v"(b), "v"(c)); return r; }
template <int CTRL> DI float dpp_add(float v) {
  return v + __int_as_float(__builtin_amdgcn_update_dpp(0, __float_as_int(v), CTRL, 0xf, 0xf, true));
}
DI float wave_sum(float v) {
  v = dpp_add<0xB1>(v); v = dpp_add<0x4E>(v); v = dpp_add<0x141>(v); v = dpp_add<0x140>(v);
  const int i = __float_as_int(v);
  return (__int_as_float(__builtin_amdgcn_readlane(i, 0)) + __int_as_float(__builtin_amdgcn_readlane(i, 16))) +
         (__int_as_float(__builtin_amdgcn_readlane(i, 32)) + __int_as_float(__builtin_amdgcn_readlane(i, 48)));
}
DI void ctl_add(unsigned addr) {
  asm volatile("s_waitcnt lgkmcnt(0)\n\ts_mov_b64 exec, 1\n\tds_add_u32 %0, %1\n\ts_mov_b64 exec, -1" ::"v"(addr), "v"(1u) : "memory");
}
DI unsigned ctl_get(unsigned addr) {
  unsigned s, t;
  asm volatile("ds_read_b32 %1, %2\n\ts_waitcnt lgkmcnt(0)\n\tv_readfirstlane_b32 %0, %1\n\ts_nop 1" : "=s"(s), "=&v"(t) : "v"(addr) : "memory");
  return s;
}
DI void ctl_wait(unsigned addr, unsigned target) {
#pragma unroll 1
  for (int it = 0; it < SPIN_CAP; ++it) {
    if (ctl_get(addr) >= target) break;
    __builtin_amdgcn_s_sleep(1);
  }
}
#define STAMP(i)
#define CNOW(v)
#define CSTAMP(i)

struct Frag { bf8 x[4]; bf8 w[2][4]; };
#define SIDE_DELAY 0

__global__ __launch_bounds__(512, 2) void h3_geglu_fused(const char* q, const char* kk, const char* vv, const char* shk, const char* ssk,
                                                         const char* sD, const char* wg, const char* wu, const char* wd, char* out, char* ws) {
  extern __shared__ __attribute__((aligned(16))) char smem[];
  const int tid = threadIdx.x, lane = tid & 63, t8 = tid & 255;
  const int w = __builtin_amdgcn_readfirstlane(tid >> 6), gw = w & 3;
  const int b = blockIdx.x, xcd = b & 7, r = b >> 3;
  const bool g1 = r < 28;
  const int l15 = lane & 15, g = lane >> 4;
  const __amdgpu_buffer_rsrc_t wsr = __builtin_amdgcn_make_buffer_rsrc(ws, 0, 16 << 20, 0x00020000);
  const int pnl = r >> 2;
  const int pm = (xcd >> 1) * 4 + (r & 3), pn = (xcd & 1) * 7 + pnl;
  const int rt = xcd * 4 + (r & 3), ct2 = r >> 2;
  const int rgrp = (xcd >> 1) * 64 + (xcd & 1) * 32 + r;
  auto wdstage = [&](unsigned dv0, int kq, int i) {
    const int s6 = i % 6;
    glds4(dv0, dv0 + 7168, dv0 + 2 * 7168, dv0 + 3 * 7168, wd + ct2 * 128 + (kq * 14 + i) * 32768, (s6 < 3 ? s6 * 4096 : SLOT + (s6 - 3) * 4096) + kq * 12288);
  };
  unsigned xv[4], wv[4];
  const char* xb = q + pm * 131072 + (w & 1) * 65536;
  const char* gb = wg + pn * 256 + (w & 1) * (32 * 3584);
  const char* ub = wu + pn * 256 + (w & 1) * (32 * 3584);
  auto stage_half = [&](int st, int hf) {
    const unsigned l = (st % 3) * SLOT + (w & 1) * 8192 + hf * 4096;
    glds4(xv[0], xv[1], xv[2], xv[3], xb + st * 128 + hf * 32768, l);
    glds4(wv[0], wv[1], wv[2], wv[3], gb + st * (64 * 3584) + hf * (16 * 3584), l + 16384);
    glds4(wv[0], wv[1], wv[2], wv[3], ub + st * (64 * 3584) + hf * (16 * 3584), l + 32768);
  };
  auto stage = [&](int st) { stage_half(st, 0); stage_half(st, 1); };
  if ((w >> 1) == 2) {
#pragma unroll
    for (int p = 0; p < 4; ++p) {
      xv[p] = (p * 8 + (lane >> 3)) * 1024 + (((lane & 7) ^ (((p & 1) << 2) | g)) << 4) - p * 1024;
      wv[p] = (p * 4 + g) * 3584 + ((l15 ^ ((g | ((p >> 1) << 2)) << 1)) << 4) - p * 1024;
    }
    if (g1) stage(0);
  }
  if (tid < 32) *(LAS unsigned*)(size_t)(L_CTL + tid * 4) = 0;
  u64 tag;
  auto tag_fetch = [&]() {
    asm volatile("s_load_dwordx2 %0, %1, 0x0\n\ts_waitcnt lgkmcnt(0)" : "=s"(tag) : "s"(ws) : "memory");
    tag += 1;
    *(LAS u64*)(size_t)L_TAG = tag;
  };
  auto tag_get = [&]() {
    const u2 t = *(const volatile LAS u2*)(size_t)L_TAG;
    tag = ((u64)(unsigned)__builtin_amdgcn_readfirstlane(t[1]) << 32) | (unsigned)__builtin_amdgcn_readfirstlane(t[0]);
  };
  u4 xr[4];
  auto rows_load = [&]() {
    asm volatile("global_load_dwordx4 %0, %4, %5\n\tglobal_load_dwordx4 %1, %4, %5 offset:1024\n\tglobal_load_dwordx4 %2, %4, %5 offset:2048\n\t"
                 "global_load_dwordx4 %3, %4, %5 offset:3072"
                 : "=&v"(xr[0]), "=&v"(xr[1]), "=&v"(xr[2]), "=&v"(xr[3]) : "v"(lane * 16), "s"(q + (rgrp * 8 + (w & 1) * 4) * 1024) : "memory");
  };
  auto rows_stat = [&]() {
    asm volatile("" : "+v"(xr[0]), "+v"(xr[1]), "+v"(xr[2]), "+v"(xr[3])::"memory");
    float e[4][8], mu[4], va[4];
#pragma unroll
    for (int i = 0; i < 4; ++i) {
#pragma unroll
      for (int j = 0; j < 4; ++j) { e[i][2 * j] = bflo(xr[i][j]); e[i][2 * j + 1] = bfhi(xr[i][j]); }
      mu[i] = fadd(fadd(fadd(e[i][0], e[i][1]), fadd(e[i][2], e[i][3])), fadd(fadd(e[i][4], e[i][5]), fadd(e[i][6], e[i][7])));
    }
    auto fold4 = [&](const float (&v)[4]) {
      const auto s0 = __builtin_amdgcn_permlane16_swap(__float_as_uint(v[0]), __float_as_uint(v[1]), false, false);
      const auto s1 = __builtin_amdgcn_permlane16_swap(__float_as_uint(v[2]), __float_as_uint(v[3]), false, false);
      const float p0 = fadd(__uint_as_float(s0[0]), __uint_as_float(s0[1])), p1 = fadd(__uint_as_float(s1[0]), __uint_as_float(s1[1]));
      const auto s2 = __builtin_amdgcn_permlane32_swap(__float_as_uint(p0), __float_as_uint(p1), false, false);
      float t = fadd(__uint_as_float(s2[0]), __uint_as_float(s2[1]));
      t = dpp_add<0xB1>(t); t = dpp_add<0x4E>(t); t = dpp_add<0x141>(t); t = dpp_add<0x140>(t);
      return t * (1.0f / 512.0f);
    };
    const float mg = fold4(mu);
#pragma unroll
    for (int i = 0; i < 4; ++i) {
      const float m = __int_as_float(__builtin_amdgcn_readlane(__float_as_int(mg), 16 * i));
      float s = 0.f;
#pragma unroll
      for (int j = 0; j < 8; ++j) { const float d = e[i][j] - m; s = ffma(d, d, s); }
      va[i] = s;
    }
    const float vg = fold4(va);
    const u2 o = {__float_as_uint(rbf(mg)), __float_as_uint(__builtin_amdgcn_rcpf(rbf(__builtin_amdgcn_sqrtf(rbf(rbf(vg) + 1e-5f)))))};
    if (l15 == 0) __builtin_amdgcn_raw_buffer_store_b64(o, wsr, WS_ROW + (rgrp * 8 + (w & 1) * 4 + g) * 8, 0, 16);
    asm volatile("" ::: "memory");
  };

  BAR();

  if (w < 4) {
    const int qq = l15 >> 2, pp = lane & 3;
    if (g1) {
      __builtin_amdgcn_s_setprio(2);
      const int wm = w >> 1, wn = w & 1;
      f4 acc[2][4][4];
#pragma unroll
      for (int s = 0; s < 2; ++s)
#pragma unroll
        for (int e = 0; e < 4; ++e)
#pragma unroll
          for (int i = 0; i < 4; ++i) acc[s][e][i] = (f4){0.f, 0.f, 0.f, 0.f};
      unsigned xa[2], wa[4];
      xa[0] = (wm * 64 + l15) * 128 + ((g ^ (l15 >> 1)) << 4); xa[1] = xa[0] ^ 64;
#pragma unroll
      for (int e = 0; e < 4; ++e) wa[e] = (8 * g + qq) * 256 + (((4 * wn + e) ^ (qq | ((g & 1) << 2))) << 5) + pp * 8;
      Frag f0, f1;
      s4 lo_;
      auto ldpart = [&](Frag& f, int ks, int j) {
        const unsigned o = 16384 + ks * 8192;
        if (j == 0) f.x[0] = lds128(xa[ks]);
        if (j >= 1 && j <= 16) {
          const int s = (j - 1) >> 3, e = ((j - 1) >> 1) & 3;
          if (j & 1) lo_ = __builtin_amdgcn_ds_read_tr16_b64_v4i16((LAS s4*)(size_t)(wa[e] + o + s * 16384));
          else {
            const s4 hi = __builtin_amdgcn_ds_read_tr16_b64_v4i16((LAS s4*)(size_t)(wa[e] + o + s * 16384 + 1024));
            f.w[s][e] = __builtin_bit_cast(bf8, __builtin_shufflevector(lo_, hi, 0, 1, 2, 3, 4, 5, 6, 7));
          }
        }
        if (j >= 17 && j < 20) f.x[j - 16] = lds128(xa[ks] + (j - 16) * 2048);
      };
      auto mm1 = [&](const Frag& f, int j) { acc[(j >> 2) & 1][j & 3][j >> 3] = mfma(f.w[(j >> 2) & 1][j & 3], f.x[j >> 3], acc[(j >> 2) & 1][j & 3][j >> 3]); };
      auto second = [&](int st) {
        const int d = (st == 2 || st == 5) ? -2 * (int)SLOT : (int)SLOT;
        const unsigned ca = C_LAND + 4 * st + 4;
        unsigned lv;
#pragma unroll
        for (int j = 0; j < 32; ++j) {
          if (j == 31) {
            unsigned sv;
            asm volatile("s_waitcnt lgkmcnt(0)\n\tv_readfirstlane_b32 %0, %1\n\ts_nop 1" : "=s"(sv) : "v"(lv) : "memory");
            if (sv < 2) ctl_wait(ca, 2);
            SB();
          }
          mm1(f0, j);
          if (j < 20) ldpart(f1, 1, j);
          if (j == 20) { xa[0] += d; OPAQUE(xa[0]); xa[1] += d; OPAQUE(xa[1]); }
          if (j == 21) { wa[0] += d; OPAQUE(wa[0]); wa[1] += d; OPAQUE(wa[1]); }
          if (j == 22) { wa[2] += d; OPAQUE(wa[2]); wa[3] += d; OPAQUE(wa[3]); }
          if (j == 25) { ctl_add(ca + (C_CONS - C_LAND - 4)); asm volatile("ds_read_b32 %0, %1" : "=v"(lv) : "v"(ca) : "memory"); }
          SB();
        }
      };
      OPAQUE(xa[0]); OPAQUE(xa[1]); OPAQUE(wa[0]); OPAQUE(wa[1]); OPAQUE(wa[2]); OPAQUE(wa[3]);
      ctl_wait(C_LAND, 2);
      STAMP(2);
#pragma unroll
      for (int j = 0; j < 20; ++j) ldpart(f0, 0, j);
      SB();
      second(0);
#pragma unroll 1
      for (int st = 1; st < 8; ++st) {
#pragma unroll
        for (int j = 0; j < 32; ++j) { mm1(f1, j); if (j < 20) ldpart(f0, 0, j); SB(); }
        second(st);
      }
      STAMP(16);
      constexpr float C0 = -2.0f * 0.7978845608028654f * 1.4426950408889634f, C1 = C0 * 0.044715f;
      f4 cs[2][4];
      f2 rs[4];
      int ln1 = __builtin_amdgcn_mbcnt_hi(-1, __builtin_amdgcn_mbcnt_lo(-1, 0));
      OPAQUE(ln1);
      const int lane = ln1, l15 = lane & 15, g = lane >> 4;
#pragma unroll
      for (int j = 0; j < 32; ++j) {
        mm1(f1, j);
        if (j < 8) cs[j >> 2][j & 3] = lds128f(L_CS + ((j >> 2) * 128 + wn * 64 + (j & 3) * 16 + 4 * g) * 4);
        if (j >= 8 && j < 12) rs[j - 8] = *(const LAS f2*)(size_t)(L_RS + (wm * 64 + (j - 8) * 16 + l15) * 8);
        SB();
      }
      STAMP(3);
      const unsigned hso = WS_H + ((pm * 8 + wm * 4) * 56 + pn * 4 + wn * 2) * 1024 + lane * 16;
#pragma unroll
      for (int i = 0; i < 4; ++i) {
        const float m = -rs[i][0], rr = rs[i][1];
        const float k0 = C0 * rr, k1 = C1 * rr * rr * rr, i2 = __builtin_amdgcn_rcpf(rr * rr);
        const f2 m2 = {m, m}, k02 = {k0, k0}, k12 = {k1, k1}, i22 = {i2, i2};
        f2 a[8], c[8], z[8];
        SB();
#pragma unroll
        for (int j = 0; j < 8; ++j) {
          const int e = j >> 1, h = (j & 1) * 2;
          a[j] = __builtin_elementwise_fma(m2, (f2){cs[0][e][h], cs[0][e][h + 1]}, (f2){acc[0][e][i][h], acc[0][e][i][h + 1]});
        }
        SB();
#pragma unroll
        for (int j = 0; j < 8; ++j) z[j] = a[j] * a[j];
        SB();
#pragma unroll
        for (int j = 0; j < 8; ++j) z[j] = __builtin_elementwise_fma(z[j], k12, k02);
        SB();
#pragma unroll
        for (int j = 0; j < 8; ++j) z[j] = z[j] * a[j];
        SB();
#pragma unroll
        for (int j = 0; j < 8; ++j) {
          const int e = j >> 1, h = (j & 1) * 2;
          z[j] = (f2){__builtin_amdgcn_exp2f(z[j][0]), __builtin_amdgcn_exp2f(z[j][1])};
          c[j] = __builtin_elementwise_fma(m2, (f2){cs[1][e][h], cs[1][e][h + 1]}, (f2){acc[1][e][i][h], acc[1][e][i][h + 1]});
          SB();
        }
#pragma unroll
        for (int j = 0; j < 8; ++j) z[j] = __builtin_elementwise_fma(z[j], i22, i22);
        SB();
#pragma unroll
        for (int j = 0; j < 8; ++j) {
          z[j] = (f2){__builtin_amdgcn_rcpf(z[j][0]), __builtin_amdgcn_rcpf(z[j][1])};
          c[j] = c[j] * a[j];
          SB();
        }
#pragma unroll
        for (int j = 0; j < 8; ++j) c[j] = c[j] * z[j];
        SB();
        u4 o0, o1;
#pragma unroll
        for (int j = 0; j < 4; ++j) { o0[j] = pk2(c[j][0], c[j][1]); o1[j] = pk2(c[4 + j][0], c[4 + j][1]); }
        __builtin_amdgcn_raw_buffer_store_b128(o0, wsr, hso + i * 56 * 1024, 0, 16);
        __builtin_amdgcn_raw_buffer_store_b128(o1, wsr, hso + i * 56 * 1024 + 1024, 0, 16);
      }
      SB();
      ctl_add(C_EPI);
      tag_get();
      __builtin_amdgcn_s_setprio(0);
      STAMP(4);
      WAITV(0);
      if (lane == 0 && __hip_atomic_fetch_add((LAS unsigned*)(size_t)C_HPUB, 1u, __ATOMIC_RELAXED, __HIP_MEMORY_SCOPE_WORKGROUP) == 3)
        flag_set(ws, WS_FH + (pm * 16 + pn) * 128, tag);
    }
    if (!g1) { ctl_wait(C_CVIN, 2); tag_get(); }
    STAMP(5);
    int ln2 = __builtin_amdgcn_mbcnt_hi(-1, __builtin_amdgcn_mbcnt_lo(-1, 0));
    OPAQUE(ln2);
    {
    const int lane = ln2, l15 = lane & 15, g = lane >> 4, qq = l15 >> 2, pp = lane & 3;
    const unsigned dv0 = (lane >> 3) * 1024 + (((lane & 7) ^ (g << 1)) * 16);
    f4 acc[4][4];
#pragma unroll
    for (int t = 0; t < 4; ++t)
#pragma unroll
      for (int i = 0; i < 4; ++i) acc[t][i] = (f4){0.f, 0.f, 0.f, 0.f};
    const unsigned da = gw * 12288 + (4 * g + qq) * 128 + (((2 * g + (qq >> 1)) & 3) << 5) + pp * 8;
    unsigned dlo[4];
#pragma unroll
    for (int t = 0; t < 4; ++t) { dlo[t] = da ^ (t * 32); OPAQUE(dlo[t]); }
    bf8 wf[4][4];
    auto ldw1 = [&](int i, int t) {
      const int s6 = i % 6;
      const unsigned a = dlo[t] + (s6 < 3 ? s6 * 4096 : SLOT + (s6 - 3) * 4096);
      wf[i & 3][t] = ldtr(a, a + 2048);
    };
    ctl_wait(C_WD + (gw >> 1) * 4, 1);
#pragma unroll
    for (int t = 0; t < 16; ++t) ldw1(t >> 2, t & 3);
    WAITL(0);
    wdstage(dv0, gw, 6); wdstage(dv0, gw, 7); wdstage(dv0, gw, 8); wdstage(dv0, gw, 9);
    STAMP(9);
    poll(ws, WS_FH + ((rt >> 1) * 16 + (l15 < 14 ? l15 : 0)) * 128, tag);
    STAMP(6);
    constexpr int HW = 5;
    u4 hf[HW][4];
    unsigned ho[4];
#pragma unroll
    for (int t = 0; t < 4; ++t) ho[t] = WS_H + ((rt * 4 + t) * 56 + gw * 14) * 1024 + lane * 16;
    auto ldh1 = [&](int i, int t) { hf[i % HW][t] = __builtin_amdgcn_raw_buffer_load_b128(wsr, ho[t] + (i & 3) * 1024, (i >> 2) * 4096, 16); };
#pragma unroll
    for (int i = 0; i < HW * 4; ++i) { ldh1(i >> 2, i & 3); SB(); }
#pragma unroll
    for (int i = 0; i < 14; ++i) {
#pragma unroll
      for (int ti = 0; ti < 4; ++ti) {
#pragma unroll
        for (int t = 0; t < 4; ++t) { acc[t][ti] = mfma(wf[i & 3][t], __builtin_bit_cast(bf8, hf[i % HW][ti]), acc[t][ti]); SB(); }
        if (i + HW < 14) { ldh1(i + HW, ti); SB(); }
      }
      if (i == 0) STAMP(19);
      if (i >= 1 && i <= 4) { WAITL(0); wdstage(dv0, gw, i + 9); }
      if (i + 4 < 14) {
        if (i == 6) WAITV(32); else if (i == 7) WAITV(28); else if (i == 8) WAITV(24); else if (i == 9) WAITV(16);
#pragma unroll
        for (int t = 0; t < 4; ++t) ldw1(i + 4, t);
      }
      SB();
    }
    STAMP(7);
    WAITL(0);
#pragma unroll
    for (int ti = 0; ti < 4; ++ti)
#pragma unroll
      for (int t = 0; t < 4; ++t) {
        const int tk = ti * 16 + l15;
        const unsigned pb = (tk < 32 ? 0u : SLOT) + gw * 12288 + (tk & 31) * 256;
        sts128f(pb + (((4 * t + g) ^ (tk & 7)) << 4), acc[t][ti]);
      }
    }
    STAMP(8);
  } else {
    auto bsync = [&](unsigned target) { ctl_add(C_BSYNC); ctl_wait(C_BSYNC, target); };
    unsigned dcr;
    if (w < 6) {
      unsigned tch[4];
      if (g1) {
        stage_half(1, 0); WAITV(12); ctl_add(C_LAND); STAMP(1);
        stage_half(1, 1); stage(2); WAITV(24); ctl_add(C_LAND + 4); STAMP(2);
#pragma unroll 1
        for (int st = 3; st < 8; ++st) {
          ctl_wait(C_CONS + 4 * (st - 3), 4);
          WAITV(0);
          ctl_add(C_LAND + 4 * (st - 1));
          stage(st);
        }
        WAITV(0);
        ctl_add(C_LAND + 28);
      }
      STAMP(3);
      {
        const unsigned o = (r * 448 + (w & 1) * 224 + lane) * 128;
        asm volatile("global_load_dword %0, %4, %6\n\tglobal_load_dword %1, %5, %6\n\tglobal_load_dword %2, %7, %6\n\tglobal_load_dword %3, %8, %6"
                     : "=&v"(tch[0]), "=&v"(tch[1]), "=&v"(tch[2]), "=&v"(tch[3])
                     : "v"(o), "v"(o + 8192), "s"(wd), "v"(o + 16384), "v"(o + (lane < 32 ? 24576 : 0)) : "memory");
      }
      asm volatile("global_load_dword %0, %1, %2" : "=&v"(dcr) : "v"(0u), "s"(sD + b * 4) : "memory");
      const unsigned dv0 = (lane >> 3) * 1024 + (((lane & 7) ^ (g << 1)) * 16);
      if (g1) ctl_wait(C_CONS + 24, 4);
#pragma unroll
      for (int i = 0; i < 6; ++i) wdstage(dv0, (w & 1) * 2 + (i & 1), i >> 1);
      if (g1) ctl_wait(C_CONS + 28, 4);
#pragma unroll
      for (int i = 0; i < 6; ++i) wdstage(dv0, (w & 1) * 2 + (i & 1), 3 + (i >> 1));
      asm volatile("s_waitcnt vmcnt(0)" : "+v"(tch[0]), "+v"(tch[1]), "+v"(tch[2]), "+v"(tch[3]), "+v"(dcr)::"memory");
      ctl_add(C_WD + (w & 1) * 4);
      STAMP(4);
    } else {
      const int sw_ = w & 1, t7 = tid & 127;
      const int cst = 2 * ((r + pnl) & 3) + ((xcd >> 1) & 1);
      const unsigned hoff = (2 * b + sw_) * 4096 + lane * 16;
      u4 wsh[8], hc[4], uc[4]; u2 hp[4];
      const int ptt = b >> 3, pct = b & 7, cp = lane & 31, tg = sw_ * 2 + (lane >> 5);
      unsigned kd[19], vd[16]; u4 shw;
      if (SIDE_DELAY && g1) ctl_wait(C_LAND, 2);
      {
        const char* wb = ((xcd >> 2) ? wu : wg) + (g1 ? pn * 256 + (cst * 64 + sw_ * 32) * 3584 : 0);
        const unsigned o = g * 3584 + l15 * 16;
        asm volatile("global_load_dwordx4 %0, %8, %12\n\tglobal_load_dwordx4 %1, %9, %12\n\tglobal_load_dwordx4 %2, %10, %12\n\tglobal_load_dwordx4 %3, %11, %12\n\t"
                     "global_load_dwordx4 %4, %8, %13\n\tglobal_load_dwordx4 %5, %9, %13\n\tglobal_load_dwordx4 %6, %10, %13\n\tglobal_load_dwordx4 %7, %11, %13"
                     : "=&v"(wsh[0]), "=&v"(wsh[1]), "=&v"(wsh[2]), "=&v"(wsh[3]), "=&v"(wsh[4]), "=&v"(wsh[5]), "=&v"(wsh[6]), "=&v"(wsh[7])
                     : "v"(o), "v"(o + 4 * 3584), "v"(o + 8 * 3584), "v"(o + 12 * 3584), "s"(wb), "s"(wb + 16 * 3584) : "memory");
        rows_load();
        const unsigned vo = tg * 16384 + cp * 4 + 4096, vo8 = vo + 8192;
        const unsigned vh = (ptt == 0 && tg == 0) ? vo - 1024 : vo - 4096;
        const char* kb = kk + ptt * 65536 + pct * 128;
        const char* vb = vv + ptt * 65536 + pct * 128;
#define LD8(d, o, vreg, sreg) asm volatile( \
          "global_load_dword %0, %8, %9 offset:-4096\n\tglobal_load_dword %1, %8, %9 offset:-3072\n\tglobal_load_dword %2, %8, %9 offset:-2048\n\t" \
          "global_load_dword %3, %8, %9 offset:-1024\n\tglobal_load_dword %4, %8, %9\n\tglobal_load_dword %5, %8, %9 offset:1024\n\t" \
          "global_load_dword %6, %8, %9 offset:2048\n\tglobal_load_dword %7, %8, %9 offset:3072" \
          : "=&v"(d[o]), "=&v"(d[o + 1]), "=&v"(d[o + 2]), "=&v"(d[o + 3]), "=&v"(d[o + 4]), "=&v"(d[o + 5]), "=&v"(d[o + 6]), "=&v"(d[o + 7]) \
          : "v"(vreg), "s"(sreg) : "memory")
        asm volatile("global_load_dword %0, %4, %5 offset:-3072\n\tglobal_load_dword %1, %4, %5 offset:-2048\n\tglobal_load_dword %2, %4, %5 offset:-1024\n\t"
                     "global_load_dwordx4 %3, %6, %7"
                     : "=&v"(kd[0]), "=&v"(kd[1]), "=&v"(kd[2]), "=&v"(shw) : "v"(vh), "s"(kb), "v"(cp * 16), "s"(shk + pct * 512) : "memory");
        LD8(kd, 3, vo, kb); LD8(vd, 0, vo, vb); LD8(kd, 11, vo8, kb); LD8(vd, 8, vo8, vb);
      }
      asm volatile("global_load_dwordx4 %0, %9, %11\n\tglobal_load_dwordx4 %1, %9, %11 offset:1024\n\tglobal_load_dwordx4 %2, %9, %11 offset:2048\n\t"
                   "global_load_dwordx4 %3, %9, %11 offset:3072\n\tglobal_load_dwordx2 %4, %10, %11\n\tglobal_load_dwordx2 %5, %9, %11 offset:1016\n\t"
                   "global_load_dwordx2 %6, %9, %11 offset:2040\n\tglobal_load_dwordx2 %7, %9, %11 offset:3064\n\tglobal_load_dword %8, %12, %13"
                   : "=&v"(hc[0]), "=&v"(hc[1]), "=&v"(hc[2]), "=&v"(hc[3]), "=&v"(hp[0]), "=&v"(hp[1]), "=&v"(hp[2]), "=&v"(hp[3]), "=&v"(dcr)
                   : "v"(hoff), "v"(lane ? hoff - 8 : hoff), "s"(ssk), "v"(0u), "s"(sD + b * 4) : "memory");
      tag_fetch();
      asm volatile("s_waitcnt vmcnt(45)" : "+v"(wsh[0]), "+v"(wsh[1]), "+v"(wsh[2]), "+v"(wsh[3]), "+v"(wsh[4]), "+v"(wsh[5]), "+v"(wsh[6]), "+v"(wsh[7])::"memory");
      STAMP(17);
      if (g1) {
        float cacc[8];
#pragma unroll
        for (int j = 0; j < 4; ++j) {
          cacc[2 * j] = fadd(fadd(fadd(bflo(wsh[0][j]), bflo(wsh[1][j])), fadd(bflo(wsh[2][j]), bflo(wsh[3][j]))),
                             fadd(fadd(bflo(wsh[4][j]), bflo(wsh[5][j])), fadd(bflo(wsh[6][j]), bflo(wsh[7][j]))));
          cacc[2 * j + 1] = fadd(fadd(fadd(bfhi(wsh[0][j]), bfhi(wsh[1][j])), fadd(bfhi(wsh[2][j]), bfhi(wsh[3][j]))),
                                 fadd(fadd(bfhi(wsh[4][j]), bfhi(wsh[5][j])), fadd(bfhi(wsh[6][j]), bfhi(wsh[7][j]))));
        }
        float c4[4], c2[2];
#pragma unroll
        for (int j = 0; j < 4; ++j) {
          const auto s2 = __builtin_amdgcn_permlane16_swap(__float_as_uint(cacc[j]), __float_as_uint(cacc[4 + j]), false, false);
          c4[j] = fadd(__uint_as_float(s2[0]), __uint_as_float(s2[1]));
        }
#pragma unroll
        for (int j = 0; j < 2; ++j) {
          const auto s2 = __builtin_amdgcn_permlane32_swap(__float_as_uint(c4[j]), __float_as_uint(c4[2 + j]), false, false);
          c2[j] = fadd(__uint_as_float(s2[0]), __uint_as_float(s2[1]));
        }
        __builtin_amdgcn_raw_buffer_store_b64((u2){__float_as_uint(c2[0]), __float_as_uint(c2[1])}, wsr,
                                              WS_COL + ((pn * 16 + cst * 2 + sw_) * 256 + (xcd >> 2) * 128 + l15 * 8 + (g & 1) * 4 + (g >> 1) * 2) * 4, 0, 16);
      }
      rows_stat();
      STAMP(18);
      asm volatile("s_waitcnt vmcnt(0)" : "+v"(kd[0]), "+v"(kd[1]), "+v"(kd[2]), "+v"(kd[3]), "+v"(kd[4]), "+v"(kd[5]), "+v"(kd[6]), "+v"(kd[7]), "+v"(kd[8]),
                   "+v"(kd[9]), "+v"(kd[10]), "+v"(kd[11]), "+v"(kd[12]), "+v"(kd[13]), "+v"(kd[14]), "+v"(kd[15]), "+v"(kd[16]), "+v"(kd[17]), "+v"(kd[18]),
                   "+v"(shw), "+v"(dcr)::"memory");
      asm volatile("" : "+v"(vd[0]), "+v"(vd[1]), "+v"(vd[2]), "+v"(vd[3]), "+v"(vd[4]), "+v"(vd[5]), "+v"(vd[6]), "+v"(vd[7]), "+v"(vd[8]), "+v"(vd[9]),
                   "+v"(vd[10]), "+v"(vd[11]), "+v"(vd[12]), "+v"(vd[13]), "+v"(vd[14]), "+v"(vd[15]),
                   "+v"(hc[0]), "+v"(hc[1]), "+v"(hc[2]), "+v"(hc[3]), "+v"(hp[0]), "+v"(hp[1]), "+v"(hp[2]), "+v"(hp[3])::"memory");
      ctl_add(C_WSYNC); ctl_wait(C_WSYNC, 2);
      if (tid == 384) flag_set(ws, WS_FS + rgrp * 128, tag);
      STAMP(20);
      {
        if (ptt == 0 && tg == 0) { kd[0] = 0; kd[1] = 0; kd[2] = 0; }
#pragma unroll
        for (int c = 0; c < 2; ++c) {
          const float w0 = bflo(shw[2 * c]), w1 = bfhi(shw[2 * c]), w2 = bflo(shw[2 * c + 1]), w3 = bfhi(shw[2 * c + 1]);
          float kf[19], u[16];
#pragma unroll
          for (int i = 0; i < 19; ++i) kf[i] = c ? bfhi(kd[i]) : bflo(kd[i]);
          SB();
#pragma unroll
          for (int i = 0; i < 16; ++i) u[i] = fmul(kf[i], w3);
          SB();
#pragma unroll
          for (int i = 0; i < 16; ++i) u[i] = ffma(kf[i + 1], w2, u[i]);
          SB();
#pragma unroll
          for (int i = 0; i < 16; ++i) u[i] = ffma(kf[i + 2], w1, u[i]);
          SB();
#pragma unroll
          for (int i = 0; i < 16; ++i) u[i] = ffma(kf[i + 3], w0, u[i]);
          SB();
#pragma unroll
          for (int i = 0; i < 16; ++i) u[i] = fmul(u[i], c ? bfhi(vd[i]) : bflo(vd[i]));
          SB();
          const unsigned ad = L_MISC + cp * 256 + c * 128;
          sts128(ad + (((6 - 2 * tg) ^ (cp & 7)) << 4), (u4){pk2(u[15], u[14]), pk2(u[13], u[12]), pk2(u[11], u[10]), pk2(u[9], u[8])});
          sts128(ad + (((7 - 2 * tg) ^ (cp & 7)) << 4), (u4){pk2(u[7], u[6]), pk2(u[5], u[4]), pk2(u[3], u[2]), pk2(u[1], u[0])});
        }
        ctl_add(C_WSYNC); ctl_wait(C_WSYNC, 4);
#pragma unroll
        for (int z = 0; z < 4; ++z) {
          const int id = t7 + 128 * z, c2 = id >> 3, ch = id & 7;
          const u4 t = lds128u(L_MISC + c2 * 128 + ((ch ^ ((c2 >> 1) & 7)) << 4));
          __builtin_amdgcn_raw_buffer_store_b128(t, wsr, WS_KVT + (pct * 64 + c2) * 4096 + 3968 - ptt * 128 + ch * 16, 0, 16);
        }
        STAMP(21);
        WAITV(0);
        ctl_add(C_WSYNC); ctl_wait(C_WSYNC, 6);
        if (tid == 384) flag_set(ws, WS_FK + (pct * 32 + ptt) * 128, tag);
        STAMP(19);
      }
      u4 c1[4];
      unsigned a0[4];
      if (lane == 0) { hp[0][0] = 0; hp[0][1] = 0; }
#pragma unroll
      for (int z = 0; z < 4; ++z) {
        a0[z] = __builtin_amdgcn_alignbit(hp[z][1], hp[z][0], 16);
        c1[z] = (u4){__builtin_amdgcn_alignbit(hc[z][0], hp[z][1], 16), __builtin_amdgcn_alignbit(hc[z][1], hc[z][0], 16),
                     __builtin_amdgcn_alignbit(hc[z][2], hc[z][1], 16), __builtin_amdgcn_alignbit(hc[z][3], hc[z][2], 16)};
      }
      if (g1) {
        const int j = l15;
        const int bc = (j >> 2) * 64 + (xcd & 1) * 32 + (j & 3) + 4 * pnl;
        poll(ws, WS_FS + (lane & 16 ? bc : pm * 16 + j) * 128, tag);
        STAMP(13);
        u2 rsv, csv[16];
        rsv = __builtin_amdgcn_raw_buffer_load_b64(wsr, WS_ROW + pm * 1024 + t7 * 8, 0, 16);
#pragma unroll
        for (int i = 0; i < 16; ++i) csv[i] = __builtin_amdgcn_raw_buffer_load_b64(wsr, WS_COL + pn * 16384 + t7 * 8 + (i & 3) * 1024, (i >> 2) * 4096, 16);
        *(LAS u2*)(size_t)(L_RS + t7 * 8) = rsv;
        float s0 = 0.f, s1 = 0.f;
#pragma unroll
        for (int i = 0; i < 16; ++i) { s0 = fadd(s0, __uint_as_float(csv[i][0])); s1 = fadd(s1, __uint_as_float(csv[i][1])); }
        *(LAS u2*)(size_t)(L_CS + t7 * 8) = (u2){__float_as_uint(s0), __float_as_uint(s1)};
        ctl_add(C_LAND + 32);
        STAMP(16);
      }
      poll(ws, WS_FK + ((b >> 5) * 32 + (lane & 31)) * 128, tag);
      STAMP(14);
#pragma unroll
      for (int z = 0; z < 4; ++z) uc[z] = __builtin_amdgcn_raw_buffer_load_b128(wsr, WS_KVT + hoff + z * 1024, 0, 16);
      if (b == 255 && w == 7) {
        poll(ws, WS_FS + lane * 128, tag); poll(ws, WS_FS + (64 + lane) * 128, tag);
        poll(ws, WS_FS + (128 + lane) * 128, tag); poll(ws, WS_FS + (192 + lane) * 128, tag);
        if (lane == 0) flag_set(ws, WS_EPOCH, tag);
      }
      if (g1) ctl_wait(C_CONS + 20, 4);
      const unsigned base = L_CV + sw_ * CV_CH;
#pragma unroll
      for (int z = 0; z < 4; ++z) {
        const unsigned p = base + 32 + lane * 16 + z * 1024;
        sts128(p, hc[z]);
        sts128(p + CV_COPY, c1[z]);
        sts128(p + 2 * CV_COPY, (u4){hp[z][1], hc[z][0], hc[z][1], hc[z][2]});
        sts128(p + 3 * CV_COPY, (u4){a0[z], c1[z][0], c1[z][1], c1[z][2]});
      }
      if (lane < 8) sts128(base + (lane >> 1) * CV_COPY + (lane & 1) * 16, (u4){0, 0, 0, 0});
      if (lane >= 32) sts128(base + CV_UR + 4096 + (lane - 32) * 16, (u4){0, 0, 0, 0});
#pragma unroll
      for (int z = 0; z < 4; ++z) sts128(base + CV_UR + lane * 16 + z * 1024, uc[z]);
      ctl_add(C_CVIN);
      STAMP(15);
    }
    u4 xq[2];
#pragma unroll
    for (int z = 0; z < 2; ++z) {
      const int id = t8 + 256 * z;
      xq[z] = *(const GAS u4*)(q + (rt * 64 + (id >> 3)) * 1024 + ct2 * 128 + (id & 7) * 16);
    }
    ctl_wait(C_CVIN, 2);
    tag_get();
    const int ch = gw >> 1, half = gw & 1;
    const unsigned base = L_CV + ch * CV_CH;
    const unsigned o = 8 * g + l15 + 1, kap = (0u - o) & 3;
    unsigned aa = base + kap * CV_COPY + 2 * (o + kap) + half * 256;
    unsigned ba = base + CV_UR + 2 * (2032 - 16 * l15 + 8 * g) - 3584 + half * 256;
    OPAQUE(aa); OPAQUE(ba);
    f4 y[8];
#pragma unroll
    for (int n = 0; n < 8; ++n) y[n] = (f4){0.f, 0.f, 0.f, 0.f};
    bf8 A[2][8], B[2][8];
    auto ldab = [&](int rho, int i) {
      const u2 lo = *(const volatile LAS u2*)(size_t)(aa + rho * 64 + i * 512), hi = *(const volatile LAS u2*)(size_t)(aa + rho * 64 + i * 512 + 8);
      A[rho & 1][i] = __builtin_bit_cast(bf8, (u4){lo[0], lo[1], hi[0], hi[1]});
      B[rho & 1][i] = lds128(ba + rho * 64 + (7 - i) * 512);
    };
#pragma unroll
    for (int i = 0; i < 8; ++i) ldab(0, i);
    SB();
    if (g1) ctl_wait(C_EPI, 4);
    STAMP(5);
    __builtin_amdgcn_s_setprio(1);
#pragma unroll
    for (int rho = 0; rho < 4; ++rho) {
      int cnt = 0;
#pragma unroll
      for (int lv = 0; lv < 8; ++lv)
#pragma unroll
        for (int qi = 0; qi <= lv; ++qi)
#pragma unroll
          for (int p = 0; p <= lv; ++p)
            if ((qi == lv || p == lv) && qi + p < 8) {
              y[qi + p] = mfma(A[rho & 1][qi], B[rho & 1][p], y[qi + p]);
              if (rho < 3 && (cnt & 3) == 0 && (cnt >> 2) < 8) ldab(rho + 1, cnt >> 2);
              ++cnt;
              SB();
            }
    }
    STAMP(6);
    const unsigned ex0 = gw < 3 ? L_MISC + gw * 4096 : L_CV + 2 * CV_CH, ex1 = (gw ^ 1) < 3 ? L_MISC + (gw ^ 1) * 4096 : L_CV + 2 * CV_CH;
#pragma unroll
    for (int n = 0; n < 4; ++n) {
      if (half) { const f4 t = y[n]; y[n] = y[4 + n]; y[4 + n] = t; }
      sts128f(ex0 + n * 1024 + lane * 16, y[4 + n]);
    }
    bsync(4);
    const float dc = ch ? bfhi(dcr) : bflo(dcr);
    u2 pk[4];
#pragma unroll
    for (int n = 0; n < 4; ++n) {
      const f4 t = lds128f(ex1 + n * 1024 + lane * 16);
      const int t0 = 256 * (4 * half + n) + 16 * l15 + 4 * g;
      const u2 uu = *(const LAS u2*)(size_t)(base + CV_UR + 2 * (2044 - t0));
      pk[n] = (u2){pk2(ffma(bfhi(uu[1]), dc, fadd(t[0], y[n][0])), ffma(bflo(uu[1]), dc, fadd(t[1], y[n][1]))),
                   pk2(ffma(bfhi(uu[0]), dc, fadd(t[2], y[n][2])), ffma(bflo(uu[0]), dc, fadd(t[3], y[n][3])))};
    }
#pragma unroll
    for (int n = 0; n < 4; n += 2) {
      const auto s0 = __builtin_amdgcn_permlane16_swap(pk[n][0], pk[n + 1][0], false, false);
      const auto s1 = __builtin_amdgcn_permlane16_swap(pk[n][1], pk[n + 1][1], false, false);
      const u4 o4 = {s0[0], s1[0], s0[1], s1[1]};
      __builtin_amdgcn_raw_buffer_store_b128(o4, wsr, WS_YT + (2 * b + ch) * 4096 + 2 * (256 * (4 * half + n + (g & 1)) + 16 * l15 + 8 * (g >> 1)), 0, 16);
    }
    STAMP(22);
    WAITV(0);
    bsync(8);
    if (tid == 256) flag_set(ws, WS_FY + b * 128, tag);
    STAMP(7);
    if (gw == 3) {
      poll(ws, lane < 32 ? WS_FY + (ct2 * 32 + lane) * 128 : WS_FS + (rt * 8 + (lane & 7)) * 128, tag);
      ctl_add(C_GO);
    } else ctl_wait(C_GO, 1);
    STAMP(8);
    u4 yq[2]; u2 st2[2];
#pragma unroll
    for (int z = 0; z < 2; ++z) {
      const int id = t8 + 256 * z;
      yq[z] = __builtin_amdgcn_raw_buffer_load_b128(wsr, WS_YT + (ct2 * 64 + (id >> 3)) * 4096 + rt * 128 + (id & 7) * 16, 0, 16);
      st2[z] = __builtin_amdgcn_raw_buffer_load_b64(wsr, WS_ROW + (rt * 64 + (id >> 3)) * 8, 0, 16);
    }
#pragma unroll
    for (int z = 0; z < 2; ++z) {
      const int id = t8 + 256 * z, c = id >> 3;
      sts128(L_YI + c * 128 + (((id & 7) ^ (c >> 3)) << 4), yq[z]);
    }
    STAMP(23);
    bsync(12);
#pragma unroll
    for (int z = 0; z < 2; ++z) {
      const int id = t8 + 256 * z, tk = id >> 3, c8 = id & 7;
      const float m = __uint_as_float(st2[z][0]), rr = __uint_as_float(st2[z][1]);
      const unsigned ya = L_YI + c8 * 1024 + (((tk >> 3) ^ c8) << 4) + (tk & 7) * 2;
      u4 so;
#pragma unroll
      for (int j = 0; j < 4; ++j) {
        const float y0 = __uint_as_float(((unsigned)*(const LAS unsigned short*)(size_t)(ya + (2 * j) * 128)) << 16);
        const float y1 = __uint_as_float(((unsigned)*(const LAS unsigned short*)(size_t)(ya + (2 * j + 1) * 128)) << 16);
        const unsigned d = pk2(fsub(bflo(xq[z][j]), m), fsub(bfhi(xq[z][j]), m));
        const unsigned xn = pk2(fmul(bflo(d), rr), fmul(bfhi(d), rr));
        so[j] = pk2(fmul(bflo(xn), y0), fmul(bfhi(xn), y1));
      }
      sts128(L_SI + id * 16, so);
      sts128(L_XI + id * 16, xq[z]);
    }
    __builtin_amdgcn_s_setprio(0);
    STAMP(9);
  }
  BAR();
  STAMP(10);
  {
    int ln3 = __builtin_amdgcn_mbcnt_hi(-1, __builtin_amdgcn_mbcnt_lo(-1, 0));
    OPAQUE(ln3);
    const int tid = w * 64 + ln3;
    const int tk = tid >> 3, c8 = tid & 7;
    const unsigned pb = (tk < 32 ? 0u : SLOT) + (tk & 31) * 256;
    f4 o0 = {0.f, 0.f, 0.f, 0.f}, o1 = {0.f, 0.f, 0.f, 0.f};
#pragma unroll
    for (int k4 = 0; k4 < 4; ++k4) {
      o0 += lds128f(pb + k4 * 12288 + (((2 * c8) ^ (tk & 7)) << 4));
      o1 += lds128f(pb + k4 * 12288 + (((2 * c8 + 1) ^ (tk & 7)) << 4));
    }
    const u4 sv = lds128u(L_SI + tid * 16), xv = lds128u(L_XI + tid * 16);
    const float ov[8] = {o0[0], o0[1], o0[2], o0[3], o1[0], o1[1], o1[2], o1[3]};
    u4 res;
#pragma unroll
    for (int j = 0; j < 4; ++j) {
      const unsigned ob = pk2(ov[2 * j], ov[2 * j + 1]);
      const float a0 = bflo(ob), a1 = bfhi(ob);
      const unsigned gb2 = pk2(a0 * __builtin_amdgcn_rcpf(1.0f + __builtin_amdgcn_exp2f(-1.4426950408889634f * a0)),
                               a1 * __builtin_amdgcn_rcpf(1.0f + __builtin_amdgcn_exp2f(-1.4426950408889634f * a1)));
      const unsigned sg = pk2(bflo(sv[j]) * bflo(gb2), bfhi(sv[j]) * bfhi(gb2));
      res[j] = pk2(bflo(xv[j]) + bflo(sg), bfhi(xv[j]) + bfhi(sg));
    }
    *(GAS u4*)(out + (rt * 64 + tk) * 1024 + ct2 * 128 + c8 * 16) = res;
  }
  STAMP(11);
}
}

extern "C" void kernel_launch(void* const* d_in, const int* in_sizes, int n_in, void* d_out, int out_size, void* d_ws, size_t ws_size,
                              hipStream_t stream) {
  (void)in_sizes; (void)n_in; (void)out_size; (void)ws_size;
  (void)hipFuncSetAttribute((const void*)h3_geglu_fused, hipFuncAttributeMaxDynamicSharedMemorySize, LDS_BYTES);
  h3_geglu_fused<<<256, 512, LDS_BYTES, stream>>>((const char*)d_in[0], (const char*)d_in[1], (const char*)d_in[2], (const char*)d_in[3],
                                                  (const char*)d_in[4], (const char*)d_in[5], (const char*)d_in[7], (const char*)d_in[8],
                                                  (const char*)d_in[9], (char*)d_out, (char*)d_ws);
}
```

```cpp
#define PROF
#define PROBE_B 9
#define PROBE_W 0
#define PROBE_I 2
#include <hip/hip_runtime.h>
#include <stdint.h>

#define LAS __attribute__((address_space(3)))
#define GAS __attribute__((address_space(1)))
#define DI __device__ __forceinline__
typedef __bf16 bf8 __attribute__((ext_vector_type(8)));
typedef __bf16 bf2 __attribute__((ext_vector_type(2)));
typedef short s4 __attribute__((ext_vector_type(4)));
typedef float f4 __attribute__((ext_vector_type(4)));
typedef float f2 __attribute__((ext_vector_type(2)));
typedef unsigned u4 __attribute__((ext_vector_type(4)));
typedef unsigned u2 __attribute__((ext_vector_type(2)));
typedef unsigned long long u64;

namespace {
constexpr unsigned WS_EPOCH = 0;
constexpr unsigned WS_FK = 4096;
constexpr unsigned WS_FS = WS_FK + 256 * 128;
constexpr unsigned WS_FH = WS_FS + 256 * 128;
constexpr unsigned WS_FY = WS_FH + 256 * 128;
constexpr unsigned WS_ROW = WS_FY + 256 * 128;
constexpr unsigned WS_COL = WS_ROW + 2048 * 8;
constexpr unsigned WS_KVT = 1u << 20;
constexpr unsigned WS_YT = 3u << 20;
constexpr unsigned WS_H = 5u << 20;
constexpr unsigned WS_PROF = 32u << 20;
constexpr unsigned SLOT = 49152;
constexpr unsigned L_MISC = 3 * SLOT;
constexpr unsigned L_RS = L_MISC + 12288;
constexpr unsigned L_CS = L_RS + 1024;
constexpr unsigned L_CTL = L_CS + 1024;
constexpr unsigned C_LAND = L_CTL, C_CONS = L_CTL + 40, C_BSYNC = L_CTL + 72, C_WSYNC = L_CTL + 76, C_CVIN = L_CTL + 80,
                   C_EPI = L_CTL + 84, C_HPUB = L_CTL + 88, C_GO = L_CTL + 92, C_WD = L_CTL + 96, L_TAG = L_CTL + 128;
constexpr unsigned L_CV = 2 * SLOT;
constexpr unsigned CV_COPY = 4160, CV_UR = 4 * CV_COPY, CV_CH = CV_UR + 4608;
constexpr unsigned L_YI = L_CV, L_SI = L_CV + 8192, L_XI = L_CV + 16384;
constexpr unsigned LDS_BYTES = 163840;
constexpr int SPIN_CAP = 1 << 18;

DI float bflo(unsigned u) { return __uint_as_float(u << 16); }
DI float bfhi(unsigned u) { return __uint_as_float(u & 0xffff0000u); }
DI unsigned pk2(float a, float b) { f2 v = {a, b}; return __builtin_bit_cast(unsigned, __builtin_convertvector(v, bf2)); }
DI float dot2(unsigned a, bf2 b, float c) { return __builtin_amdgcn_fdot2_f32_bf16(__builtin_bit_cast(bf2, a), b, c, false); }
DI float rbf(float a) { return bflo(pk2(a, a)); }
DI f4 mfma(bf8 a, bf8 b, f4 c) { return __builtin_amdgcn_mfma_f32_16x16x32_bf16(a, b, c, 0, 0, 0); }
DI bf8 lds128(unsigned a) { return *(const LAS bf8*)(size_t)a; }
DI u4 lds128u(unsigned a) { return *(const LAS u4*)(size_t)a; }
DI f4 lds128f(unsigned a) { return *(const LAS f4*)(size_t)a; }
DI void sts128(unsigned a, u4 v) { *(LAS u4*)(size_t)a = v; }
DI void sts128f(unsigned a, f4 v) { *(LAS f4*)(size_t)a = v; }
DI bf8 ldtr(unsigned a0, unsigned a1) {
  s4 lo = __builtin_amdgcn_ds_read_tr16_b64_v4i16((LAS s4*)(size_t)a0);
  s4 hi = __builtin_amdgcn_ds_read_tr16_b64_v4i16((LAS s4*)(size_t)a1);
  return __builtin_bit_cast(bf8, __builtin_shufflevector(lo, hi, 0, 1, 2, 3, 4, 5, 6, 7));
}
#define WAITV(n) asm volatile("s_waitcnt vmcnt(%0)" ::"i"(n) : "memory")
#define WAITL(n) asm volatile("s_waitcnt lgkmcnt(%0)" ::"i"(n) : "memory")
#define SB() __builtin_amdgcn_sched_barrier(0)
#define OPAQUE(x) asm volatile("" : "+v"(x))
#define BAR() do { asm volatile("s_waitcnt lgkmcnt(0)" ::: "memory"); __builtin_amdgcn_s_barrier(); asm volatile("" ::: "memory"); } while (0)

DI void glds2(unsigned v0, unsigned v1, const char* sb, unsigned l) {
  asm volatile("s_mov_b32 m0, %3\n\ts_nop 0\n\tglobal_load_lds_dwordx4 %0, %2\n\tglobal_load_lds_dwordx4 %1, %2 offset:1024"
               ::"v"(v0), "v"(v1), "s"(sb), "s"(l) : "memory");
}
DI void glds4(unsigned v0, unsigned v1, unsigned v2, unsigned v3, const char* sb, unsigned l) {
  asm volatile("s_mov_b32 m0, %5\n\ts_nop 0\n\tglobal_load_lds_dwordx4 %0, %4\n\tglobal_load_lds_dwordx4 %1, %4 offset:1024\n\t"
               "global_load_lds_dwordx4 %2, %4 offset:2048\n\tglobal_load_lds_dwordx4 %3, %4 offset:3072"
               ::"v"(v0), "v"(v1), "v"(v2), "v"(v3), "s"(sb), "s"(l) : "memory");
}
DI void flag_set(char* ws, unsigned off, u64 tag) {
  __hip_atomic_store((GAS u64*)(ws + off), tag, __ATOMIC_RELAXED, __HIP_MEMORY_SCOPE_AGENT);
}
DI void poll(const char* ws, unsigned off, u64 tag) {
#pragma unroll 1
  for (int it = 0; it < SPIN_CAP; ++it) {
    u64 x = __hip_atomic_load((GAS u64*)(ws + off), __ATOMIC_RELAXED, __HIP_MEMORY_SCOPE_AGENT);
    if (__all(x == tag)) break;
    __builtin_amdgcn_s_sleep(4);
  }
  asm volatile("" ::: "memory");
}
DI float fadd(float a, float b) { float r; asm("v_add_f32 %0, %1, %2" : "=v"(r) : "v"(a), "v"(b)); return r; }
DI float fsub(float a, float b) { float r; asm("v_sub_f32 %0, %1, %2" : "=v"(r) : "v"(a), "v"(b)); return r; }
DI float fmul(float a, float b) { float r; asm("v_mul_f32 %0, %1, %2" : "=v"(r) : "v"(a), "v"(b)); return r; }
DI float ffma(float a, float b, float c) { float r; asm("v_fma_f32 %0, %1, %2, %3" : "=v"(r) : "v"(a), "v"(b), "v"(c)); return r; }
template <int CTRL> DI float dpp_add(float v) {
  return v + __int_as_float(__builtin_amdgcn_update_dpp(0, __float_as_int(v), CTRL, 0xf, 0xf, true));
}
DI float wave_sum(float v) {
  v = dpp_add<0xB1>(v); v = dpp_add<0x4E>(v); v = dpp_add<0x141>(v); v = dpp_add<0x140>(v);
  const int i = __float_as_int(v);
  return (__int_as_float(__builtin_amdgcn_readlane(i, 0)) + __int_as_float(__builtin_amdgcn_readlane(i, 16))) +
         (__int_as_float(__builtin_amdgcn_readlane(i, 32)) + __int_as_float(__builtin_amdgcn_readlane(i, 48)));
}
DI void ctl_add(unsigned addr) {
  asm volatile("s_waitcnt lgkmcnt(0)\n\ts_mov_b64 exec, 1\n\tds_add_u32 %0, %1\n\ts_mov_b64 exec, -1" ::"v"(addr), "v"(1u) : "memory");
}
DI unsigned ctl_get(unsigned addr) {
  unsigned s, t;
  asm volatile("ds_read_b32 %1, %2\n\ts_waitcnt lgkmcnt(0)\n\tv_readfirstlane_b32 %0, %1\n\ts_nop 1" : "=s"(s), "=&v"(t) : "v"(addr) : "memory");
  return s;
}
DI void ctl_wait(unsigned addr, unsigned target) {
#pragma unroll 1
  for (int it = 0; it < SPIN_CAP; ++it) {
    if (ctl_get(addr) >= target) break;
    __builtin_amdgcn_s_sleep(1);
  }
}
#define CSTAMP(i) do { u64 t_; asm volatile("s_memtime %0\n\ts_waitcnt lgkmcnt(0)" : "=s"(t_)::"memory"); tt[i] = (unsigned)t_; } while (0)
#define CNOW(v) do { u64 t_; asm volatile("s_memtime %0\n\ts_waitcnt lgkmcnt(0)" : "=s"(t_)::"memory"); v = (unsigned)t_; } while (0)
#define STAMP(i) do { u64 t_; asm volatile("s_memrealtime %0\n\ts_waitcnt lgkmcnt(0)" : "=s"(t_)::"memory"); tt[i] = (unsigned)t_; } while (0)

struct Frag { bf8 x[4]; bf8 w[2][4]; };
#define SIDE_DELAY 0

__global__ __launch_bounds__(512, 2) void h3_geglu_fused(const char* q, const char* kk, const char* vv, const char* shk, const char* ssk,
                                                         const char* sD, const char* wg, const char* wu, const char* wd, char* out, char* ws) {
  extern __shared__ __attribute__((aligned(16))) char smem[];
  const int tid = threadIdx.x, lane = tid & 63, t8 = tid & 255;
  const int w = __builtin_amdgcn_readfirstlane(tid >> 6), gw = w & 3;
  const int b = blockIdx.x, xcd = b & 7, r = b >> 3;
  const bool g1 = r < 28;
  const int l15 = lane & 15, g = lane >> 4;
  const __amdgpu_buffer_rsrc_t wsr = __builtin_amdgcn_make_buffer_rsrc(ws, 0, 16 << 20, 0x00020000);
  const int pnl = r >> 2;
  const int pm = (xcd >> 1) * 4 + (r & 3), pn = (xcd & 1) * 7 + pnl;
  const int rt = xcd * 4 + (r & 3), ct2 = r >> 2;
  const int rgrp = (xcd >> 1) * 64 + (xcd & 1) * 32 + r;
  unsigned tt[24] = {0};
  STAMP(0);
  auto wdstage = [&](unsigned dv0, int kq, int i) {
    const int s6 = i % 6;
    glds4(dv0, dv0 + 7168, dv0 + 2 * 7168, dv0 + 3 * 7168, wd + ct2 * 128 + (kq * 14 + i) * 32768, (s6 < 3 ? s6 * 4096 : SLOT + (s6 - 3) * 4096) + kq * 12288);
  };
  unsigned xv[4], wv[4];
  const char* xb = q + pm * 131072 + (w & 1) * 65536;
  const char* gb = wg + pn * 256 + (w & 1) * (32 * 3584);
  const char* ub = wu + pn * 256 + (w & 1) * (32 * 3584);
  auto stage_half = [&](int st, int hf) {
    const unsigned l = (st % 3) * SLOT + (w & 1) * 8192 + hf * 4096;
    glds4(xv[0], xv[1], xv[2], xv[3], xb + st * 128 + hf * 32768, l);
    glds4(wv[0], wv[1], wv[2], wv[3], gb + st * (64 * 3584) + hf * (16 * 3584), l + 16384);
    glds4(wv[0], wv[1], wv[2], wv[3], ub + st * (64 * 3584) + hf * (16 * 3584), l + 32768);
  };
  auto stage = [&](int st) { stage_half(st, 0); stage_half(st, 1); };
  if ((w >> 1) == 2) {
#pragma unroll
    for (int p = 0; p < 4; ++p) {
      xv[p] = (p * 8 + (lane >> 3)) * 1024 + (((lane & 7) ^ (((p & 1) << 2) | g)) << 4) - p * 1024;
      wv[p] = (p * 4 + g) * 3584 + ((l15 ^ ((g | ((p >> 1) << 2)) << 1)) << 4) - p * 1024;
    }
    if (g1) stage(0);
  }
  if (tid < 32) *(LAS unsigned*)(size_t)(L_CTL + tid * 4) = 0;
  u64 tag;
  auto tag_fetch = [&]() {
    asm volatile("s_load_dwordx2 %0, %1, 0x0\n\ts_waitcnt lgkmcnt(0)" : "=s"(tag) : "s"(ws) : "memory");
    tag += 1;
    *(LAS u64*)(size_t)L_TAG = tag;
  };
  auto tag_get = [&]() {
    const u2 t = *(const volatile LAS u2*)(size_t)L_TAG;
    tag = ((u64)(unsigned)__builtin_amdgcn_readfirstlane(t[1]) << 32) | (unsigned)__builtin_amdgcn_readfirstlane(t[0]);
  };
  u4 xr[4];
  auto rows_load = [&]() {
    asm volatile("global_load_dwordx4 %0, %4, %5\n\tglobal_load_dwordx4 %1, %4, %5 offset:1024\n\tglobal_load_dwordx4 %2, %4, %5 offset:2048\n\t"
                 "global_load_dwordx4 %3, %4, %5 offset:3072"
                 : "=&v"(xr[0]), "=&v"(xr[1]), "=&v"(xr[2]), "=&v"(xr[3]) : "v"(lane * 16), "s"(q + (rgrp * 8 + (w & 1) * 4) * 1024) : "memory");
  };
  auto rows_stat = [&]() {
    asm volatile("" : "+v"(xr[0]), "+v"(xr[1]), "+v"(xr[2]), "+v"(xr[3])::"memory");
    float e[4][8], mu[4], va[4];
#pragma unroll
    for (int i = 0; i < 4; ++i) {
#pragma unroll
      for (int j = 0; j < 4; ++j) { e[i][2 * j] = bflo(xr[i][j]); e[i][2 * j + 1] = bfhi(xr[i][j]); }
      mu[i] = fadd(fadd(fadd(e[i][0], e[i][1]), fadd(e[i][2], e[i][3])), fadd(fadd(e[i][4], e[i][5]), fadd(e[i][6], e[i][7])));
    }
    auto fold4 = [&](const float (&v)[4]) {
      const auto s0 = __builtin_amdgcn_permlane16_swap(__float_as_uint(v[0]), __float_as_uint(v[1]), false, false);
      const auto s1 = __builtin_amdgcn_permlane16_swap(__float_as_uint(v[2]), __float_as_uint(v[3]), false, false);
      const float p0 = fadd(__uint_as_float(s0[0]), __uint_as_float(s0[1])), p1 = fadd(__uint_as_float(s1[0]), __uint_as_float(s1[1]));
      const auto s2 = __builtin_amdgcn_permlane32_swap(__float_as_uint(p0), __float_as_uint(p1), false, false);
      float t = fadd(__uint_as_float(s2[0]), __uint_as_float(s2[1]));
      t = dpp_add<0xB1>(t); t = dpp_add<0x4E>(t); t = dpp_add<0x141>(t); t = dpp_add<0x140>(t);
      return t * (1.0f / 512.0f);
    };
    const float mg = fold4(mu);
#pragma unroll
    for (int i = 0; i < 4; ++i) {
      const float m = __int_as_float(__builtin_amdgcn_readlane(__float_as_int(mg), 16 * i));
      float s = 0.f;
#pragma unroll
      for (int j = 0; j < 8; ++j) { const float d = e[i][j] - m; s = ffma(d, d, s); }
      va[i] = s;
    }
    const float vg = fold4(va);
    const u2 o = {__float_as_uint(rbf(mg)), __float_as_uint(__builtin_amdgcn_rcpf(rbf(__builtin_amdgcn_sqrtf(rbf(rbf(vg) + 1e-5f)))))};
    if (l15 == 0) __builtin_amdgcn_raw_buffer_store_b64(o, wsr, WS_ROW + (rgrp * 8 + (w & 1) * 4 + g) * 8, 0, 16);
    asm volatile("" ::: "memory");
  };

  BAR();

  if (w < 4) {
    const int qq = l15 >> 2, pp = lane & 3;
    if (g1) {
      __builtin_amdgcn_s_setprio(2);
      const int wm = w >> 1, wn = w & 1;
      f4 acc[2][4][4];
#pragma unroll
      for (int s = 0; s < 2; ++s)
#pragma unroll
        for (int e = 0; e < 4; ++e)
#pragma unroll
          for (int i = 0; i < 4; ++i) acc[s][e][i] = (f4){0.f, 0.f, 0.f, 0.f};
      unsigned xa[2], wa[4];
      xa[0] = (wm * 64 + l15) * 128 + ((g ^ (l15 >> 1)) << 4); xa[1] = xa[0] ^ 64;
#pragma unroll
      for (int e = 0; e < 4; ++e) wa[e] = (8 * g + qq) * 256 + (((4 * wn + e) ^ (qq | ((g & 1) << 2))) << 5) + pp * 8;
      Frag f0, f1;
      s4 lo_;
      auto ldpart = [&](Frag& f, int ks, int j) {
        const unsigned o = 16384 + ks * 8192;
        if (j == 0) f.x[0] = lds128(xa[ks]);
        if (j >= 1 && j <= 16) {
          const int s = (j - 1) >> 3, e = ((j - 1) >> 1) & 3;
          if (j & 1) lo_ = __builtin_amdgcn_ds_read_tr16_b64_v4i16((LAS s4*)(size_t)(wa[e] + o + s * 16384));
          else {
            const s4 hi = __builtin_amdgcn_ds_read_tr16_b64_v4i16((LAS s4*)(size_t)(wa[e] + o + s * 16384 + 1024));
            f.w[s][e] = __builtin_bit_cast(bf8, __builtin_shufflevector(lo_, hi, 0, 1, 2, 3, 4, 5, 6, 7));
          }
        }
        if (j >= 17 && j < 20) f.x[j - 16] = lds128(xa[ks] + (j - 16) * 2048);
      };
      auto mm1 = [&](const Frag& f, int j) { acc[(j >> 2) & 1][j & 3][j >> 3] = mfma(f.w[(j >> 2) & 1][j & 3], f.x[j >> 3], acc[(j >> 2) & 1][j & 3][j >> 3]); };
      auto second = [&](int st) {
        const int d = (st == 2 || st == 5) ? -2 * (int)SLOT : (int)SLOT;
        const unsigned ca = C_LAND + 4 * st + 4;
        unsigned lv;
#pragma unroll
        for (int j = 0; j < 32; ++j) {
          if (j == 31) {
            unsigned sv;
            asm volatile("s_waitcnt lgkmcnt(0)\n\tv_readfirstlane_b32 %0, %1\n\ts_nop 1" : "=s"(sv) : "v"(lv) : "memory");
            if (sv < 2) ctl_wait(ca, 2);
            SB();
          }
          mm1(f0, j);
          if (j < 20) ldpart(f1, 1, j);
          if (j == 20) { xa[0] += d; OPAQUE(xa[0]); xa[1] += d; OPAQUE(xa[1]); }
          if (j == 21) { wa[0] += d; OPAQUE(wa[0]); wa[1] += d; OPAQUE(wa[1]); }
          if (j == 22) { wa[2] += d; OPAQUE(wa[2]); wa[3] += d; OPAQUE(wa[3]); }
          if (j == 25) { ctl_add(ca + (C_CONS - C_LAND - 4)); asm volatile("ds_read_b32 %0, %1" : "=v"(lv) : "v"(ca) : "memory"); }
          SB();
        }
      };
      OPAQUE(xa[0]); OPAQUE(xa[1]); OPAQUE(wa[0]); OPAQUE(wa[1]); OPAQUE(wa[2]); OPAQUE(wa[3]);
      ctl_wait(C_LAND, 2);
      STAMP(2);
#pragma unroll
      for (int j = 0; j < 20; ++j) ldpart(f0, 0, j);
      SB();
      second(0);
#pragma unroll 1
      for (int st = 1; st < 8; ++st) {
#pragma unroll
        for (int j = 0; j < 32; ++j) { mm1(f1, j); if (j < 20) ldpart(f0, 0, j); SB(); }
        second(st);
      }
      STAMP(16);
      constexpr float C0 = -2.0f * 0.7978845608028654f * 1.4426950408889634f, C1 = C0 * 0.044715f;
      f4 cs[2][4];
      f2 rs[4];
      int ln1 = __builtin_amdgcn_mbcnt_hi(-1, __builtin_amdgcn_mbcnt_lo(-1, 0));
      OPAQUE(ln1);
      const int lane = ln1, l15 = lane & 15, g = lane >> 4;
#pragma unroll
      for (int j = 0; j < 32; ++j) {
        mm1(f1, j);
        if (j < 8) cs[j >> 2][j & 3] = lds128f(L_CS + ((j >> 2) * 128 + wn * 64 + (j & 3) * 16 + 4 * g) * 4);
        if (j >= 8 && j < 12) rs[j - 8] = *(const LAS f2*)(size_t)(L_RS + (wm * 64 + (j - 8) * 16 + l15) * 8);
        SB();
      }
      STAMP(3);
      const unsigned hso = WS_H + ((pm * 8 + wm * 4) * 56 + pn * 4 + wn * 2) * 1024 + lane * 16;
#pragma unroll
      for (int i = 0; i < 4; ++i) {
        const float m = -rs[i][0], rr = rs[i][1];
        const float k0 = C0 * rr, k1 = C1 * rr * rr * rr, i2 = __builtin_amdgcn_rcpf(rr * rr);
        const f2 m2 = {m, m}, k02 = {k0, k0}, k12 = {k1, k1}, i22 = {i2, i2};
        f2 a[8], c[8], z[8];
        SB();
#pragma unroll
        for (int j = 0; j < 8; ++j) {
          const int e = j >> 1, h = (j & 1) * 2;
          a[j] = __builtin_elementwise_fma(m2, (f2){cs[0][e][h], cs[0][e][h + 1]}, (f2){acc[0][e][i][h], acc[0][e][i][h + 1]});
        }
        SB();
#pragma unroll
        for (int j = 0; j < 8; ++j) z[j] = a[j] * a[j];
        SB();
#pragma unroll
        for (int j = 0; j < 8; ++j) z[j] = __builtin_elementwise_fma(z[j], k12, k02);
        SB();
#pragma unroll
        for (int j = 0; j < 8; ++j) z[j] = z[j] * a[j];
        SB();
#pragma unroll
        for (int j = 0; j < 8; ++j) {
          const int e = j >> 1, h = (j & 1) * 2;
          z[j] = (f2){__builtin_amdgcn_exp2f(z[j][0]), __builtin_amdgcn_exp2f(z[j][1])};
          c[j] = __builtin_elementwise_fma(m2, (f2){cs[1][e][h], cs[1][e][h + 1]}, (f2){acc[1][e][i][h], acc[1][e][i][h + 1]});
          SB();
        }
#pragma unroll
        for (int j = 0; j < 8; ++j) z[j] = __builtin_elementwise_fma(z[j], i22, i22);
        SB();
#pragma unroll
        for (int j = 0; j < 8; ++j) {
          z[j] = (f2){__builtin_amdgcn_rcpf(z[j][0]), __builtin_amdgcn_rcpf(z[j][1])};
          c[j] = c[j] * a[j];
          SB();
        }
#pragma unroll
        for (int j = 0; j < 8; ++j) c[j] = c[j] * z[j];
        SB();
        u4 o0, o1;
#pragma unroll
        for (int j = 0; j < 4; ++j) { o0[j] = pk2(c[j][0], c[j][1]); o1[j] = pk2(c[4 + j][0], c[4 + j][1]); }
        __builtin_amdgcn_raw_buffer_store_b128(o0, wsr, hso + i * 56 * 1024, 0, 16);
        __builtin_amdgcn_raw_buffer_store_b128(o1, wsr, hso + i * 56 * 1024 + 1024, 0, 16);
      }
      SB();
      ctl_add(C_EPI);
      tag_get();
      __builtin_amdgcn_s_setprio(0);
      STAMP(4);
      WAITV(0);
      if (lane == 0 && __hip_atomic_fetch_add((LAS unsigned*)(size_t)C_HPUB, 1u, __ATOMIC_RELAXED, __HIP_MEMORY_SCOPE_WORKGROUP) == 3)
        flag_set(ws, WS_FH + (pm * 16 + pn) * 128, tag);
    }
    if (!g1) { ctl_wait(C_CVIN, 2); tag_get(); }
    STAMP(5);
    int ln2 = __builtin_amdgcn_mbcnt_hi(-1, __builtin_amdgcn_mbcnt_lo(-1, 0));
    OPAQUE(ln2);
    {
    const int lane = ln2, l15 = lane & 15, g = lane >> 4, qq = l15 >> 2, pp = lane & 3;
    const unsigned dv0 = (lane >> 3) * 1024 + (((lane & 7) ^ (g << 1)) * 16);
    f4 acc[4][4];
#pragma unroll
    for (int t = 0; t < 4; ++t)
#pragma unroll
      for (int i = 0; i < 4; ++i) acc[t][i] = (f4){0.f, 0.f, 0.f, 0.f};
    const unsigned da = gw * 12288 + (4 * g + qq) * 128 + (((2 * g + (qq >> 1)) & 3) << 5) + pp * 8;
    unsigned dlo[4];
#pragma unroll
    for (int t = 0; t < 4; ++t) { dlo[t] = da ^ (t * 32); OPAQUE(dlo[t]); }
    bf8 wf[4][4];
    auto ldw1 = [&](int i, int t) {
      const int s6 = i % 6;
      const unsigned a = dlo[t] + (s6 < 3 ? s6 * 4096 : SLOT + (s6 - 3) * 4096);
      wf[i & 3][t] = ldtr(a, a + 2048);
    };
    ctl_wait(C_WD + (gw >> 1) * 4, 1);
#pragma unroll
    for (int t = 0; t < 16; ++t) ldw1(t >> 2, t & 3);
    WAITL(0);
    wdstage(dv0, gw, 6); wdstage(dv0, gw, 7); wdstage(dv0, gw, 8); wdstage(dv0, gw, 9);
    STAMP(9);
    poll(ws, WS_FH + ((rt >> 1) * 16 + (l15 < 14 ? l15 : 0)) * 128, tag);
    STAMP(6);
    constexpr int HW = 5;
    u4 hf[HW][4];
    unsigned ho[4];
#pragma unroll
    for (int t = 0; t < 4; ++t) ho[t] = WS_H + ((rt * 4 + t) * 56 + gw * 14) * 1024 + lane * 16;
    auto ldh1 = [&](int i, int t) { hf[i % HW][t] = __builtin_amdgcn_raw_buffer_load_b128(wsr, ho[t] + (i & 3) * 1024, (i >> 2) * 4096, 16); };
#pragma unroll
    for (int i = 0; i < HW * 4; ++i) { ldh1(i >> 2, i & 3); SB(); }
#pragma unroll
    for (int i = 0; i < 14; ++i) {
#pragma unroll
      for (int ti = 0; ti < 4; ++ti) {
#pragma unroll
        for (int t = 0; t < 4; ++t) { acc[t][ti] = mfma(wf[i & 3][t], __builtin_bit_cast(bf8, hf[i % HW][ti]), acc[t][ti]); SB(); }
        if (i + HW < 14) { ldh1(i + HW, ti); SB(); }
      }
      if (i == 0) STAMP(19);
      if (i >= 1 && i <= 4) { WAITL(0); wdstage(dv0, gw, i + 9); }
      if (i + 4 < 14) {
        if (i == 6) WAITV(32); else if (i == 7) WAITV(28); else if (i == 8) WAITV(24); else if (i == 9) WAITV(16);
#pragma unroll
        for (int t = 0; t < 4; ++t) ldw1(i + 4, t);
      }
      SB();
    }
    STAMP(7);
    WAITL(0);
#pragma unroll
    for (int ti = 0; ti < 4; ++ti)
#pragma unroll
      for (int t = 0; t < 4; ++t) {
        const int tk = ti * 16 + l15;
        const unsigned pb = (tk < 32 ? 0u : SLOT) + gw * 12288 + (tk & 31) * 256;
        sts128f(pb + (((4 * t + g) ^ (tk & 7)) << 4), acc[t][ti]);
      }
    }
    STAMP(8);
  } else {
    auto bsync = [&](unsigned target) { ctl_add(C_BSYNC); ctl_wait(C_BSYNC, target); };
    unsigned dcr;
    if (w < 6) {
      unsigned tch[4];
      if (g1) {
        stage_half(1, 0); WAITV(12); ctl_add(C_LAND); STAMP(1);
        stage_half(1, 1); stage(2); WAITV(24); ctl_add(C_LAND + 4); STAMP(2);
#pragma unroll 1
        for (int st = 3; st < 8; ++st) {
          ctl_wait(C_CONS + 4 * (st - 3), 4);
          WAITV(0);
          ctl_add(C_LAND + 4 * (st - 1));
          stage(st);
        }
        WAITV(0);
        ctl_add(C_LAND + 28);
      }
      STAMP(3);
      {
        const unsigned o = (r * 448 + (w & 1) * 224 + lane) * 128;
        asm volatile("global_load_dword %0, %4, %6\n\tglobal_load_dword %1, %5, %6\n\tglobal_load_dword %2, %7, %6\n\tglobal_load_dword %3, %8, %6"
                     : "=&v"(tch[0]), "=&v"(tch[1]), "=&v"(tch[2]), "=&v"(tch[3])
                     : "v"(o), "v"(o + 8192), "s"(wd), "v"(o + 16384), "v"(o + (lane < 32 ? 24576 : 0)) : "memory");
      }
      asm volatile("global_load_dword %0, %1, %2" : "=&v"(dcr) : "v"(0u), "s"(sD + b * 4) : "memory");
      const unsigned dv0 = (lane >> 3) * 1024 + (((lane & 7) ^ (g << 1)) * 16);
      if (g1) ctl_wait(C_CONS + 24, 4);
#pragma unroll
      for (int i = 0; i < 6; ++i) wdstage(dv0, (w & 1) * 2 + (i & 1), i >> 1);
      if (g1) ctl_wait(C_CONS + 28, 4);
#pragma unroll
      for (int i = 0; i < 6; ++i) wdstage(dv0, (w & 1) * 2 + (i & 1), 3 + (i >> 1));
      asm volatile("s_waitcnt vmcnt(0)" : "+v"(tch[0]), "+v"(tch[1]), "+v"(tch[2]), "+v"(tch[3]), "+v"(dcr)::"memory");
      ctl_add(C_WD + (w & 1) * 4);
      STAMP(4);
    } else {
      const int sw_ = w & 1, t7 = tid & 127;
      const int cst = 2 * ((r + pnl) & 3) + ((xcd >> 1) & 1);
      const unsigned hoff = (2 * b + sw_) * 4096 + lane * 16;
      u4 wsh[8], hc[4], uc[4]; u2 hp[4];
      const int ptt = b >> 3, pct = b & 7, cp = lane & 31, tg = sw_ * 2 + (lane >> 5);
      unsigned kd[19], vd[16]; u4 shw;
      if (SIDE_DELAY && g1) ctl_wait(C_LAND, 2);
      {
        const char* wb = ((xcd >> 2) ? wu : wg) + (g1 ? pn * 256 + (cst * 64 + sw_ * 32) * 3584 : 0);
        const unsigned o = g * 3584 + l15 * 16;
        asm volatile("global_load_dwordx4 %0, %8, %12\n\tglobal_load_dwordx4 %1, %9, %12\n\tglobal_load_dwordx4 %2, %10, %12\n\tglobal_load_dwordx4 %3, %11, %12\n\t"
                     "global_load_dwordx4 %4, %8, %13\n\tglobal_load_dwordx4 %5, %9, %13\n\tglobal_load_dwordx4 %6, %10, %13\n\tglobal_load_dwordx4 %7, %11, %13"
                     : "=&v"(wsh[0]), "=&v"(wsh[1]), "=&v"(wsh[2]), "=&v"(wsh[3]), "=&v"(wsh[4]), "=&v"(wsh[5]), "=&v"(wsh[6]), "=&v"(wsh[7])
                     : "v"(o), "v"(o + 4 * 3584), "v"(o + 8 * 3584), "v"(o + 12 * 3584), "s"(wb), "s"(wb + 16 * 3584) : "memory");
        rows_load();
        const unsigned vo = tg * 16384 + cp * 4 + 4096, vo8 = vo + 8192;
        const unsigned vh = (ptt == 0 && tg == 0) ? vo - 1024 : vo - 4096;
        const char* kb = kk + ptt * 65536 + pct * 128;
        const char* vb = vv + ptt * 65536 + pct * 128;
#define LD8(d, o, vreg, sreg) asm volatile( \
          "global_load_dword %0, %8, %9 offset:-4096\n\tglobal_load_dword %1, %8, %9 offset:-3072\n\tglobal_load_dword %2, %8, %9 offset:-2048\n\t" \
          "global_load_dword %3, %8, %9 offset:-1024\n\tglobal_load_dword %4, %8, %9\n\tglobal_load_dword %5, %8, %9 offset:1024\n\t" \
          "global_load_dword %6, %8, %9 offset:2048\n\tglobal_load_dword %7, %8, %9 offset:3072" \
          : "=&v"(d[o]), "=&v"(d[o + 1]), "=&v"(d[o + 2]), "=&v"(d[o + 3]), "=&v"(d[o + 4]), "=&v"(d[o + 5]), "=&v"(d[o + 6]), "=&v"(d[o + 7]) \
          : "v"(vreg), "s"(sreg) : "memory")
        asm volatile("global_load_dword %0, %4, %5 offset:-3072\n\tglobal_load_dword %1, %4, %5 offset:-2048\n\tglobal_load_dword %2, %4, %5 offset:-1024\n\t"
                     "global_load_dwordx4 %3, %6, %7"
                     : "=&v"(kd[0]), "=&v"(kd[1]), "=&v"(kd[2]), "=&v"(shw) : "v"(vh), "s"(kb), "v"(cp * 16), "s"(shk + pct * 512) : "memory");
        LD8(kd, 3, vo, kb); LD8(vd, 0, vo, vb); LD8(kd, 11, vo8, kb); LD8(vd, 8, vo8, vb);
      }
      asm volatile("global_load_dwordx4 %0, %9, %11\n\tglobal_load_dwordx4 %1, %9, %11 offset:1024\n\tglobal_load_dwordx4 %2, %9, %11 offset:2048\n\t"
                   "global_load_dwordx4 %3, %9, %11 offset:3072\n\tglobal_load_dwordx2 %4, %10, %11\n\tglobal_load_dwordx2 %5, %9, %11 offset:1016\n\t"
                   "global_load_dwordx2 %6, %9, %11 offset:2040\n\tglobal_load_dwordx2 %7, %9, %11 offset:3064\n\tglobal_load_dword %8, %12, %13"
                   : "=&v"(hc[0]), "=&v"(hc[1]), "=&v"(hc[2]), "=&v"(hc[3]), "=&v"(hp[0]), "=&v"(hp[1]), "=&v"(hp[2]), "=&v"(hp[3]), "=&v"(dcr)
                   : "v"(hoff), "v"(lane ? hoff - 8 : hoff), "s"(ssk), "v"(0u), "s"(sD + b * 4) : "memory");
      tag_fetch();
      asm volatile("s_waitcnt vmcnt(45)" : "+v"(wsh[0]), "+v"(wsh[1]), "+v"(wsh[2]), "+v"(wsh[3]), "+v"(wsh[4]), "+v"(wsh[5]), "+v"(wsh[6]), "+v"(wsh[7])::"memory");
      STAMP(17);
      if (g1) {
        float cacc[8];
#pragma unroll
        for (int j = 0; j < 4; ++j) {
          cacc[2 * j] = fadd(fadd(fadd(bflo(wsh[0][j]), bflo(wsh[1][j])), fadd(bflo(wsh[2][j]), bflo(wsh[3][j]))),
                             fadd(fadd(bflo(wsh[4][j]), bflo(wsh[5][j])), fadd(bflo(wsh[6][j]), bflo(wsh[7][j]))));
          cacc[2 * j + 1] = fadd(fadd(fadd(bfhi(wsh[0][j]), bfhi(wsh[1][j])), fadd(bfhi(wsh[2][j]), bfhi(wsh[3][j]))),
                                 fadd(fadd(bfhi(wsh[4][j]), bfhi(wsh[5][j])), fadd(bfhi(wsh[6][j]), bfhi(wsh[7][j]))));
        }
        float c4[4], c2[2];
#pragma unroll
        for (int j = 0; j < 4; ++j) {
          const auto s2 = __builtin_amdgcn_permlane16_swap(__float_as_uint(cacc[j]), __float_as_uint(cacc[4 + j]), false, false);
          c4[j] = fadd(__uint_as_float(s2[0]), __uint_as_float(s2[1]));
        }
#pragma unroll
        for (int j = 0; j < 2; ++j) {
          const auto s2 = __builtin_amdgcn_permlane32_swap(__float_as_uint(c4[j]), __float_as_uint(c4[2 + j]), false, false);
          c2[j] = fadd(__uint_as_float(s2[0]), __uint_as_float(s2[1]));
        }
        __builtin_amdgcn_raw_buffer_store_b64((u2){__float_as_uint(c2[0]), __float_as_uint(c2[1])}, wsr,
                                              WS_COL + ((pn * 16 + cst * 2 + sw_) * 256 + (xcd >> 2) * 128 + l15 * 8 + (g & 1) * 4 + (g >> 1) * 2) * 4, 0, 16);
      }
      rows_stat();
      STAMP(18);
      asm volatile("s_waitcnt vmcnt(0)" : "+v"(kd[0]), "+v"(kd[1]), "+v"(kd[2]), "+v"(kd[3]), "+v"(kd[4]), "+v"(kd[5]), "+v"(kd[6]), "+v"(kd[7]), "+v"(kd[8]),
                   "+v"(kd[9]), "+v"(kd[10]), "+v"(kd[11]), "+v"(kd[12]), "+v"(kd[13]), "+v"(kd[14]), "+v"(kd[15]), "+v"(kd[16]), "+v"(kd[17]), "+v"(kd[18]),
                   "+v"(shw), "+v"(dcr)::"memory");
      asm volatile("" : "+v"(vd[0]), "+v"(vd[1]), "+v"(vd[2]), "+v"(vd[3]), "+v"(vd[4]), "+v"(vd[5]), "+v"(vd[6]), "+v"(vd[7]), "+v"(vd[8]), "+v"(vd[9]),
                   "+v"(vd[10]), "+v"(vd[11]), "+v"(vd[12]), "+v"(vd[13]), "+v"(vd[14]), "+v"(vd[15]),
                   "+v"(hc[0]), "+v"(hc[1]), "+v"(hc[2]), "+v"(hc[3]), "+v"(hp[0]), "+v"(hp[1]), "+v"(hp[2]), "+v"(hp[3])::"memory");
      ctl_add(C_WSYNC); ctl_wait(C_WSYNC, 2);
      if (tid == 384) flag_set(ws, WS_FS + rgrp * 128, tag);
      STAMP(20);
      {
        if (ptt == 0 && tg == 0) { kd[0] = 0; kd[1] = 0; kd[2] = 0; }
#pragma unroll
        for (int c = 0; c < 2; ++c) {
          const float w0 = bflo(shw[2 * c]), w1 = bfhi(shw[2 * c]), w2 = bflo(shw[2 * c + 1]), w3 = bfhi(shw[2 * c + 1]);
          float kf[19], u[16];
#pragma unroll
          for (int i = 0; i < 19; ++i) kf[i] = c ? bfhi(kd[i]) : bflo(kd[i]);
          SB();
#pragma unroll
          for (int i = 0; i < 16; ++i) u[i] = fmul(kf[i], w3);
          SB();
#pragma unroll
          for (int i = 0; i < 16; ++i) u[i] = ffma(kf[i + 1], w2, u[i]);
          SB();
#pragma unroll
          for (int i = 0; i < 16; ++i) u[i] = ffma(kf[i + 2], w1, u[i]);
          SB();
#pragma unroll
          for (int i = 0; i < 16; ++i) u[i] = ffma(kf[i + 3], w0, u[i]);
          SB();
#pragma unroll
          for (int i = 0; i < 16; ++i) u[i] = fmul(u[i], c ? bfhi(vd[i]) : bflo(vd[i]));
          SB();
          const unsigned ad = L_MISC + cp * 256 + c * 128;
          sts128(ad + (((6 - 2 * tg) ^ (cp & 7)) << 4), (u4){pk2(u[15], u[14]), pk2(u[13], u[12]), pk2(u[11], u[10]), pk2(u[9], u[8])});
          sts128(ad + (((7 - 2 * tg) ^ (cp & 7)) << 4), (u4){pk2(u[7], u[6]), pk2(u[5], u[4]), pk2(u[3], u[2]), pk2(u[1], u[0])});
        }
        ctl_add(C_WSYNC); ctl_wait(C_WSYNC, 4);
#pragma unroll
        for (int z = 0; z < 4; ++z) {
          const int id = t7 + 128 * z, c2 = id >> 3, ch = id & 7;
          const u4 t = lds128u(L_MISC + c2 * 128 + ((ch ^ ((c2 >> 1) & 7)) << 4));
          __builtin_amdgcn_raw_buffer_store_b128(t, wsr, WS_KVT + (pct * 64 + c2) * 4096 + 3968 - ptt * 128 + ch * 16, 0, 16);
        }
        STAMP(21);
        WAITV(0);
        ctl_add(C_WSYNC); ctl_wait(C_WSYNC, 6);
        if (tid == 384) flag_set(ws, WS_FK + (pct * 32 + ptt) * 128, tag);
        STAMP(19);
      }
      u4 c1[4];
      unsigned a0[4];
      if (lane == 0) { hp[0][0] = 0; hp[0][1] = 0; }
#pragma unroll
      for (int z = 0; z < 4; ++z) {
        a0[z] = __builtin_amdgcn_alignbit(hp[z][1], hp[z][0], 16);
        c1[z] = (u4){__builtin_amdgcn_alignbit(hc[z][0], hp[z][1], 16), __builtin_amdgcn_alignbit(hc[z][1], hc[z][0], 16),
                     __builtin_amdgcn_alignbit(hc[z][2], hc[z][1], 16), __builtin_amdgcn_alignbit(hc[z][3], hc[z][2], 16)};
      }
      if (g1) {
        const int j = l15;
        const int bc = (j >> 2) * 64 + (xcd & 1) * 32 + (j & 3) + 4 * pnl;
        poll(ws, WS_FS + (lane & 16 ? bc : pm * 16 + j) * 128, tag);
        STAMP(13);
        u2 rsv, csv[16];
        rsv = __builtin_amdgcn_raw_buffer_load_b64(wsr, WS_ROW + pm * 1024 + t7 * 8, 0, 16);
#pragma unroll
        for (int i = 0; i < 16; ++i) csv[i] = __builtin_amdgcn_raw_buffer_load_b64(wsr, WS_COL + pn * 16384 + t7 * 8 + (i & 3) * 1024, (i >> 2) * 4096, 16);
        *(LAS u2*)(size_t)(L_RS + t7 * 8) = rsv;
        float s0 = 0.f, s1 = 0.f;
#pragma unroll
        for (int i = 0; i < 16; ++i) { s0 = fadd(s0, __uint_as_float(csv[i][0])); s1 = fadd(s1, __uint_as_float(csv[i][1])); }
        *(LAS u2*)(size_t)(L_CS + t7 * 8) = (u2){__float_as_uint(s0), __float_as_uint(s1)};
        ctl_add(C_LAND + 32);
        STAMP(16);
      }
      poll(ws, WS_FK + ((b >> 5) * 32 + (lane & 31)) * 128, tag);
      STAMP(14);
#pragma unroll
      for (int z = 0; z < 4; ++z) uc[z] = __builtin_amdgcn_raw_buffer_load_b128(wsr, WS_KVT + hoff + z * 1024, 0, 16);
      if (b == 255 && w == 7) {
        poll(ws, WS_FS + lane * 128, tag); poll(ws, WS_FS + (64 + lane) * 128, tag);
        poll(ws, WS_FS + (128 + lane) * 128, tag); poll(ws, WS_FS + (192 + lane) * 128, tag);
        if (lane == 0) flag_set(ws, WS_EPOCH, tag);
      }
      if (g1) ctl_wait(C_CONS + 20, 4);
      const unsigned base = L_CV + sw_ * CV_CH;
#pragma unroll
      for (int z = 0; z < 4; ++z) {
        const unsigned p = base + 32 + lane * 16 + z * 1024;
        sts128(p, hc[z]);
        sts128(p + CV_COPY, c1[z]);
        sts128(p + 2 * CV_COPY, (u4){hp[z][1], hc[z][0], hc[z][1], hc[z][2]});
        sts128(p + 3 * CV_COPY, (u4){a0[z], c1[z][0], c1[z][1], c1[z][2]});
      }
      if (lane < 8) sts128(base + (lane >> 1) * CV_COPY + (lane & 1) * 16, (u4){0, 0, 0, 0});
      if (lane >= 32) sts128(base + CV_UR + 4096 + (lane - 32) * 16, (u4){0, 0, 0, 0});
#pragma unroll
      for (int z = 0; z < 4; ++z) sts128(base + CV_UR + lane * 16 + z * 1024, uc[z]);
      ctl_add(C_CVIN);
      STAMP(15);
    }
    u4 xq[2];
#pragma unroll
    for (int z = 0; z < 2; ++z) {
      const int id = t8 + 256 * z;
      xq[z] = *(const GAS u4*)(q + (rt * 64 + (id >> 3)) * 1024 + ct2 * 128 + (id & 7) * 16);
    }
    ctl_wait(C_CVIN, 2);
    tag_get();
    const int ch = gw >> 1, half = gw & 1;
    const unsigned base = L_CV + ch * CV_CH;
    const unsigned o = 8 * g + l15 + 1, kap = (0u - o) & 3;
    unsigned aa = base + kap * CV_COPY + 2 * (o + kap) + half * 256;
    unsigned ba = base + CV_UR + 2 * (2032 - 16 * l15 + 8 * g) - 3584 + half * 256;
    OPAQUE(aa); OPAQUE(ba);
    f4 y[8];
#pragma unroll
    for (int n = 0; n < 8; ++n) y[n] = (f4){0.f, 0.f, 0.f, 0.f};
    bf8 A[2][8], B[2][8];
    auto ldab = [&](int rho, int i) {
      const u2 lo = *(const volatile LAS u2*)(size_t)(aa + rho * 64 + i * 512), hi = *(const volatile LAS u2*)(size_t)(aa + rho * 64 + i * 512 + 8);
      A[rho & 1][i] = __builtin_bit_cast(bf8, (u4){lo[0], lo[1], hi[0], hi[1]});
      B[rho & 1][i] = lds128(ba + rho * 64 + (7 - i) * 512);
    };
#pragma unroll
    for (int i = 0; i < 8; ++i) ldab(0, i);
    SB();
    if (g1) ctl_wait(C_EPI, 4);
    STAMP(5);
    __builtin_amdgcn_s_setprio(1);
#pragma unroll
    for (int rho = 0; rho < 4; ++rho) {
      int cnt = 0;
#pragma unroll
      for (int lv = 0; lv < 8; ++lv)
#pragma unroll
        for (int qi = 0; qi <= lv; ++qi)
#pragma unroll
          for (int p = 0; p <= lv; ++p)
            if ((qi == lv || p == lv) && qi + p < 8) {
              y[qi + p] = mfma(A[rho & 1][qi], B[rho & 1][p], y[qi + p]);
              if (rho < 3 && (cnt & 3) == 0 && (cnt >> 2) < 8) ldab(rho + 1, cnt >> 2);
              ++cnt;
              SB();
            }
    }
    STAMP(6);
    const unsigned ex0 = gw < 3 ? L_MISC + gw * 4096 : L_CV + 2 * CV_CH, ex1 = (gw ^ 1) < 3 ? L_MISC + (gw ^ 1) * 4096 : L_CV + 2 * CV_CH;
#pragma unroll
    for (int n = 0; n < 4; ++n) {
      if (half) { const f4 t = y[n]; y[n] = y[4 + n]; y[4 + n] = t; }
      sts128f(ex0 + n * 1024 + lane * 16, y[4 + n]);
    }
    bsync(4);
    const float dc = ch ? bfhi(dcr) : bflo(dcr);
    u2 pk[4];
#pragma unroll
    for (int n = 0; n < 4; ++n) {
      const f4 t = lds128f(ex1 + n * 1024 + lane * 16);
      const int t0 = 256 * (4 * half + n) + 16 * l15 + 4 * g;
      const u2 uu = *(const LAS u2*)(size_t)(base + CV_UR + 2 * (2044 - t0));
      pk[n] = (u2){pk2(ffma(bfhi(uu[1]), dc, fadd(t[0], y[n][0])), ffma(bflo(uu[1]), dc, fadd(t[1], y[n][1]))),
                   pk2(ffma(bfhi(uu[0]), dc, fadd(t[2], y[n][2])), ffma(bflo(uu[0]), dc, fadd(t[3], y[n][3])))};
    }
#pragma unroll
    for (int n = 0; n < 4; n += 2) {
      const auto s0 = __builtin_amdgcn_permlane16_swap(pk[n][0], pk[n + 1][0], false, false);
      const auto s1 = __builtin_amdgcn_permlane16_swap(pk[n][1], pk[n + 1][1], false, false);
      const u4 o4 = {s0[0], s1[0], s0[1], s1[1]};
      __builtin_amdgcn_raw_buffer_store_b128(o4, wsr, WS_YT + (2 * b + ch) * 4096 + 2 * (256 * (4 * half + n + (g & 1)) + 16 * l15 + 8 * (g >> 1)), 0, 16);
    }
    STAMP(22);
    WAITV(0);
    bsync(8);
    if (tid == 256) flag_set(ws, WS_FY + b * 128, tag);
    STAMP(7);
    if (gw == 3) {
      poll(ws, lane < 32 ? WS_FY + (ct2 * 32 + lane) * 128 : WS_FS + (rt * 8 + (lane & 7)) * 128, tag);
      ctl_add(C_GO);
    } else ctl_wait(C_GO, 1);
    STAMP(8);
    u4 yq[2]; u2 st2[2];
#pragma unroll
    for (int z = 0; z < 2; ++z) {
      const int id = t8 + 256 * z;
      yq[z] = __builtin_amdgcn_raw_buffer_load_b128(wsr, WS_YT + (ct2 * 64 + (id >> 3)) * 4096 + rt * 128 + (id & 7) * 16, 0, 16);
      st2[z] = __builtin_amdgcn_raw_buffer_load_b64(wsr, WS_ROW + (rt * 64 + (id >> 3)) * 8, 0, 16);
    }
#pragma unroll
    for (int z = 0; z < 2; ++z) {
      const int id = t8 + 256 * z, c = id >> 3;
      sts128(L_YI + c * 128 + (((id & 7) ^ (c >> 3)) << 4), yq[z]);
    }
    STAMP(23);
    bsync(12);
#pragma unroll
    for (int z = 0; z < 2; ++z) {
      const int id = t8 + 256 * z, tk = id >> 3, c8 = id & 7;
      const float m = __uint_as_float(st2[z][0]), rr = __uint_as_float(st2[z][1]);
      const unsigned ya = L_YI + c8 * 1024 + (((tk >> 3) ^ c8) << 4) + (tk & 7) * 2;
      u4 so;
#pragma unroll
      for (int j = 0; j < 4; ++j) {
        const float y0 = __uint_as_float(((unsigned)*(const LAS unsigned short*)(size_t)(ya + (2 * j) * 128)) << 16);
        const float y1 = __uint_as_float(((unsigned)*(const LAS unsigned short*)(size_t)(ya + (2 * j + 1) * 128)) << 16);
        const unsigned d = pk2(fsub(bflo(xq[z][j]), m), fsub(bfhi(xq[z][j]), m));
        const unsigned xn = pk2(fmul(bflo(d), rr), fmul(bfhi(d), rr));
        so[j] = pk2(fmul(bflo(xn), y0), fmul(bfhi(xn), y1));
      }
      sts128(L_SI + id * 16, so);
      sts128(L_XI + id * 16, xq[z]);
    }
    __builtin_amdgcn_s_setprio(0);
    STAMP(9);
  }
  BAR();
  STAMP(10);
  {
    int ln3 = __builtin_amdgcn_mbcnt_hi(-1, __builtin_amdgcn_mbcnt_lo(-1, 0));
    OPAQUE(ln3);
    const int tid = w * 64 + ln3;
    const int tk = tid >> 3, c8 = tid & 7;
    const unsigned pb = (tk < 32 ? 0u : SLOT) + (tk & 31) * 256;
    f4 o0 = {0.f, 0.f, 0.f, 0.f}, o1 = {0.f, 0.f, 0.f, 0.f};
#pragma unroll
    for (int k4 = 0; k4 < 4; ++k4) {
      o0 += lds128f(pb + k4 * 12288 + (((2 * c8) ^ (tk & 7)) << 4));
      o1 += lds128f(pb + k4 * 12288 + (((2 * c8 + 1) ^ (tk & 7)) << 4));
    }
    const u4 sv = lds128u(L_SI + tid * 16), xv = lds128u(L_XI + tid * 16);
    const float ov[8] = {o0[0], o0[1], o0[2], o0[3], o1[0], o1[1], o1[2], o1[3]};
    u4 res;
#pragma unroll
    for (int j = 0; j < 4; ++j) {
      const unsigned ob = pk2(ov[2 * j], ov[2 * j + 1]);
      const float a0 = bflo(ob), a1 = bfhi(ob);
      const unsigned gb2 = pk2(a0 * __builtin_amdgcn_rcpf(1.0f + __builtin_amdgcn_exp2f(-1.4426950408889634f * a0)),
                               a1 * __builtin_amdgcn_rcpf(1.0f + __builtin_amdgcn_exp2f(-1.4426950408889634f * a1)));
      const unsigned sg = pk2(bflo(sv[j]) * bflo(gb2), bfhi(sv[j]) * bfhi(gb2));
      res[j] = pk2(bflo(xv[j]) + bflo(sg), bfhi(xv[j]) + bfhi(sg));
    }
    *(GAS u4*)(out + (rt * 64 + tk) * 1024 + ct2 * 128 + c8 * 16) = res;
  }
  STAMP(11);
  asm volatile("s_waitcnt vmcnt(0)" ::: "memory");
  STAMP(12);
  if (b == PROBE_B && w == PROBE_W) {
    const unsigned until = tt[12] + 8u * (tt[PROBE_I] - tt[0]);
    for (int it = 0; it < (1 << 20); ++it) { u64 t_; asm volatile("s_memrealtime %0\n\ts_waitcnt lgkmcnt(0)" : "=s"(t_)::"memory"); if ((int)((unsigned)t_ - until) >= 0) break; }
  }
}
}

extern "C" void kernel_launch(void* const* d_in, const int* in_sizes, int n_in, void* d_out, int out_size, void* d_ws, size_t ws_size,
                              hipStream_t stream) {
  (void)in_sizes; (void)n_in; (void)out_size; (void)ws_size;
  (void)hipFuncSetAttribute((const void*)h3_geglu_fused, hipFuncAttributeMaxDynamicSharedMemorySize, LDS_BYTES);
  h3_geglu_fused<<<256, 512, LDS_BYTES, stream>>>((const char*)d_in[0], (const char*)d_in[1], (const char*)d_in[2], (const char*)d_in[3],
                                                  (const char*)d_in[4], (const char*)d_in[5], (const char*)d_in[7], (const char*)d_in[8],
                                                  (const char*)d_in[9], (char*)d_out, (char*)d_ws);
}
```

```cpp
#include <hip/hip_runtime.h>
#include <stdint.h>

#define LAS __attribute__((address_space(3)))
#define GAS __attribute__((address_space(1)))
#define DI __device__ __forceinline__
typedef __bf16 bf8 __attribute__((ext_vector_type(8)));
typedef __bf16 bf2 __attribute__((ext_vector_type(2)));
typedef short s4 __attribute__((ext_vector_type(4)));
typedef float f4 __attribute__((ext_vector_type(4)));
typedef float f2 __attribute__((ext_vector_type(2)));
typedef unsigned u4 __attribute__((ext_vector_type(4)));
typedef unsigned u2 __attribute__((ext_vector_type(2)));
typedef unsigned long long u64;

namespace {
constexpr unsigned WS_EPOCH = 0;
constexpr unsigned WS_FK = 4096;
constexpr unsigned WS_FS = WS_FK + 256 * 128;
constexpr unsigned WS_FH = WS_FS + 256 * 128;
constexpr unsigned WS_FY = WS_FH + 256 * 128;
constexpr unsigned WS_ROW = WS_FY + 256 * 128;
constexpr unsigned WS_COL = WS_ROW + 2048 * 8;
constexpr unsigned WS_KVT = 1u << 20;
constexpr unsigned WS_YT = 3u << 20;
constexpr unsigned WS_H = 5u << 20;
constexpr unsigned WS_PROF = 32u << 20;
constexpr unsigned SLOT = 49152;
constexpr unsigned L_MISC = 3 * SLOT;
constexpr unsigned L_RS = L_MISC + 12288;
constexpr unsigned L_CS = L_RS + 1024;
constexpr unsigned L_CTL = L_CS + 1024;
constexpr unsigned C_LAND = L_CTL, C_CONS = L_CTL + 40, C_BSYNC = L_CTL + 72, C_WSYNC = L_CTL + 76, C_CVIN = L_CTL + 80,
                   C_EPI = L_CTL + 84, C_HPUB = L_CTL + 88, C_GO = L_CTL + 92, C_WD = L_CTL + 96, L_TAG = L_CTL + 128;
constexpr unsigned L_CV = 2 * SLOT;
constexpr unsigned CV_COPY = 4160, CV_UR = 4 * CV_COPY, CV_CH = CV_UR + 4608;
constexpr unsigned L_YI = L_CV, L_SI = L_CV + 8192, L_XI = L_CV + 16384;
constexpr unsigned LDS_BYTES = 163840;
constexpr int SPIN_CAP = 1 << 18;

DI float bflo(unsigned u) { return __uint_as_float(u << 16); }
DI float bfhi(unsigned u) { return __uint_as_float(u & 0xffff0000u); }
DI unsigned pk2(float a, float b) { f2 v = {a, b}; return __builtin_bit_cast(unsigned, __builtin_convertvector(v, bf2)); }
DI float dot2(unsigned a, bf2 b, float c) { return __builtin_amdgcn_fdot2_f32_bf16(__builtin_bit_cast(bf2, a), b, c, false); }
DI float rbf(float a) { return bflo(pk2(a, a)); }
DI f4 mfma(bf8 a, bf8 b, f4 c) { return __builtin_amdgcn_mfma_f32_16x16x32_bf16(a, b, c, 0, 0, 0); }
DI bf8 lds128(unsigned a) { return *(const LAS bf8*)(size_t)a; }
DI u4 lds128u(unsigned a) { return *(const LAS u4*)(size_t)a; }
DI f4 lds128f(unsigned a) { return *(const LAS f4*)(size_t)a; }
DI void sts128(unsigned a, u4 v) { *(LAS u4*)(size_t)a = v; }
DI void sts128f(unsigned a, f4 v) { *(LAS f4*)(size_t)a = v; }
DI bf8 ldtr(unsigned a0, unsigned a1) {
  s4 lo = __builtin_amdgcn_ds_read_tr16_b64_v4i16((LAS s4*)(size_t)a0);
  s4 hi = __builtin_amdgcn_ds_read_tr16_b64_v4i16((LAS s4*)(size_t)a1);
  return __builtin_bit_cast(bf8, __builtin_shufflevector(lo, hi, 0, 1, 2, 3, 4, 5, 6, 7));
}
#define WAITV(n) asm volatile("s_waitcnt vmcnt(%0)" ::"i"(n) : "memory")
#define WAITL(n) asm volatile("s_waitcnt lgkmcnt(%0)" ::"i"(n) : "memory")
#define SB() __builtin_amdgcn_sched_barrier(0)
#define OPAQUE(x) asm volatile("" : "+v"(x))
#define BAR() do { asm volatile("s_waitcnt lgkmcnt(0)" ::: "memory"); __builtin_amdgcn_s_barrier(); asm volatile("" ::: "memory"); } while (0)

DI void glds2(unsigned v0, unsigned v1, const char* sb, unsigned l) {
  asm volatile("s_mov_b32 m0, %3\n\ts_nop 0\n\tglobal_load_lds_dwordx4 %0, %2\n\tglobal_load_lds_dwordx4 %1, %2 offset:1024"
               ::"v"(v0), "v"(v1), "s"(sb), "s"(l) : "memory");
}
DI void glds4(unsigned v0, unsigned v1, unsigned v2, unsigned v3, const char* sb, unsigned l) {
  asm volatile("s_mov_b32 m0, %5\n\ts_nop 0\n\tglobal_load_lds_dwordx4 %0, %4\n\tglobal_load_lds_dwordx4 %1, %4 offset:1024\n\t"
               "global_load_lds_dwordx4 %2, %4 offset:2048\n\tglobal_load_lds_dwordx4 %3, %4 offset:3072"
               ::"v"(v0), "v"(v1), "v"(v2), "v"(v3), "s"(sb), "s"(l) : "memory");
}
DI void flag_set(char* ws, unsigned off, u64 tag) {
  __hip_atomic_store((GAS u64*)(ws + off), tag, __ATOMIC_RELAXED, __HIP_MEMORY_SCOPE_AGENT);
}
DI void poll(const char* ws, unsigned off, u64 tag) {
#pragma unroll 1
  for (int it = 0; it < SPIN_CAP; ++it) {
    u64 x = __hip_atomic_load((GAS u64*)(ws + off), __ATOMIC_RELAXED, __HIP_MEMORY_SCOPE_AGENT);
    if (__all(x == tag)) break;
    __builtin_amdgcn_s_sleep(4);
  }
  asm volatile("" ::: "memory");
}
DI float fadd(float a, float b) { float r; asm("v_add_f32 %0, %1, %2" : "=v"(r) : "v"(a), "v"(b)); return r; }
DI float fsub(float a, float b) { float r; asm("v_sub_f32 %0, %1, %2" : "=v"(r) : "v"(a), "v"(b)); return r; }
DI float fmul(float a, float b) { float r; asm("v_mul_f32 %0, %1, %2" : "=v"(r) : "v"(a), "v"(b)); return r; }
DI float ffma(float a, float b, float c) { float r; asm("v_fma_f32 %0, %1, %2, %3" : "=v"(r) : "v"(a), "v"(b), "v"(c)); return r; }
template <int CTRL> DI float dpp_add(float v) {
  return v + __int_as_float(__builtin_amdgcn_update_dpp(0, __float_as_int(v), CTRL, 0xf, 0xf, true));
}
DI float wave_sum(float v) {
  v = dpp_add<0xB1>(v); v = dpp_add<0x4E>(v); v = dpp_add<0x141>(v); v = dpp_add<0x140>(v);
  const int i = __float_as_int(v);
  return (__int_as_float(__builtin_amdgcn_readlane(i, 0)) + __int_as_float(__builtin_amdgcn_readlane(i, 16))) +
         (__int_as_float(__builtin_amdgcn_readlane(i, 32)) + __int_as_float(__builtin_amdgcn_readlane(i, 48)));
}
DI void ctl_add(unsigned addr) {
  asm volatile("s_waitcnt lgkmcnt(0)\n\ts_mov_b64 exec, 1\n\tds_add_u32 %0, %1\n\ts_mov_b64 exec, -1" ::"v"(addr), "v"(1u) : "memory");
}
DI unsigned ctl_get(unsigned addr) {
  unsigned s, t;
  asm volatile("ds_read_b32 %1, %2\n\ts_waitcnt lgkmcnt(0)\n\tv_readfirstlane_b32 %0, %1\n\ts_nop 1" : "=s"(s), "=&v"(t) : "v"(addr) : "memory");
  return s;
}
DI void ctl_wait(unsigned addr, unsigned target) {
#pragma unroll 1
  for (int it = 0; it < SPIN_CAP; ++it) {
    if (ctl_get(addr) >= target) break;
    __builtin_amdgcn_s_sleep(1);
  }
}
#define STAMP(i)
#define CNOW(v)
#define CSTAMP(i)

struct Frag { bf8 x[4]; bf8 w[2][4]; };
#define SIDE_DELAY 0

__global__ __launch_bounds__(512, 2) void h3_geglu_fused(const char* q, const char* kk, const char* vv, const char* shk, const char* ssk,
                                                         const char* sD, const char* wg, const char* wu, const char* wd, char* out, char* ws) {
  extern __shared__ __attribute__((aligned(16))) char smem[];
  const int tid = threadIdx.x, lane = tid & 63, t8 = tid & 255;
  const int w = __builtin_amdgcn_readfirstlane(tid >> 6), gw = w & 3;
  const int b = blockIdx.x, xcd = b & 7, r = b >> 3;
  const bool g1 = r < 28;
  const int l15 = lane & 15, g = lane >> 4;
  const __amdgpu_buffer_rsrc_t wsr = __builtin_amdgcn_make_buffer_rsrc(ws, 0, 16 << 20, 0x00020000);
  const int pnl = r >> 2;
  const int pm = (xcd >> 1) * 4 + (r & 3), pn = (xcd & 1) * 7 + pnl;
  const int rt = xcd * 4 + (r & 3), ct2 = r >> 2;
  const int rgrp = (xcd >> 1) * 64 + (xcd & 1) * 32 + r;
  auto wdstage = [&](unsigned dv0, int kq, int i) {
    const int s6 = i % 6;
    glds4(dv0, dv0 + 7168, dv0 + 2 * 7168, dv0 + 3 * 7168, wd + ct2 * 128 + (kq * 14 + i) * 32768, (s6 < 3 ? s6 * 4096 : SLOT + (s6 - 3) * 4096) + kq * 12288);
  };
  unsigned xv[4], wv[4];
  const char* xb = q + pm * 131072 + (w & 1) * 65536;
  const char* gb = wg + pn * 256 + (w & 1) * (32 * 3584);
  const char* ub = wu + pn * 256 + (w & 1) * (32 * 3584);
  auto stage_half = [&](int st, int hf) {
    const unsigned l = (st % 3) * SLOT + (w & 1) * 8192 + hf * 4096;
    glds4(xv[0], xv[1], xv[2], xv[3], xb + st * 128 + hf * 32768, l);
    glds4(wv[0], wv[1], wv[2], wv[3], gb + st * (64 * 3584) + hf * (16 * 3584), l + 16384);
    glds4(wv[0], wv[1], wv[2], wv[3], ub + st * (64 * 3584) + hf * (16 * 3584), l + 32768);
  };
  auto stage = [&](int st) { stage_half(st, 0); stage_half(st, 1); };
  if ((w >> 1) == 2) {
#pragma unroll
    for (int p = 0; p < 4; ++p) {
      xv[p] = (p * 8 + (lane >> 3)) * 1024 + (((lane & 7) ^ (((p & 1) << 2) | g)) << 4) - p * 1024;
      wv[p] = (p * 4 + g) * 3584 + ((l15 ^ ((g | ((p >> 1) << 2)) << 1)) << 4) - p * 1024;
    }
    if (g1) stage(0);
  }
  if (tid < 32) *(LAS unsigned*)(size_t)(L_CTL + tid * 4) = 0;
  u64 tag;
  auto tag_fetch = [&]() {
    asm volatile("s_load_dwordx2 %0, %1, 0x0\n\ts_waitcnt lgkmcnt(0)" : "=s"(tag) : "s"(ws) : "memory");
    tag += 1;
    *(LAS u64*)(size_t)L_TAG = tag;
  };
  auto tag_get = [&]() {
    const u2 t = *(const volatile LAS u2*)(size_t)L_TAG;
    tag = ((u64)(unsigned)__builtin_amdgcn_readfirstlane(t[1]) << 32) | (unsigned)__builtin_amdgcn_readfirstlane(t[0]);
  };
  u4 xr[4];
  auto rows_load = [&]() {
    asm volatile("global_load_dwordx4 %0, %4, %5\n\tglobal_load_dwordx4 %1, %4, %5 offset:1024\n\tglobal_load_dwordx4 %2, %4, %5 offset:2048\n\t"
                 "global_load_dwordx4 %3, %4, %5 offset:3072"
                 : "=&v"(xr[0]), "=&v"(xr[1]), "=&v"(xr[2]), "=&v"(xr[3]) : "v"(lane * 16), "s"(q + (rgrp * 8 + (w & 1) * 4) * 1024) : "memory");
  };
  auto rows_stat = [&]() {
    asm volatile("" : "+v"(xr[0]), "+v"(xr[1]), "+v"(xr[2]), "+v"(xr[3])::"memory");
    float e[4][8], mu[4], va[4];
#pragma unroll
    for (int i = 0; i < 4; ++i) {
#pragma unroll
      for (int j = 0; j < 4; ++j) { e[i][2 * j] = bflo(xr[i][j]); e[i][2 * j + 1] = bfhi(xr[i][j]); }
      mu[i] = fadd(fadd(fadd(e[i][0], e[i][1]), fadd(e[i][2], e[i][3])), fadd(fadd(e[i][4], e[i][5]), fadd(e[i][6], e[i][7])));
    }
    auto fold4 = [&](const float (&v)[4]) {
      const auto s0 = __builtin_amdgcn_permlane16_swap(__float_as_uint(v[0]), __float_as_uint(v[1]), false, false);
      const auto s1 = __builtin_amdgcn_permlane16_swap(__float_as_uint(v[2]), __float_as_uint(v[3]), false, false);
      const float p0 = fadd(__uint_as_float(s0[0]), __uint_as_float(s0[1])), p1 = fadd(__uint_as_float(s1[0]), __uint_as_float(s1[1]));
      const auto s2 = __builtin_amdgcn_permlane32_swap(__float_as_uint(p0), __float_as_uint(p1), false, false);
      float t = fadd(__uint_as_float(s2[0]), __uint_as_float(s2[1]));
      t = dpp_add<0xB1>(t); t = dpp_add<0x4E>(t); t = dpp_add<0x141>(t); t = dpp_add<0x140>(t);
      return t * (1.0f / 512.0f);
    };
    const float mg = fold4(mu);
#pragma unroll
    for (int i = 0; i < 4; ++i) {
      const float m = __int_as_float(__builtin_amdgcn_readlane(__float_as_int(mg), 16 * i));
      float s = 0.f;
#pragma unroll
      for (int j = 0; j < 8; ++j) { const float d = e[i][j] - m; s = ffma(d, d, s); }
      va[i] = s;
    }
    const float vg = fold4(va);
    const u2 o = {__float_as_uint(rbf(mg)), __float_as_uint(__builtin_amdgcn_rcpf(rbf(__builtin_amdgcn_sqrtf(rbf(rbf(vg) + 1e-5f)))))};
    if (l15 == 0) __builtin_amdgcn_raw_buffer_store_b64(o, wsr, WS_ROW + (rgrp * 8 + (w & 1) * 4 + g) * 8, 0, 16);
    asm volatile("" ::: "memory");
  };

  BAR();

  if (w < 4) {
    const int qq = l15 >> 2, pp = lane & 3;
    if (g1) {
      __builtin_amdgcn_s_setprio(2);
      const int wm = w >> 1, wn = w & 1;
      f4 acc[2][4][4];
#pragma unroll
      for (int s = 0; s < 2; ++s)
#pragma unroll
        for (int e = 0; e < 4; ++e)
#pragma unroll
          for (int i = 0; i < 4; ++i) acc[s][e][i] = (f4){0.f, 0.f, 0.f, 0.f};
      unsigned xa[2], wa[4];
      xa[0] = (wm * 64 + l15) * 128 + ((g ^ (l15 >> 1)) << 4); xa[1] = xa[0] ^ 64;
#pragma unroll
      for (int e = 0; e < 4; ++e) wa[e] = (8 * g + qq) * 256 + (((4 * wn + e) ^ (qq | ((g & 1) << 2))) << 5) + pp * 8;
      Frag f0, f1;
      s4 lo_;
      auto ldpart = [&](Frag& f, int ks, int j) {
        const unsigned o = 16384 + ks * 8192;
        if (j == 0) f.x[0] = lds128(xa[ks]);
        if (j >= 1 && j <= 16) {
          const int s = (j - 1) >> 3, e = ((j - 1) >> 1) & 3;
          if (j & 1) lo_ = __builtin_amdgcn_ds_read_tr16_b64_v4i16((LAS s4*)(size_t)(wa[e] + o + s * 16384));
          else {
            const s4 hi = __builtin_amdgcn_ds_read_tr16_b64_v4i16((LAS s4*)(size_t)(wa[e] + o + s * 16384 + 1024));
            f.w[s][e] = __builtin_bit_cast(bf8, __builtin_shufflevector(lo_, hi, 0, 1, 2, 3, 4, 5, 6, 7));
          }
        }
        if (j >= 17 && j < 20) f.x[j - 16] = lds128(xa[ks] + (j - 16) * 2048);
      };
      auto mm1 = [&](const Frag& f, int j) { acc[(j >> 2) & 1][j & 3][j >> 3] = mfma(f.w[(j >> 2) & 1][j & 3], f.x[j >> 3], acc[(j >> 2) & 1][j & 3][j >> 3]); };
      auto second = [&](int st) {
        const int d = (st == 2 || st == 5) ? -2 * (int)SLOT : (int)SLOT;
        const unsigned ca = C_LAND + 4 * st + 4;
        unsigned lv;
#pragma unroll
        for (int j = 0; j < 32; ++j) {
          if (j == 31) {
            unsigned sv;
            asm volatile("s_waitcnt lgkmcnt(0)\n\tv_readfirstlane_b32 %0, %1\n\ts_nop 1" : "=s"(sv) : "v"(lv) : "memory");
            if (sv < 2) ctl_wait(ca, 2);
            SB();
          }
          mm1(f0, j);
          if (j < 20) ldpart(f1, 1, j);
          if (j == 20) { xa[0] += d; OPAQUE(xa[0]); xa[1] += d; OPAQUE(xa[1]); }
          if (j == 21) { wa[0] += d; OPAQUE(wa[0]); wa[1] += d; OPAQUE(wa[1]); }
          if (j == 22) { wa[2] += d; OPAQUE(wa[2]); wa[3] += d; OPAQUE(wa[3]); }
          if (j == 25) { ctl_add(ca + (C_CONS - C_LAND - 4)); asm volatile("ds_read_b32 %0, %1" : "=v"(lv) : "v"(ca) : "memory"); }
          SB();
        }
      };
      OPAQUE(xa[0]); OPAQUE(xa[1]); OPAQUE(wa[0]); OPAQUE(wa[1]); OPAQUE(wa[2]); OPAQUE(wa[3]);
      ctl_wait(C_LAND, 2);
      STAMP(2);
#pragma unroll
      for (int j = 0; j < 20; ++j) ldpart(f0, 0, j);
      SB();
      second(0);
#pragma unroll 1
      for (int st = 1; st < 8; ++st) {
#pragma unroll
        for (int j = 0; j < 32; ++j) { mm1(f1, j); if (j < 20) ldpart(f0, 0, j); SB(); }
        second(st);
      }
      STAMP(16);
      constexpr float C0 = -2.0f * 0.7978845608028654f * 1.4426950408889634f, C1 = C0 * 0.044715f;
      f4 cs[2][4];
      f2 rs[4];
      int ln1 = __builtin_amdgcn_mbcnt_hi(-1, __builtin_amdgcn_mbcnt_lo(-1, 0));
      OPAQUE(ln1);
      const int lane = ln1, l15 = lane & 15, g = lane >> 4;
#pragma unroll
      for (int j = 0; j < 32; ++j) {
        mm1(f1, j);
        if (j < 8) cs[j >> 2][j & 3] = lds128f(L_CS + ((j >> 2) * 128 + wn * 64 + (j & 3) * 16 + 4 * g) * 4);
        if (j >= 8 && j < 12) rs[j - 8] = *(const LAS f2*)(size_t)(L_RS + (wm * 64 + (j - 8) * 16 + l15) * 8);
        SB();
      }
      STAMP(3);
      const unsigned hso = WS_H + ((pm * 8 + wm * 4) * 56 + pn * 4 + wn * 2) * 1024 + lane * 16;
#pragma unroll
      for (int i = 0; i < 4; ++i) {
        const float m = -rs[i][0], rr = rs[i][1];
        const float k0 = C0 * rr, k1 = C1 * rr * rr * rr, i2 = __builtin_amdgcn_rcpf(rr * rr);
        const f2 m2 = {m, m}, k02 = {k0, k0}, k12 = {k1, k1}, i22 = {i2, i2};
        f2 a[8], c[8], z[8];
        SB();
#pragma unroll
        for (int j = 0; j < 8; ++j) {
          const int e = j >> 1, h = (j & 1) * 2;
          a[j] = __builtin_elementwise_fma(m2, (f2){cs[0][e][h], cs[0][e][h + 1]}, (f2){acc[0][e][i][h], acc[0][e][i][h + 1]});
        }
        SB();
#pragma unroll
        for (int j = 0; j < 8; ++j) z[j] = a[j] * a[j];
        SB();
#pragma unroll
        for (int j = 0; j < 8; ++j) z[j] = __builtin_elementwise_fma(z[j], k12, k02);
        SB();
#pragma unroll
        for (int j = 0; j < 8; ++j) z[j] = z[j] * a[j];
        SB();
#pragma unroll
        for (int j = 0; j < 8; ++j) {
          const int e = j >> 1, h = (j & 1) * 2;
          z[j] = (f2){__builtin_amdgcn_exp2f(z[j][0]), __builtin_amdgcn_exp2f(z[j][1])};
          c[j] = __builtin_elementwise_fma(m2, (f2){cs[1][e][h], cs[1][e][h + 1]}, (f2){acc[1][e][i][h], acc[1][e][i][h + 1]});
          SB();
        }
#pragma unroll
        for (int j = 0; j < 8; ++j) z[j] = __builtin_elementwise_fma(z[j], i22, i22);
        SB();
#pragma unroll
        for (int j = 0; j < 8; ++j) {
          z[j] = (f2){__builtin_amdgcn_rcpf(z[j][0]), __builtin_amdgcn_rcpf(z[j][1])};
          c[j] = c[j] * a[j];
          SB();
        }
#pragma unroll
        for (int j = 0; j < 8; ++j) c[j] = c[j] * z[j];
        SB();
        u4 o0, o1;
#pragma unroll
        for (int j = 0; j < 4; ++j) { o0[j] = pk2(c[j][0], c[j][1]); o1[j] = pk2(c[4 + j][0], c[4 + j][1]); }
        __builtin_amdgcn_raw_buffer_store_b128(o0, wsr, hso + i * 56 * 1024, 0, 16);
        __builtin_amdgcn_raw_buffer_store_b128(o1, wsr, hso + i * 56 * 1024 + 1024, 0, 16);
      }
      SB();
      ctl_add(C_EPI);
      tag_get();
      __builtin_amdgcn_s_setprio(0);
      STAMP(4);
      WAITV(0);
      if (lane == 0 && __hip_atomic_fetch_add((LAS unsigned*)(size_t)C_HPUB, 1u, __ATOMIC_RELAXED, __HIP_MEMORY_SCOPE_WORKGROUP) == 3)
        flag_set(ws, WS_FH + (pm * 16 + pn) * 128, tag);
    }
    if (!g1) { ctl_wait(C_CVIN, 2); tag_get(); }
    STAMP(5);
    int ln2 = __builtin_amdgcn_mbcnt_hi(-1, __builtin_amdgcn_mbcnt_lo(-1, 0));
    OPAQUE(ln2);
    {
    const int lane = ln2, l15 = lane & 15, g = lane >> 4, qq = l15 >> 2, pp = lane & 3;
    const unsigned dv0 = (lane >> 3) * 1024 + (((lane & 7) ^ (g << 1)) * 16);
    f4 acc[4][4];
#pragma unroll
    for (int t = 0; t < 4; ++t)
#pragma unroll
      for (int i = 0; i < 4; ++i) acc[t][i] = (f4){0.f, 0.f, 0.f, 0.f};
    const unsigned da = gw * 12288 + (4 * g + qq) * 128 + (((2 * g + (qq >> 1)) & 3) << 5) + pp * 8;
    unsigned dlo[4];
#pragma unroll
    for (int t = 0; t < 4; ++t) { dlo[t] = da ^ (t * 32); OPAQUE(dlo[t]); }
    bf8 wf[4][4];
    auto ldw1 = [&](int i, int t) {
      const int s6 = i % 6;
      const unsigned a = dlo[t] + (s6 < 3 ? s6 * 4096 : SLOT + (s6 - 3) * 4096);
      wf[i & 3][t] = ldtr(a, a + 2048);
    };
    ctl_wait(C_WD + (gw >> 1) * 4, 1);
#pragma unroll
    for (int t = 0; t < 16; ++t) ldw1(t >> 2, t & 3);
    WAITL(0);
    wdstage(dv0, gw, 6); wdstage(dv0, gw, 7); wdstage(dv0, gw, 8); wdstage(dv0, gw, 9);
    STAMP(9);
    poll(ws, WS_FH + ((rt >> 1) * 16 + (l15 < 14 ? l15 : 0)) * 128, tag);
    STAMP(6);
    constexpr int HW = 5;
    u4 hf[HW][4];
    unsigned ho[4];
#pragma unroll
    for (int t = 0; t < 4; ++t) ho[t] = WS_H + ((rt * 4 + t) * 56 + gw * 14) * 1024 + lane * 16;
    auto ldh1 = [&](int i, int t) { hf[i % HW][t] = __builtin_amdgcn_raw_buffer_load_b128(wsr, ho[t] + (i & 3) * 1024, (i >> 2) * 4096, 16); };
#pragma unroll
    for (int i = 0; i < HW * 4; ++i) { ldh1(i >> 2, i & 3); SB(); }
#pragma unroll
    for (int i = 0; i < 14; ++i) {
#pragma unroll
      for (int ti = 0; ti < 4; ++ti) {
#pragma unroll
        for (int t = 0; t < 4; ++t) { acc[t][ti] = mfma(wf[i & 3][t], __builtin_bit_cast(bf8, hf[i % HW][ti]), acc[t][ti]); SB(); }
        if (i + HW < 14) { ldh1(i + HW, ti); SB(); }
      }
      if (i == 0) STAMP(19);
      if (i >= 1 && i <= 4) { WAITL(0); wdstage(dv0, gw, i + 9); }
      if (i + 4 < 14) {
        if (i == 6) WAITV(32); else if (i == 7) WAITV(28); else if (i == 8) WAITV(24); else if (i == 9) WAITV(16);
#pragma unroll
        for (int t = 0; t < 4; ++t) ldw1(i + 4, t);
      }
      SB();
    }
    STAMP(7);
    WAITL(0);
#pragma unroll
    for (int ti = 0; ti < 4; ++ti)
#pragma unroll
      for (int t = 0; t < 4; ++t) {
        const int tk = ti * 16 + l15;
        const unsigned pb = (tk < 32 ? 0u : SLOT) + gw * 12288 + (tk & 31) * 256;
        sts128f(pb + (((4 * t + g) ^ (tk & 7)) << 4), acc[t][ti]);
      }
    }
    STAMP(8);
  } else {
    auto bsync = [&](unsigned target) { ctl_add(C_BSYNC); ctl_wait(C_BSYNC, target); };
    unsigned dcr;
    if (w < 6) {
      unsigned tch[4];
      if (g1) {
        stage_half(1, 0); WAITV(12); ctl_add(C_LAND); STAMP(1);
        stage_half(1, 1); stage(2); WAITV(24); ctl_add(C_LAND + 4); STAMP(2);
#pragma unroll 1
        for (int st = 3; st < 8; ++st) {
          ctl_wait(C_CONS + 4 * (st - 3), 4);
          WAITV(0);
          ctl_add(C_LAND + 4 * (st - 1));
          stage(st);
        }
        WAITV(0);
        ctl_add(C_LAND + 28);
      }
      STAMP(3);
      {
        const unsigned o = (r * 448 + (w & 1) * 224 + lane) * 128;
        asm volatile("global_load_dword %0, %4, %6\n\tglobal_load_dword %1, %5, %6\n\tglobal_load_dword %2, %7, %6\n\tglobal_load_dword %3, %8, %6"
                     : "=&v"(tch[0]), "=&v"(tch[1]), "=&v"(tch[2]), "=&v"(tch[3])
                     : "v"(o), "v"(o + 8192), "s"(wd), "v"(o + 16384), "v"(o + (lane < 32 ? 24576 : 0)) : "memory");
      }
      asm volatile("global_load_dword %0, %1, %2" : "=&v"(dcr) : "v"(0u), "s"(sD + b * 4) : "memory");
      const unsigned dv0 = (lane >> 3) * 1024 + (((lane & 7) ^ (g << 1)) * 16);
      if (g1) ctl_wait(C_CONS + 24, 4);
#pragma unroll
      for (int i = 0; i < 6; ++i) wdstage(dv0, (w & 1) * 2 + (i & 1), i >> 1);
      if (g1) ctl_wait(C_CONS + 28, 4);
#pragma unroll
      for (int i = 0; i < 6; ++i) wdstage(dv0, (w & 1) * 2 + (i & 1), 3 + (i >> 1));
      asm volatile("s_waitcnt vmcnt(0)" : "+v"(tch[0]), "+v"(tch[1]), "+v"(tch[2]), "+v"(tch[3]), "+v"(dcr)::"memory");
      ctl_add(C_WD + (w & 1) * 4);
      STAMP(4);
    } else {
      const int sw_ = w & 1, t7 = tid & 127;
      const int cst = 2 * ((r + pnl) & 3) + ((xcd >> 1) & 1);
      const unsigned hoff = (2 * b + sw_) * 4096 + lane * 16;
      u4 wsh[8], hc[4], uc[4]; u2 hp[4];
      const int ptt = b >> 3, pct = b & 7, cp = lane & 31, tg = sw_ * 2 + (lane >> 5);
      unsigned kd[19], vd[16]; u4 shw;
      if (SIDE_DELAY && g1) ctl_wait(C_LAND, 2);
      {
        const char* wb = ((xcd >> 2) ? wu : wg) + (g1 ? pn * 256 + (cst * 64 + sw_ * 32) * 3584 : 0);
        const unsigned o = g * 3584 + l15 * 16;
        asm volatile("global_load_dwordx4 %0, %8, %12\n\tglobal_load_dwordx4 %1, %9, %12\n\tglobal_load_dwordx4 %2, %10, %12\n\tglobal_load_dwordx4 %3, %11, %12\n\t"
                     "global_load_dwordx4 %4, %8, %13\n\tglobal_load_dwordx4 %5, %9, %13\n\tglobal_load_dwordx4 %6, %10, %13\n\tglobal_load_dwordx4 %7, %11, %13"
                     : "=&v"(wsh[0]), "=&v"(wsh[1]), "=&v"(wsh[2]), "=&v"(wsh[3]), "=&v"(wsh[4]), "=&v"(wsh[5]), "=&v"(wsh[6]), "=&v"(wsh[7])
                     : "v"(o), "v"(o + 4 * 3584), "v"(o + 8 * 3584), "v"(o + 12 * 3584), "s"(wb), "s"(wb + 16 * 3584) : "memory");
        rows_load();
        const unsigned vo = tg * 16384 + cp * 4 + 4096, vo8 = vo + 8192;
        const unsigned vh = (ptt == 0 && tg == 0) ? vo - 1024 : vo - 4096;
        const char* kb = kk + ptt * 65536 + pct * 128;
        const char* vb = vv + ptt * 65536 + pct * 128;
#define LD8(d, o, vreg, sreg) asm volatile( \
          "global_load_dword %0, %8, %9 offset:-4096\n\tglobal_load_dword %1, %8, %9 offset:-3072\n\tglobal_load_dword %2, %8, %9 offset:-2048\n\t" \
          "global_load_dword %3, %8, %9 offset:-1024\n\tglobal_load_dword %4, %8, %9\n\tglobal_load_dword %5, %8, %9 offset:1024\n\t" \
          "global_load_dword %6, %8, %9 offset:2048\n\tglobal_load_dword %7, %8, %9 offset:3072" \
          : "=&v"(d[o]), "=&v"(d[o + 1]), "=&v"(d[o + 2]), "=&v"(d[o + 3]), "=&v"(d[o + 4]), "=&v"(d[o + 5]), "=&v"(d[o + 6]), "=&v"(d[o + 7]) \
          : "v"(vreg), "s"(sreg) : "memory")
        asm volatile("global_load_dword %0, %4, %5 offset:-3072\n\tglobal_load_dword %1, %4, %5 offset:-2048\n\tglobal_load_dword %2, %4, %5 offset:-1024\n\t"
                     "global_load_dwordx4 %3, %6, %7"
                     : "=&v"(kd[0]), "=&v"(kd[1]), "=&v"(kd[2]), "=&v"(shw) : "v"(vh), "s"(kb), "v"(cp * 16), "s"(shk + pct * 512) : "memory");
        LD8(kd, 3, vo, kb); LD8(vd, 0, vo, vb); LD8(kd, 11, vo8, kb); LD8(vd, 8, vo8, vb);
      }
      asm volatile("global_load_dwordx4 %0, %9, %11\n\tglobal_load_dwordx4 %1, %9, %11 offset:1024\n\tglobal_load_dwordx4 %2, %9, %11 offset:2048\n\t"
                   "global_load_dwordx4 %3, %9, %11 offset:3072\n\tglobal_load_dwordx2 %4, %10, %11\n\tglobal_load_dwordx2 %5, %9, %11 offset:1016\n\t"
                   "global_load_dwordx2 %6, %9, %11 offset:2040\n\tglobal_load_dwordx2 %7, %9, %11 offset:3064\n\tglobal_load_dword %8, %12, %13"
                   : "=&v"(hc[0]), "=&v"(hc[1]), "=&v"(hc[2]), "=&v"(hc[3]), "=&v"(hp[0]), "=&v"(hp[1]), "=&v"(hp[2]), "=&v"(hp[3]), "=&v"(dcr)
                   : "v"(hoff), "v"(lane ? hoff - 8 : hoff), "s"(ssk), "v"(0u), "s"(sD + b * 4) : "memory");
      tag_fetch();
      asm volatile("s_waitcnt vmcnt(45)" : "+v"(wsh[0]), "+v"(wsh[1]), "+v"(wsh[2]), "+v"(wsh[3]), "+v"(wsh[4]), "+v"(wsh[5]), "+v"(wsh[6]), "+v"(wsh[7])::"memory");
      STAMP(17);
      if (g1) {
        float cacc[8];
#pragma unroll
        for (int j = 0; j < 4; ++j) {
          cacc[2 * j] = fadd(fadd(fadd(bflo(wsh[0][j]), bflo(wsh[1][j])), fadd(bflo(wsh[2][j]), bflo(wsh[3][j]))),
                             fadd(fadd(bflo(wsh[4][j]), bflo(wsh[5][j])), fadd(bflo(wsh[6][j]), bflo(wsh[7][j]))));
          cacc[2 * j + 1] = fadd(fadd(fadd(bfhi(wsh[0][j]), bfhi(wsh[1][j])), fadd(bfhi(wsh[2][j]), bfhi(wsh[3][j]))),
                                 fadd(fadd(bfhi(wsh[4][j]), bfhi(wsh[5][j])), fadd(bfhi(wsh[6][j]), bfhi(wsh[7][j]))));
        }
        float c4[4], c2[2];
#pragma unroll
        for (int j = 0; j < 4; ++j) {
          const auto s2 = __builtin_amdgcn_permlane16_swap(__float_as_uint(cacc[j]), __float_as_uint(cacc[4 + j]), false, false);
          c4[j] = fadd(__uint_as_float(s2[0]), __uint_as_float(s2[1]));
        }
#pragma unroll
        for (int j = 0; j < 2; ++j) {
          const auto s2 = __builtin_amdgcn_permlane32_swap(__float_as_uint(c4[j]), __float_as_uint(c4[2 + j]), false, false);
          c2[j] = fadd(__uint_as_float(s2[0]), __uint_as_float(s2[1]));
        }
        __builtin_amdgcn_raw_buffer_store_b64((u2){__float_as_uint(c2[0]), __float_as_uint(c2[1])}, wsr,
                                              WS_COL + ((pn * 16 + cst * 2 + sw_) * 256 + (xcd >> 2) * 128 + l15 * 8 + (g & 1) * 4 + (g >> 1) * 2) * 4, 0, 16);
      }
      rows_stat();
      STAMP(18);
      asm volatile("s_waitcnt vmcnt(0)" : "+v"(kd[0]), "+v"(kd[1]), "+v"(kd[2]), "+v"(kd[3]), "+v"(kd[4]), "+v"(kd[5]), "+v"(kd[6]), "+v"(kd[7]), "+v"(kd[8]),
                   "+v"(kd[9]), "+v"(kd[10]), "+v"(kd[11]), "+v"(kd[12]), "+v"(kd[13]), "+v"(kd[14]), "+v"(kd[15]), "+v"(kd[16]), "+v"(kd[17]), "+v"(kd[18]),
                   "+v"(shw), "+v"(dcr)::"memory");
      asm volatile("" : "+v"(vd[0]), "+v"(vd[1]), "+v"(vd[2]), "+v"(vd[3]), "+v"(vd[4]), "+v"(vd[5]), "+v"(vd[6]), "+v"(vd[7]), "+v"(vd[8]), "+v"(vd[9]),
                   "+v"(vd[10]), "+v"(vd[11]), "+v"(vd[12]), "+v"(vd[13]), "+v"(vd[14]), "+v"(vd[15]),
                   "+v"(hc[0]), "+v"(hc[1]), "+v"(hc[2]), "+v"(hc[3]), "+v"(hp[0]), "+v"(hp[1]), "+v"(hp[2]), "+v"(hp[3])::"memory");
      ctl_add(C_WSYNC); ctl_wait(C_WSYNC, 2);
      if (tid == 384) flag_set(ws, WS_FS + rgrp * 128, tag);
      STAMP(20);
      {
        if (ptt == 0 && tg == 0) { kd[0] = 0; kd[1] = 0; kd[2] = 0; }
#pragma unroll
        for (int c = 0; c < 2; ++c) {
          const float w0 = bflo(shw[2 * c]), w1 = bfhi(shw[2 * c]), w2 = bflo(shw[2 * c + 1]), w3 = bfhi(shw[2 * c + 1]);
          float kf[19], u[16];
#pragma unroll
          for (int i = 0; i < 19; ++i) kf[i] = c ? bfhi(kd[i]) : bflo(kd[i]);
          SB();
#pragma unroll
          for (int i = 0; i < 16; ++i) u[i] = fmul(kf[i], w3);
          SB();
#pragma unroll
          for (int i = 0; i < 16; ++i) u[i] = ffma(kf[i + 1], w2, u[i]);
          SB();
#pragma unroll
          for (int i = 0; i < 16; ++i) u[i] = ffma(kf[i + 2], w1, u[i]);
          SB();
#pragma unroll
          for (int i = 0; i < 16; ++i) u[i] = ffma(kf[i + 3], w0, u[i]);
          SB();
#pragma unroll
          for (int i = 0; i < 16; ++i) u[i] = fmul(u[i], c ? bfhi(vd[i]) : bflo(vd[i]));
          SB();
          const unsigned ad = L_MISC + cp * 256 + c * 128;
          sts128(ad + (((6 - 2 * tg) ^ (cp & 7)) << 4), (u4){pk2(u[15], u[14]), pk2(u[13], u[12]), pk2(u[11], u[10]), pk2(u[9], u[8])});
          sts128(ad + (((7 - 2 * tg) ^ (cp & 7)) << 4), (u4){pk2(u[7], u[6]), pk2(u[5], u[4]), pk2(u[3], u[2]), pk2(u[1], u[0])});
        }
        ctl_add(C_WSYNC); ctl_wait(C_WSYNC, 4);
#pragma unroll
        for (int z = 0; z < 4; ++z) {
          const int id = t7 + 128 * z, c2 = id >> 3, ch = id & 7;
          const u4 t = lds128u(L_MISC + c2 * 128 + ((ch ^ ((c2 >> 1) & 7)) << 4));
          __builtin_amdgcn_raw_buffer_store_b128(t, wsr, WS_KVT + (pct * 64 + c2) * 4096 + 3968 - ptt * 128 + ch * 16, 0, 16);
        }
        STAMP(21);
        WAITV(0);
        ctl_add(C_WSYNC); ctl_wait(C_WSYNC, 6);
        if (tid == 384) flag_set(ws, WS_FK + (pct * 32 + ptt) * 128, tag);
        STAMP(19);
      }
      u4 c1[4];
      unsigned a0[4];
      if (lane == 0) { hp[0][0] = 0; hp[0][1] = 0; }
#pragma unroll
      for (int z = 0; z < 4; ++z) {
        a0[z] = __builtin_amdgcn_alignbit(hp[z][1], hp[z][0], 16);
        c1[z] = (u4){__builtin_amdgcn_alignbit(hc[z][0], hp[z][1], 16), __builtin_amdgcn_alignbit(hc[z][1], hc[z][0], 16),
                     __builtin_amdgcn_alignbit(hc[z][2], hc[z][1], 16), __builtin_amdgcn_alignbit(hc[z][3], hc[z][2], 16)};
      }
      if (g1) {
        const int j = l15;
        const int bc = (j >> 2) * 64 + (xcd & 1) * 32 + (j & 3) + 4 * pnl;
        poll(ws, WS_FS + (lane & 16 ? bc : pm * 16 + j) * 128, tag);
        STAMP(13);
        u2 rsv, csv[16];
        rsv = __builtin_amdgcn_raw_buffer_load_b64(wsr, WS_ROW + pm * 1024 + t7 * 8, 0, 16);
#pragma unroll
        for (int i = 0; i < 16; ++i) csv[i] = __builtin_amdgcn_raw_buffer_load_b64(wsr, WS_COL + pn * 16384 + t7 * 8 + (i & 3) * 1024, (i >> 2) * 4096, 16);
        *(LAS u2*)(size_t)(L_RS + t7 * 8) = rsv;
        float s0 = 0.f, s1 = 0.f;
#pragma unroll
        for (int i = 0; i < 16; ++i) { s0 = fadd(s0, __uint_as_float(csv[i][0])); s1 = fadd(s1, __uint_as_float(csv[i][1])); }
        *(LAS u2*)(size_t)(L_CS + t7 * 8) = (u2){__float_as_uint(s0), __float_as_uint(s1)};
        ctl_add(C_LAND + 32);
        STAMP(16);
      }
      poll(ws, WS_FK + ((b >> 5) * 32 + (lane & 31)) * 128, tag);
      STAMP(14);
#pragma unroll
      for (int z = 0; z < 4; ++z) uc[z] = __builtin_amdgcn_raw_buffer_load_b128(wsr, WS_KVT + hoff + z * 1024, 0, 16);
      if (b == 255 && w == 7) {
        poll(ws, WS_FS + lane * 128, tag); poll(ws, WS_FS + (64 + lane) * 128, tag);
        poll(ws, WS_FS + (128 + lane) * 128, tag); poll(ws, WS_FS + (192 + lane) * 128, tag);
        if (lane == 0) flag_set(ws, WS_EPOCH, tag);
      }
      if (g1) ctl_wait(C_CONS + 20, 4);
      const unsigned base = L_CV + sw_ * CV_CH;
#pragma unroll
      for (int z = 0; z < 4; ++z) {
        const unsigned p = base + 32 + lane * 16 + z * 1024;
        sts128(p, hc[z]);
        sts128(p + CV_COPY, c1[z]);
        sts128(p + 2 * CV_COPY, (u4){hp[z][1], hc[z][0], hc[z][1], hc[z][2]});
        sts128(p + 3 * CV_COPY, (u4){a0[z], c1[z][0], c1[z][1], c1[z][2]});
      }
      if (lane < 8) sts128(base + (lane >> 1) * CV_COPY + (lane & 1) * 16, (u4){0, 0, 0, 0});
      if (lane >= 32) sts128(base + CV_UR + 4096 + (lane - 32) * 16, (u4){0, 0, 0, 0});
#pragma unroll
      for (int z = 0; z < 4; ++z) sts128(base + CV_UR + lane * 16 + z * 1024, uc[z]);
      ctl_add(C_CVIN);
      STAMP(15);
    }
    u4 xq[2];
#pragma unroll
    for (int z = 0; z < 2; ++z) {
      const int id = t8 + 256 * z;
      xq[z] = *(const GAS u4*)(q + (rt * 64 + (id >> 3)) * 1024 + ct2 * 128 + (id & 7) * 16);
    }
    ctl_wait(C_CVIN, 2);
    tag_get();
    const int ch = gw >> 1, half = gw & 1;
    const unsigned base = L_CV + ch * CV_CH;
    const unsigned o = 8 * g + l15 + 1, kap = (0u - o) & 3;
    unsigned aa = base + kap * CV_COPY + 2 * (o + kap) + half * 256;
    unsigned ba = base + CV_UR + 2 * (2032 - 16 * l15 + 8 * g) - 3584 + half * 256;
    OPAQUE(aa); OPAQUE(ba);
    f4 y[8];
#pragma unroll
    for (int n = 0; n < 8; ++n) y[n] = (f4){0.f, 0.f, 0.f, 0.f};
    bf8 A[2][8], B[2][8];
    auto ldab = [&](int rho, int i) {
      const u2 lo = *(const volatile LAS u2*)(size_t)(aa + rho * 64 + i * 512), hi = *(const volatile LAS u2*)(size_t)(aa + rho * 64 + i * 512 + 8);
      A[rho & 1][i] = __builtin_bit_cast(bf8, (u4){lo[0], lo[1], hi[0], hi[1]});
      B[rho & 1][i] = lds128(ba + rho * 64 + (7 - i) * 512);
    };
#pragma unroll
    for (int i = 0; i < 8; ++i) ldab(0, i);
    SB();
    if (g1) ctl_wait(C_EPI, 4);
    STAMP(5);
    __builtin_amdgcn_s_setprio(1);
#pragma unroll
    for (int rho = 0; rho < 4; ++rho) {
      int cnt = 0;
#pragma unroll
      for (int lv = 0; lv < 8; ++lv)
#pragma unroll
        for (int qi = 0; qi <= lv; ++qi)
#pragma unroll
          for (int p = 0; p <= lv; ++p)
            if ((qi == lv || p == lv) && qi + p < 8) {
              y[qi + p] = mfma(A[rho & 1][qi], B[rho & 1][p], y[qi + p]);
              if (rho < 3 && (cnt & 3) == 0 && (cnt >> 2) < 8) ldab(rho + 1, cnt >> 2);
              ++cnt;
              SB();
            }
    }
    STAMP(6);
    const unsigned ex0 = gw < 3 ? L_MISC + gw * 4096 : L_CV + 2 * CV_CH, ex1 = (gw ^ 1) < 3 ? L_MISC + (gw ^ 1) * 4096 : L_CV + 2 * CV_CH;
#pragma unroll
    for (int n = 0; n < 4; ++n) {
      if (half) { const f4 t = y[n]; y[n] = y[4 + n]; y[4 + n] = t; }
      sts128f(ex0 + n * 1024 + lane * 16, y[4 + n]);
    }
    bsync(4);
    const float dc = ch ? bfhi(dcr) : bflo(dcr);
    u2 pk[4];
#pragma unroll
    for (int n = 0; n < 4; ++n) {
      const f4 t = lds128f(ex1 + n * 1024 + lane * 16);
      const int t0 = 256 * (4 * half + n) + 16 * l15 + 4 * g;
      const u2 uu = *(const LAS u2*)(size_t)(base + CV_UR + 2 * (2044 - t0));
      pk[n] = (u2){pk2(ffma(bfhi(uu[1]), dc, fadd(t[0], y[n][0])), ffma(bflo(uu[1]), dc, fadd(t[1], y[n][1]))),
                   pk2(ffma(bfhi(uu[0]), dc, fadd(t[2], y[n][2])), ffma(bflo(uu[0]), dc, fadd(t[3], y[n][3])))};
    }
#pragma unroll
    for (int n = 0; n < 4; n += 2) {
      const auto s0 = __builtin_amdgcn_permlane16_swap(pk[n][0], pk[n + 1][0], false, false);
      const auto s1 = __builtin_amdgcn_permlane16_swap(pk[n][1], pk[n + 1][1], false, false);
      const u4 o4 = {s0[0], s1[0], s0[1], s1[1]};
      __builtin_amdgcn_raw_buffer_store_b128(o4, wsr, WS_YT + (2 * b + ch) * 4096 + 2 * (256 * (4 * half + n + (g & 1)) + 16 * l15 + 8 * (g >> 1)), 0, 16);
    }
    STAMP(22);
    WAITV(0);
    bsync(8);
    if (tid == 256) flag_set(ws, WS_FY + b * 128, tag);
    STAMP(7);
    if (gw == 3) {
      poll(ws, lane < 32 ? WS_FY + (ct2 * 32 + lane) * 128 : WS_FS + (rt * 8 + (lane & 7)) * 128, tag);
      ctl_add(C_GO);
    } else ctl_wait(C_GO, 1);
    STAMP(8);
    u4 yq[2]; u2 st2[2];
#pragma unroll
    for (int z = 0; z < 2; ++z) {
      const int id = t8 + 256 * z;
      yq[z] = __builtin_amdgcn_raw_buffer_load_b128(wsr, WS_YT + (ct2 * 64 + (id >> 3)) * 4096 + rt * 128 + (id & 7) * 16, 0, 16);
      st2[z] = __builtin_amdgcn_raw_buffer_load_b64(wsr, WS_ROW + (rt * 64 + (id >> 3)) * 8, 0, 16);
    }
#pragma unroll
    for (int z = 0; z < 2; ++z) {
      const int id = t8 + 256 * z, c = id >> 3;
      sts128(L_YI + c * 128 + (((id & 7) ^ (c >> 3)) << 4), yq[z]);
    }
    STAMP(23);
    bsync(12);
#pragma unroll
    for (int z = 0; z < 2; ++z) {
      const int id = t8 + 256 * z, tk = id >> 3, c8 = id & 7;
      const float m = __uint_as_float(st2[z][0]), rr = __uint_as_float(st2[z][1]);
      const unsigned ya = L_YI + c8 * 1024 + (((tk >> 3) ^ c8) << 4) + (tk & 7) * 2;
      u4 so;
#pragma unroll
      for (int j = 0; j < 4; ++j) {
        const float y0 = __uint_as_float(((unsigned)*(const LAS unsigned short*)(size_t)(ya + (2 * j) * 128)) << 16);
        const float y1 = __uint_as_float(((unsigned)*(const LAS unsigned short*)(size_t)(ya + (2 * j + 1) * 128)) << 16);
        const unsigned d = pk2(fsub(bflo(xq[z][j]), m), fsub(bfhi(xq[z][j]), m));
        const unsigned xn = pk2(fmul(bflo(d), rr), fmul(bfhi(d), rr));
        so[j] = pk2(fmul(bflo(xn), y0), fmul(bfhi(xn), y1));
      }
      sts128(L_SI + id * 16, so);
      sts128(L_XI + id * 16, xq[z]);
    }
    __builtin_amdgcn_s_setprio(0);
    STAMP(9);
  }
  BAR();
  STAMP(10);
  {
    int ln3 = __builtin_amdgcn_mbcnt_hi(-1, __builtin_amdgcn_mbcnt_lo(-1, 0));
    OPAQUE(ln3);
    const int tid = w * 64 + ln3;
    const int tk = tid >> 3, c8 = tid & 7;
    const unsigned pb = (tk < 32 ? 0u : SLOT) + (tk & 31) * 256;
    f4 o0 = {0.f, 0.f, 0.f, 0.f}, o1 = {0.f, 0.f, 0.f, 0.f};
#pragma unroll
    for (int k4 = 0; k4 < 4; ++k4) {
      o0 += lds128f(pb + k4 * 12288 + (((2 * c8) ^ (tk & 7)) << 4));
      o1 += lds128f(pb + k4 * 12288 + (((2 * c8 + 1) ^ (tk & 7)) << 4));
    }
    const u4 sv = lds128u(L_SI + tid * 16), xv = lds128u(L_XI + tid * 16);
    const float ov[8] = {o0[0], o0[1], o0[2], o0[3], o1[0], o1[1], o1[2], o1[3]};
    u4 res;
#pragma unroll
    for (int j = 0; j < 4; ++j) {
      const unsigned ob = pk2(ov[2 * j], ov[2 * j + 1]);
      const float a0 = bflo(ob), a1 = bfhi(ob);
      const unsigned gb2 = pk2(a0 * __builtin_amdgcn_rcpf(1.0f + __builtin_amdgcn_exp2f(-1.4426950408889634f * a0)),
                               a1 * __builtin_amdgcn_rcpf(1.0f + __builtin_amdgcn_exp2f(-1.4426950408889634f * a1)));
      const unsigned sg = pk2(bflo(sv[j]) * bflo(gb2), bfhi(sv[j]) * bfhi(gb2));
      res[j] = pk2(bflo(xv[j]) + bflo(sg), bfhi(xv[j]) + bfhi(sg));
    }
    const __amdgpu_buffer_rsrc_t outr = __builtin_amdgcn_make_buffer_rsrc(out, 0, 2 << 20, 0x00020000);
    __builtin_amdgcn_raw_buffer_store_b128(res, outr, (rt * 64 + tk) * 1024 + ct2 * 128 + c8 * 16, 0, 2);
  }
  STAMP(11);
}
}

extern "C" void kernel_launch(void* const* d_in, const int* in_sizes, int n_in, void* d_out, int out_size, void* d_ws, size_t ws_size,
                              hipStream_t stream) {
  (void)in_sizes; (void)n_in; (void)out_size; (void)ws_size;
  (void)hipFuncSetAttribute((const void*)h3_geglu_fused, hipFuncAttributeMaxDynamicSharedMemorySize, LDS_BYTES);
  h3_geglu_fused<<<256, 512, LDS_BYTES, stream>>>((const char*)d_in[0], (const char*)d_in[1], (const char*)d_in[2], (const char*)d_in[3],
                                                  (const char*)d_in[4], (const char*)d_in[5], (const char*)d_in[7], (const char*)d_in[8],
                                                  (const char*)d_in[9], (char*)d_out, (char*)d_ws);
}
```

```cpp
#include <hip/hip_runtime.h>
#include <stdint.h>

#define LAS __attribute__((address_space(3)))
#define GAS __attribute__((address_space(1)))
#define DI __device__ __forceinline__
typedef __bf16 bf8 __attribute__((ext_vector_type(8)));
typedef __bf16 bf2 __attribute__((ext_vector_type(2)));
typedef short s4 __attribute__((ext_vector_type(4)));
typedef float f4 __attribute__((ext_vector_type(4)));
typedef float f2 __attribute__((ext_vector_type(2)));
typedef unsigned u4 __attribute__((ext_vector_type(4)));
typedef unsigned u2 __attribute__((ext_vector_type(2)));
typedef unsigned u2a __attribute__((ext_vector_type(2), aligned(4)));
typedef unsigned long long u64;

namespace {
constexpr unsigned WS_EPOCH = 0;
constexpr unsigned WS_FK = 4096;
constexpr unsigned WS_FS = WS_FK + 256 * 128;
constexpr unsigned WS_FH = WS_FS + 256 * 128;
constexpr unsigned WS_FY = WS_FH + 256 * 128;
constexpr unsigned WS_ROW = WS_FY + 256 * 128;
constexpr unsigned WS_COL = WS_ROW + 2048 * 8;
constexpr unsigned WS_KVT = 1u << 20;
constexpr unsigned WS_YT = 3u << 20;
constexpr unsigned WS_H = 5u << 20;
constexpr unsigned WS_PROF = 32u << 20;
constexpr unsigned SLOT = 49152;
constexpr unsigned L_MISC = 3 * SLOT;
constexpr unsigned L_RS = 159744;
constexpr unsigned L_CS = L_RS + 1024;
constexpr unsigned L_CTL = L_CS + 1024;
constexpr unsigned C_LAND = L_CTL, C_CONS = L_CTL + 40, C_BSYNC = L_CTL + 72, C_PRE = L_CTL + 76, C_WSYNC = L_CTL + 80, C_EPI = L_CTL + 84,
                   C_HPUB = L_CTL + 88, C_GO = L_CTL + 92, C_WD = L_CTL + 96, C_CVIN = L_CTL + 104, C_TAG = L_CTL + 108, C_PAIR = L_CTL + 112,
                   C_RD = L_CTL + 128, C_LD = L_CTL + 144, C_HOK = L_CTL + 160, L_TAG = L_CTL + 192;
constexpr unsigned CV_COPY = 4160, CV_UR = 2 * CV_COPY, CV_CH = CV_UR + 4608;
constexpr unsigned L_CV = L_RS - 2 * CV_CH;
static_assert(L_CV >= 131072, "W_down ring");
constexpr unsigned L_YI = L_CV;
constexpr unsigned LDS_BYTES = 163840;
constexpr int SPIN_CAP = 1 << 18;

DI float bflo(unsigned u) { return __uint_as_float(u << 16); }
DI float bfhi(unsigned u) { return __uint_as_float(u & 0xffff0000u); }
DI unsigned pk2(float a, float b) { f2 v = {a, b}; return __builtin_bit_cast(unsigned, __builtin_convertvector(v, bf2)); }
DI float dot2(unsigned a, bf2 b, float c) { return __builtin_amdgcn_fdot2_f32_bf16(__builtin_bit_cast(bf2, a), b, c, false); }
DI float rbf(float a) { return bflo(pk2(a, a)); }
DI f4 mfma(bf8 a, bf8 b, f4 c) { return __builtin_amdgcn_mfma_f32_16x16x32_bf16(a, b, c, 0, 0, 0); }
DI bf8 lds128(unsigned a) { return *(const LAS bf8*)(size_t)a; }
DI u4 lds128u(unsigned a) { return *(const LAS u4*)(size_t)a; }
DI f4 lds128f(unsigned a) { return *(const LAS f4*)(size_t)a; }
DI void sts128(unsigned a, u4 v) { *(LAS u4*)(size_t)a = v; }
DI void sts128f(unsigned a, f4 v) { *(LAS f4*)(size_t)a = v; }
DI bf8 ldtr(unsigned a0, unsigned a1) {
  s4 lo = __builtin_amdgcn_ds_read_tr16_b64_v4i16((LAS s4*)(size_t)a0);
  s4 hi = __builtin_amdgcn_ds_read_tr16_b64_v4i16((LAS s4*)(size_t)a1);
  return __builtin_bit_cast(bf8, __builtin_shufflevector(lo, hi, 0, 1, 2, 3, 4, 5, 6, 7));
}
#define WAITV(n) asm volatile("s_waitcnt vmcnt(%0)" ::"i"(n) : "memory")
#define WAITL(n) asm volatile("s_waitcnt lgkmcnt(%0)" ::"i"(n) : "memory")
#define SB() __builtin_amdgcn_sched_barrier(0)
#define OPAQUE(x) asm volatile("" : "+v"(x))
#define BAR() do { asm volatile("s_waitcnt lgkmcnt(0)" ::: "memory"); __builtin_amdgcn_s_barrier(); asm volatile("" ::: "memory"); } while (0)

DI void glds2(unsigned v0, unsigned v1, const char* sb, unsigned l) {
  asm volatile("s_mov_b32 m0, %3\n\ts_nop 0\n\tglobal_load_lds_dwordx4 %0, %2\n\tglobal_load_lds_dwordx4 %1, %2 offset:1024"
               ::"v"(v0), "v"(v1), "s"(sb), "s"(l) : "memory");
}
DI void glds4(unsigned v0, unsigned v1, unsigned v2, unsigned v3, const char* sb, unsigned l) {
  asm volatile("s_mov_b32 m0, %5\n\ts_nop 0\n\tglobal_load_lds_dwordx4 %0, %4\n\tglobal_load_lds_dwordx4 %1, %4 offset:1024\n\t"
               "global_load_lds_dwordx4 %2, %4 offset:2048\n\tglobal_load_lds_dwordx4 %3, %4 offset:3072"
               ::"v"(v0), "v"(v1), "v"(v2), "v"(v3), "s"(sb), "s"(l) : "memory");
}
DI void flag_set(char* ws, unsigned off, u64 tag) {
  __hip_atomic_store((GAS u64*)(ws + off), tag, __ATOMIC_RELAXED, __HIP_MEMORY_SCOPE_AGENT);
}
DI void poll(const char* ws, unsigned off, u64 tag) {
#pragma unroll 1
  for (int it = 0; it < SPIN_CAP; ++it) {
    u64 x = __hip_atomic_load((GAS u64*)(ws + off), __ATOMIC_RELAXED, __HIP_MEMORY_SCOPE_AGENT);
    if (__all(x == tag)) break;
    __builtin_amdgcn_s_sleep(4);
  }
  asm volatile("" ::: "memory");
}
DI float fadd(float a, float b) { float r; asm("v_add_f32 %0, %1, %2" : "=v"(r) : "v"(a), "v"(b)); return r; }
DI float fsub(float a, float b) { float r; asm("v_sub_f32 %0, %1, %2" : "=v"(r) : "v"(a), "v"(b)); return r; }
DI float fmul(float a, float b) { float r; asm("v_mul_f32 %0, %1, %2" : "=v"(r) : "v"(a), "v"(b)); return r; }
DI float ffma(float a, float b, float c) { float r; asm("v_fma_f32 %0, %1, %2, %3" : "=v"(r) : "v"(a), "v"(b), "v"(c)); return r; }
DI void poll2(const char* ws, unsigned off0, unsigned off1, u64 tag) {
#pragma unroll 1
  for (int it = 0; it < SPIN_CAP; ++it) {
    const u64 x = __hip_atomic_load((GAS u64*)(ws + off0), __ATOMIC_RELAXED, __HIP_MEMORY_SCOPE_AGENT);
    const u64 y = __hip_atomic_load((GAS u64*)(ws + off1), __ATOMIC_RELAXED, __HIP_MEMORY_SCOPE_AGENT);
    if (__all(x == tag && y == tag)) break;
    __builtin_amdgcn_s_sleep(8);
  }
  asm volatile("" ::: "memory");
}
template <int CTRL> DI float dpp_add(float v) {
  return v + __int_as_float(__builtin_amdgcn_update_dpp(0, __float_as_int(v), CTRL, 0xf, 0xf, true));
}
DI float wave_sum(float v) {
  v = dpp_add<0xB1>(v); v = dpp_add<0x4E>(v); v = dpp_add<0x141>(v); v = dpp_add<0x140>(v);
  const int i = __float_as_int(v);
  return (__int_as_float(__builtin_amdgcn_readlane(i, 0)) + __int_as_float(__builtin_amdgcn_readlane(i, 16))) +
         (__int_as_float(__builtin_amdgcn_readlane(i, 32)) + __int_as_float(__builtin_amdgcn_readlane(i, 48)));
}
DI void ctl_add(unsigned addr) {
  asm volatile("s_waitcnt lgkmcnt(0)\n\ts_mov_b64 exec, 1\n\tds_add_u32 %0, %1\n\ts_mov_b64 exec, -1" ::"v"(addr), "v"(1u) : "memory");
}
DI unsigned ctl_get(unsigned addr) {
  unsigned s, t;
  asm volatile("ds_read_b32 %1, %2\n\ts_waitcnt lgkmcnt(0)\n\tv_readfirstlane_b32 %0, %1\n\ts_nop 1" : "=s"(s), "=&v"(t) : "v"(addr) : "memory");
  return s;
}
DI void ctl_spin(unsigned addr, unsigned target) {
#pragma unroll 1
  for (int it = 0; it < SPIN_CAP; ++it)
    if (ctl_get(addr) >= target) break;
}
DI void ctl_wait(unsigned addr, unsigned target) {
#pragma unroll 1
  for (int it = 0; it < SPIN_CAP; ++it) {
    if (ctl_get(addr) >= target) break;
    __builtin_amdgcn_s_sleep(1);
  }
}
#define STAMP(i)
#define CNOW(v)
#define CSTAMP(i)

#define HWIN 4
#define WREG 6
#define SFIRST 0
#define PEEK_AT 5
#define SIDE_SLEEP 0
#define PF_WAVES 4
#define S1_EARLY 1
#define BAR_AT 32
#define KBAR() do { if (g1) BAR(); } while (0)
struct Frag { bf8 x[4]; bf8 w[2][4]; };

__global__ __launch_bounds__(512, 2) void h3_geglu_fused(const char* q, const char* kk, const char* vv, const char* shk, const char* ssk,
                                                         const char* sD, const char* wg, const char* wu, const char* wd, char* out, char* ws) {
  extern __shared__ __attribute__((aligned(16))) char smem[];
  const int lane = threadIdx.x & 63, w = __builtin_amdgcn_readfirstlane(threadIdx.x >> 6) ^ (SFIRST ? 4 : 0), gw = w & 3;
  const int tid = w * 64 + lane, t8 = tid & 255;
  const int b = blockIdx.x, xcd = b & 7, r = b >> 3;
  const bool g1 = r < 28;
  const int l15 = lane & 15, g = lane >> 4;
  const __amdgpu_buffer_rsrc_t wsr = __builtin_amdgcn_make_buffer_rsrc(ws, 0, 16 << 20, 0x00020000);
  const int pnl = r >> 2;
  const int pm = (xcd >> 1) * 4 + (r & 3), pn = (xcd & 1) * 7 + pnl;
  const int rt = xcd * 4 + (r & 3), ct2 = r >> 2;
  auto wslot = [](int i) -> unsigned { const int s = i & 7; return s < 2 ? 2 * SLOT + s * 4096 : s < 5 ? (s - 2) * 4096 : SLOT + (s - 5) * 4096; };
  auto wdstage = [&](unsigned dv0, int kq, int i) {
    glds4(dv0, dv0 + 7168, dv0 + 2 * 7168, dv0 + 3 * 7168, wd + ct2 * 128 + (kq * 14 + i) * 32768, wslot(i) + kq * ((i & 7) < 2 ? 8192 : 12288));
  };
  u64 tag;
  auto tag_get = [&]() {
    const u2 t = *(const volatile LAS u2*)(size_t)L_TAG;
    tag = ((u64)(unsigned)__builtin_amdgcn_readfirstlane(t[1]) << 32) | (unsigned)__builtin_amdgcn_readfirstlane(t[0]);
  };

  u4 xq; u2 st2;
  auto tail_load = [&]() {
    int ln = __builtin_amdgcn_mbcnt_hi(-1, __builtin_amdgcn_mbcnt_lo(-1, 0));
    OPAQUE(ln);
    const int tk = w * 8 + (ln >> 3), c8 = ln & 7;
    xq = *(const GAS u4*)(q + (rt * 64 + tk) * 1024 + ct2 * 128 + c8 * 16);
    st2 = __builtin_amdgcn_raw_buffer_load_b64(wsr, WS_ROW + (rt * 64 + tk) * 8, 0, 16);
  };

  if (w < 4) {
    {
      const int ptt = b >> 3, pct = b & 7;
      const unsigned lk = 2 * SLOT + gw * 5120;
      const unsigned kvo = (lane >> 3) * 1024 + (((lane & 7) ^ ((g & 1) << 2)) << 4);
      const bool top = ptt == 0 && gw == 0;
      const char* kb = kk + (ptt * 64 + gw * 16) * 1024 + pct * 128;
      u2 sw;
      asm volatile("s_mov_b32 m0, %7\n\ts_nop 0\n\tglobal_load_lds_dwordx4 %1, %4\n\tglobal_load_lds_dwordx4 %1, %5 offset:1024\n\t"
                   "global_load_lds_dwordx4 %2, %5 offset:2048\n\tglobal_load_lds_dwordx4 %1, %6 offset:3072\n\ts_add_u32 m0, m0, 4096\n\t"
                   "global_load_dwordx2 %0, %3, %8\n\tglobal_load_lds_dwordx4 %2, %9"
                   : "=&v"(sw)
                   : "v"(kvo), "v"(kvo + 7168), "v"(lane * 8), "s"(top ? kb : kb - 8192), "s"(kb - 1024), "s"(vv + (ptt * 64 + gw * 16) * 1024 + pct * 128 - 3072),
                     "s"(lk), "s"(shk + pct * 512), "s"(vv + (ptt * 64 + gw * 16) * 1024 + pct * 128 + 1024)
                   : "memory", "scc");
      const int qq = l15 >> 2, pp = lane & 3;
      unsigned ra = lk + qq * 128 + (((2 * g + (pp >> 1)) ^ ((qq >> 1) << 2)) << 4) + (pp & 1) * 8;
      OPAQUE(ra);
      asm volatile("s_waitcnt vmcnt(0)" : "+v"(sw)::"memory");
      STAMP(18);
      u2 kr[5], vr[4];
#pragma unroll
      for (int i = 0; i < 5; ++i) kr[i] = __builtin_bit_cast(u2, __builtin_amdgcn_ds_read_tr16_b64_v4i16((LAS s4*)(size_t)(ra + 512 + i * 512)));
#pragma unroll
      for (int i = 0; i < 4; ++i) vr[i] = __builtin_bit_cast(u2, __builtin_amdgcn_ds_read_tr16_b64_v4i16((LAS s4*)(size_t)(ra + 3072 + i * 512)));
      if (top) kr[0] = (u2){0, 0};
      const float w0 = bflo(sw[0]), w1 = bfhi(sw[0]), w2 = bflo(sw[1]), w3 = bfhi(sw[1]);
      float kf[20], u[16];
#pragma unroll
      for (int i = 0; i < 5; ++i) { kf[4 * i] = bflo(kr[i][0]); kf[4 * i + 1] = bfhi(kr[i][0]); kf[4 * i + 2] = bflo(kr[i][1]); kf[4 * i + 3] = bfhi(kr[i][1]); }
      SB();
#pragma unroll
      for (int i = 0; i < 16; ++i) u[i] = fmul(kf[i + 1], w3);
      SB();
#pragma unroll
      for (int i = 0; i < 16; ++i) u[i] = ffma(kf[i + 2], w2, u[i]);
      SB();
#pragma unroll
      for (int i = 0; i < 16; ++i) u[i] = ffma(kf[i + 3], w1, u[i]);
      SB();
#pragma unroll
      for (int i = 0; i < 16; ++i) u[i] = ffma(kf[i + 4], w0, u[i]);
      SB();
#pragma unroll
      for (int i = 0; i < 16; ++i) { const unsigned vw = vr[i >> 2][(i >> 1) & 1]; u[i] = fmul(u[i], (i & 1) ? bfhi(vw) : bflo(vw)); }
      SB();
      const unsigned ad = L_MISC + lane * 128;
      sts128(ad + (((7 - 2 * gw) ^ (lane & 7)) << 4), (u4){pk2(u[7], u[6]), pk2(u[5], u[4]), pk2(u[3], u[2]), pk2(u[1], u[0])});
      sts128(ad + (((6 - 2 * gw) ^ (lane & 7)) << 4), (u4){pk2(u[15], u[14]), pk2(u[13], u[12]), pk2(u[11], u[10]), pk2(u[9], u[8])});
      STAMP(1);
      BAR();
    }
    if (g1) {
      const int qq = l15 >> 2, pp = lane & 3;
      const int wm = w >> 1, wn = w & 1;
      f4 acc[2][4][4];
#pragma unroll
      for (int s = 0; s < 2; ++s)
#pragma unroll
        for (int e = 0; e < 4; ++e)
#pragma unroll
          for (int i = 0; i < 4; ++i) acc[s][e][i] = (f4){0.f, 0.f, 0.f, 0.f};
      unsigned xa[2], wa[4];
      xa[0] = (wm * 64 + l15) * 128 + ((g ^ (l15 >> 1)) << 4); xa[1] = xa[0] ^ 64;
#pragma unroll
      for (int e = 0; e < 4; ++e) wa[e] = (8 * g + qq) * 256 + (((4 * wn + e) ^ (qq | ((g & 1) << 2))) << 5) + pp * 8;
      Frag f0, f1;
      s4 lo_;
      auto ldpart = [&](Frag& f, int ks, int j) {
        const unsigned o = 16384 + ks * 8192;
        if (j == 0) f.x[0] = lds128(xa[ks]);
        if (j >= 1 && j <= 16) {
          const int s = (j - 1) >> 3, e = ((j - 1) >> 1) & 3;
          if (j & 1) lo_ = __builtin_amdgcn_ds_read_tr16_b64_v4i16((LAS s4*)(size_t)(wa[e] + o + s * 16384));
          else {
            const s4 hi = __builtin_amdgcn_ds_read_tr16_b64_v4i16((LAS s4*)(size_t)(wa[e] + o + s * 16384 + 1024));
            f.w[s][e] = __builtin_bit_cast(bf8, __builtin_shufflevector(lo_, hi, 0, 1, 2, 3, 4, 5, 6, 7));
          }
        }
        if (j >= 17 && j < 20) f.x[j - 16] = lds128(xa[ks] + (j - 16) * 2048);
      };
      auto mm1 = [&](const Frag& f, int j) { acc[(j >> 2) & 1][j & 3][j >> 3] = mfma(f.w[(j >> 2) & 1][j & 3], f.x[j >> 3], acc[(j >> 2) & 1][j & 3][j >> 3]); };
      auto second = [&](int st) {
        const int d = (st == 2 || st == 5) ? -2 * (int)SLOT : (int)SLOT;
#pragma unroll
        for (int j = 0; j < 32; ++j) {
          if (j == BAR_AT) { BAR(); SB(); }
          mm1(f0, j);
          if (j < 20) ldpart(f1, 1, j);
          if (j == 20) { xa[0] += d; OPAQUE(xa[0]); xa[1] += d; OPAQUE(xa[1]); }
          if (j == 21) { wa[0] += d; OPAQUE(wa[0]); wa[1] += d; OPAQUE(wa[1]); }
          if (j == 22) { wa[2] += d; OPAQUE(wa[2]); wa[3] += d; OPAQUE(wa[3]); }
          SB();
        }
        if (BAR_AT == 32) { BAR(); SB(); }
      };
      OPAQUE(xa[0]); OPAQUE(xa[1]); OPAQUE(wa[0]); OPAQUE(wa[1]); OPAQUE(wa[2]); OPAQUE(wa[3]);
      STAMP(2); CSTAMP(20);
#pragma unroll
      for (int j = 0; j < 20; ++j) ldpart(f0, 0, j);
      SB();
      second(0);
#pragma unroll 1
      for (int st = 1; st < 8; ++st) {
#pragma unroll
        for (int j = 0; j < 32; ++j) { mm1(f1, j); if (j < 20) ldpart(f0, 0, j); SB(); }
        second(st);
      }
      STAMP(16); CSTAMP(21);
      constexpr float C0 = -2.0f * 0.7978845608028654f * 1.4426950408889634f, C1 = C0 * 0.044715f;
      f4 cs[2][4];
      f2 rs[4];
      int ln1 = __builtin_amdgcn_mbcnt_hi(-1, __builtin_amdgcn_mbcnt_lo(-1, 0));
      OPAQUE(ln1);
      const int lane = ln1, l15 = lane & 15, g = lane >> 4;
#pragma unroll
      for (int j = 0; j < 32; ++j) {
        mm1(f1, j);
        if (j < 8) cs[j >> 2][j & 3] = lds128f(L_CS + ((j >> 2) * 128 + wn * 64 + (j & 3) * 16 + 4 * g) * 4);
        if (j >= 8 && j < 12) rs[j - 8] = *(const LAS f2*)(size_t)(L_RS + (wm * 64 + (j - 8) * 16 + l15) * 8);
        SB();
      }
      STAMP(3);
      const unsigned hso = WS_H + ((pm * 8 + wm * 4) * 56 + pn * 4 + wn * 2) * 1024 + lane * 16;
#pragma unroll
      for (int i = 0; i < 4; ++i) {
        const float m = -rs[i][0], rr = rs[i][1];
        const float k0 = C0 * rr, k1 = C1 * rr * rr * rr, i2 = __builtin_amdgcn_rcpf(rr * rr);
        const f2 m2 = {m, m}, k02 = {k0, k0}, k12 = {k1, k1}, i22 = {i2, i2};
        f2 a[8], c[8], z[8];
        SB();
#pragma unroll
        for (int j = 0; j < 8; ++j) {
          const int e = j >> 1, h = (j & 1) * 2;
          a[j] = __builtin_elementwise_fma(m2, (f2){cs[0][e][h], cs[0][e][h + 1]}, (f2){acc[0][e][i][h], acc[0][e][i][h + 1]});
        }
        SB();
#pragma unroll
        for (int j = 0; j < 8; ++j) z[j] = a[j] * a[j];
        SB();
#pragma unroll
        for (int j = 0; j < 8; ++j) z[j] = __builtin_elementwise_fma(z[j], k12, k02);
        SB();
#pragma unroll
        for (int j = 0; j < 8; ++j) z[j] = z[j] * a[j];
        SB();
#pragma unroll
        for (int j = 0; j < 8; ++j) {
          const int e = j >> 1, h = (j & 1) * 2;
          z[j] = (f2){__builtin_amdgcn_exp2f(z[j][0]), __builtin_amdgcn_exp2f(z[j][1])};
          c[j] = __builtin_elementwise_fma(m2, (f2){cs[1][e][h], cs[1][e][h + 1]}, (f2){acc[1][e][i][h], acc[1][e][i][h + 1]});
          SB();
        }
#pragma unroll
        for (int j = 0; j < 8; ++j) z[j] = __builtin_elementwise_fma(z[j], i22, i22);
        SB();
#pragma unroll
        for (int j = 0; j < 8; ++j) {
          z[j] = (f2){__builtin_amdgcn_rcpf(z[j][0]), __builtin_amdgcn_rcpf(z[j][1])};
          c[j] = c[j] * a[j];
          SB();
        }
#pragma unroll
        for (int j = 0; j < 8; ++j) c[j] = c[j] * z[j];
        SB();
        u4 o0, o1;
#pragma unroll
        for (int j = 0; j < 4; ++j) { o0[j] = pk2(c[j][0], c[j][1]); o1[j] = pk2(c[4 + j][0], c[4 + j][1]); }
        __builtin_amdgcn_raw_buffer_store_b128(o0, wsr, hso + i * 56 * 1024, 0, 16);
        __builtin_amdgcn_raw_buffer_store_b128(o1, wsr, hso + i * 56 * 1024 + 1024, 0, 16);
      }
      SB();
      ctl_add(C_EPI);
      tag_get();
      STAMP(4);
    } else if (w < PF_WAVES) {
#pragma unroll 1
      for (int j = (r - 28) * PF_WAVES + w; j < 224; j += 4 * PF_WAVES) {
        if (ctl_get(C_TAG) >= 2) break;
        const unsigned o = ((((j >> 2) & 3) * 14 + (j >> 4)) * 32 + (j & 3) * 8 + (lane >> 3)) * 1024 + (lane & 7) * 128;
        asm volatile("s_waitcnt vmcnt(1)\n\ts_mov_b32 m0, %2\n\ts_nop 0\n\tglobal_load_lds_dword %0, %1" ::"v"(o), "s"(wd), "s"(131072 + w * 256) : "memory");
      }
      WAITV(0);
    }
    int ln2 = __builtin_amdgcn_mbcnt_hi(-1, __builtin_amdgcn_mbcnt_lo(-1, 0));
    OPAQUE(ln2);
    {
      const int lane = ln2, l15 = lane & 15, g = lane >> 4, qq = l15 >> 2, pp = lane & 3;
      const unsigned dv0 = (lane >> 3) * 1024 + (((lane & 7) ^ (g << 1)) * 16);
      f4 acc[4][4];
#pragma unroll
      for (int t = 0; t < 4; ++t)
#pragma unroll
        for (int i = 0; i < 4; ++i) acc[t][i] = (f4){0.f, 0.f, 0.f, 0.f};
      const unsigned da = (4 * g + qq) * 128 + (((2 * g + (qq >> 1)) & 3) << 5) + pp * 8;
      unsigned dlo[4];
#pragma unroll
      for (int t = 0; t < 4; ++t) { dlo[t] = (da ^ (t * 32)) + gw * 12288; OPAQUE(dlo[t]); }
      const unsigned dh = 2 * SLOT - gw * 4096;
      constexpr int WR = 6, HW = HWIN;
      bf8 wf[WR][4];
      auto waddr = [&](int i, int t) -> unsigned {
        if ((i & 7) >= 2) return dlo[t] + wslot(i);
        unsigned d_ = dh;
        asm volatile("" : "+s"(d_));
        unsigned a_ = dlo[t] + d_;
        OPAQUE(a_);
        return a_ + (i & 1) * 4096;
      };
      __builtin_amdgcn_s_setprio(2);
      if (g1) {
        WAITV(0);
        if (lane == 0 && __hip_atomic_fetch_add((LAS unsigned*)(size_t)C_HPUB, 1u, __ATOMIC_RELAXED, __HIP_MEMORY_SCOPE_WORKGROUP) == 3)
          flag_set(ws, WS_FH + (pm * 16 + pn) * 128, tag);
      }
      STAMP(5);
      ctl_wait(C_WD + (gw >> 1) * 4, 2);
      if (!g1) tag_get();
#pragma unroll
      for (int t = 0; t < WR * 4; ++t) { const unsigned a_ = waddr(t >> 2, t & 3); wf[t >> 2][t & 3] = ldtr(a_, a_ + 2048); }
      WAITL(0);
      STAMP(17);
#pragma unroll
      for (int i = 8; i < 14; ++i) wdstage(dv0, gw, i);
      __builtin_amdgcn_s_setprio(0);
      STAMP(9);
      poll(ws, WS_FH + ((rt >> 1) * 16 + ((gw * 7) >> 1) + (lane & 3)) * 128, tag);
      STAMP(6);
      u4 hf[HW][4];
      unsigned ho[4];
#pragma unroll
      for (int t = 0; t < 4; ++t) ho[t] = WS_H + ((rt * 4 + t) * 56 + gw * 14) * 1024 + lane * 16;
      auto ldh1 = [&](int i, int t) { hf[i % HW][t] = __builtin_amdgcn_raw_buffer_load_b128(wsr, ho[t] + (i & 3) * 1024, (i >> 2) * 4096, 16); };
#pragma unroll
      for (int i = 0; i < HW * 4; ++i) { ldh1(i >> 2, i & 3); SB(); }
      s4 lo_;
      unsigned wa_;
#pragma unroll
      for (int i = 0; i < 14; ++i) {
        const int nw = i + WR - 1;
        if (i == 11) { tail_load(); SB(); }
#pragma unroll
        for (int j = 0; j < 16; ++j) {
          const int ti = j >> 2, t = j & 3;
          acc[t][ti] = mfma(wf[i % WR][t], __builtin_bit_cast(bf8, hf[i % HW][ti]), acc[t][ti]);
          if (i >= 1 && nw < 14 && t != 3 && (j - ti) < 8) {
            const int e = j - ti;
            if (!(e & 1)) wa_ = waddr(nw, e >> 1);
            const s4 v_ = __builtin_amdgcn_ds_read_tr16_b64_v4i16((LAS s4*)(size_t)(wa_ + (e & 1) * 2048));
            if (e & 1) wf[nw % WR][e >> 1] = __builtin_bit_cast(bf8, __builtin_shufflevector(lo_, v_, 0, 1, 2, 3, 4, 5, 6, 7)); else lo_ = v_;
          }
          if (t == 3 && i + HW < 14) ldh1(i + HW, ti);
          SB();
        }
        if (i == 0) STAMP(19);
      }
      STAMP(7);
#pragma unroll
      for (int ti = 0; ti < 4; ++ti)
#pragma unroll
        for (int t = 0; t < 4; ++t)
          sts128f((ti >> 1) * SLOT + gw * 12288 + ((ti & 1) * 16 + l15) * 256 + (((4 * t + g) ^ (l15 & 7)) << 4), acc[t][ti]);
    }
    STAMP(8);
  } else {
    const int t7 = tid & 127, bw = w & 1;
    unsigned dcr;
    if (w < 6) {
      unsigned xv[4], wv[4];
#pragma unroll
      for (int p = 0; p < 4; ++p) {
        xv[p] = (p * 8 + (lane >> 3)) * 1024 + (((lane & 7) ^ (((p & 1) << 2) | g)) << 4) - p * 1024;
        wv[p] = (p * 4 + g) * 3584 + ((l15 ^ ((g | ((p >> 1) << 2)) << 1)) << 4) - p * 1024;
      }
      auto quarter = [&](int st, int qt) {
        const unsigned l = (st % 3) * SLOT + qt * 4096;
        glds4(xv[0], xv[1], xv[2], xv[3], q + pm * 131072 + qt * 32768 + st * 128, l);
        glds4(wv[0], wv[1], wv[2], wv[3], wg + pn * 256 + (st * 64 + qt * 16) * 3584, l + 16384);
        glds4(wv[0], wv[1], wv[2], wv[3], wu + pn * 256 + (st * 64 + qt * 16) * 3584, l + 32768);
      };
      auto stage = [&](int st) { quarter(st, 2 * bw); quarter(st, 2 * bw + 1); };
      const unsigned dv0 = (lane >> 3) * 1024 + (((lane & 7) ^ (g << 1)) * 16);
      if (g1) { stage(0); if (S1_EARLY) { quarter(1, 2 * bw); WAITV(12); } else WAITV(0); }
      STAMP(1);
      BAR();
      if (g1) { if (!S1_EARLY) quarter(1, 2 * bw); quarter(1, 2 * bw + 1); stage(2); WAITV(24); }
      KBAR();
#pragma unroll 1
      for (int st = 3; st < 8; ++st) {
        if (g1) { stage(st); WAITV(24); }
        KBAR();
      }
      STAMP(13);
      if (!g1) ctl_wait(C_TAG, 2);
      asm volatile("global_load_dword %0, %1, %2" : "=&v"(dcr) : "v"(0u), "s"(sD + b * 4) : "memory");
#pragma unroll
      for (int i = 0; i < 4; ++i) wdstage(dv0, bw * 2 + (i & 1), i >> 1);
      WAITV(17);
      KBAR();
      STAMP(3);
#pragma unroll
      for (int i = 0; i < 6; ++i) wdstage(dv0, bw * 2 + (i & 1), 2 + (i >> 1));
      KBAR();
      STAMP(19);
      tag_get();
      asm volatile("s_waitcnt vmcnt(32)" : "+v"(dcr)::"memory");
#pragma unroll
      for (int i = 0; i < 6; ++i) wdstage(dv0, bw * 2 + (i & 1), 5 + (i >> 1));
      WAITV(24);
      ctl_add(C_WD + bw * 4);
      STAMP(2);
      WAITV(0);
      ctl_add(C_WD + bw * 4);
      STAMP(4);
    } else {
      if (t7 < 48) *(LAS unsigned*)(size_t)(L_CTL + t7 * 4) = 0;
      const int ptt = b >> 3, pct = b & 7;
      const int rgrp = (xcd >> 1) * 64 + (xcd & 1) * 32 + r;
      const int cst = 2 * ((r + pnl) & 3) + ((xcd >> 1) & 1);
      const unsigned hoff = (2 * b + bw) * 4096 + lane * 16;
      u4 xr[4];
      auto rows_load = [&]() {
        asm volatile("global_load_dwordx4 %0, %4, %5\n\tglobal_load_dwordx4 %1, %4, %5 offset:1024\n\tglobal_load_dwordx4 %2, %4, %5 offset:2048\n\t"
                     "global_load_dwordx4 %3, %4, %5 offset:3072"
                     : "=&v"(xr[0]), "=&v"(xr[1]), "=&v"(xr[2]), "=&v"(xr[3]) : "v"(lane * 16), "s"(q + (rgrp * 8 + (w & 1) * 4) * 1024) : "memory");
      };
      auto rows_stat = [&]() {
        asm volatile("" : "+v"(xr[0]), "+v"(xr[1]), "+v"(xr[2]), "+v"(xr[3])::"memory");
        float e[4][8], mu[4], va[4];
#pragma unroll
        for (int i = 0; i < 4; ++i) {
#pragma unroll
          for (int j = 0; j < 4; ++j) { e[i][2 * j] = bflo(xr[i][j]); e[i][2 * j + 1] = bfhi(xr[i][j]); }
          mu[i] = fadd(fadd(fadd(e[i][0], e[i][1]), fadd(e[i][2], e[i][3])), fadd(fadd(e[i][4], e[i][5]), fadd(e[i][6], e[i][7])));
        }
        auto fold4 = [&](const float (&v)[4]) {
          const auto s0 = __builtin_amdgcn_permlane16_swap(__float_as_uint(v[0]), __float_as_uint(v[1]), false, false);
          const auto s1 = __builtin_amdgcn_permlane16_swap(__float_as_uint(v[2]), __float_as_uint(v[3]), false, false);
          const float p0 = fadd(__uint_as_float(s0[0]), __uint_as_float(s0[1])), p1 = fadd(__uint_as_float(s1[0]), __uint_as_float(s1[1]));
          const auto s2 = __builtin_amdgcn_permlane32_swap(__float_as_uint(p0), __float_as_uint(p1), false, false);
          float t = fadd(__uint_as_float(s2[0]), __uint_as_float(s2[1]));
          t = dpp_add<0xB1>(t); t = dpp_add<0x4E>(t); t = dpp_add<0x141>(t); t = dpp_add<0x140>(t);
          return t * (1.0f / 512.0f);
        };
        const float mg = fold4(mu);
#pragma unroll
        for (int i = 0; i < 4; ++i) {
          const float m = __int_as_float(__builtin_amdgcn_readlane(__float_as_int(mg), 16 * i));
          float s = 0.f;
#pragma unroll
          for (int j = 0; j < 8; ++j) { const float d = e[i][j] - m; s = ffma(d, d, s); }
          va[i] = s;
        }
        const float vg = fold4(va);
        const u2 o = {__float_as_uint(rbf(mg)), __float_as_uint(__builtin_amdgcn_rcpf(rbf(__builtin_amdgcn_sqrtf(rbf(rbf(vg) + 1e-5f)))))};
        if (l15 == 0) __builtin_amdgcn_raw_buffer_store_b64(o, wsr, WS_ROW + (rgrp * 8 + (w & 1) * 4 + g) * 8, 0, 16);
        asm volatile("" ::: "memory");
      };

      u4 wsh[8], hc[4], uc[4], c1[4]; unsigned hp[4];
      {
        const char* wb = ((xcd >> 2) ? wu : wg) + (g1 ? pn * 256 + (cst * 64 + bw * 32) * 3584 : 0);
        const unsigned o = g * 3584 + l15 * 16;
        if (SIDE_SLEEP) __builtin_amdgcn_s_sleep(SIDE_SLEEP);
        asm volatile("global_load_dwordx4 %0, %8, %12\n\tglobal_load_dwordx4 %1, %9, %12\n\tglobal_load_dwordx4 %2, %10, %12\n\tglobal_load_dwordx4 %3, %11, %12\n\t"
                     "global_load_dwordx4 %4, %8, %13\n\tglobal_load_dwordx4 %5, %9, %13\n\tglobal_load_dwordx4 %6, %10, %13\n\tglobal_load_dwordx4 %7, %11, %13"
                     : "=&v"(wsh[0]), "=&v"(wsh[1]), "=&v"(wsh[2]), "=&v"(wsh[3]), "=&v"(wsh[4]), "=&v"(wsh[5]), "=&v"(wsh[6]), "=&v"(wsh[7])
                     : "v"(o), "v"(o + 4 * 3584), "v"(o + 8 * 3584), "v"(o + 12 * 3584), "s"(wb), "s"(wb + 16 * 3584) : "memory");
        rows_load();
      }
      asm volatile("s_load_dwordx2 %0, %1, 0x0\n\ts_waitcnt lgkmcnt(0)" : "=s"(tag) : "s"(ws) : "memory");
      tag += 1;
      *(LAS u64*)(size_t)L_TAG = tag;
      BAR();
#pragma unroll
      for (int z = 0; z < 4; ++z) {
        const int id = t7 + 128 * z, c2 = id >> 3, ch = id & 7;
        const u4 t = lds128u(L_MISC + c2 * 128 + ((ch ^ (c2 & 7)) << 4));
        __builtin_amdgcn_raw_buffer_store_b128(t, wsr, WS_KVT + (pct * 64 + c2) * 4096 + 3968 - ptt * 128 + ch * 16, 0, 16);
      }
      asm volatile("s_waitcnt vmcnt(4)" : "+v"(wsh[0]), "+v"(wsh[1]), "+v"(wsh[2]), "+v"(wsh[3]), "+v"(wsh[4]), "+v"(wsh[5]), "+v"(wsh[6]), "+v"(wsh[7])::"memory");
      STAMP(17);
      if (g1) {
        float cacc[8];
#pragma unroll
        for (int j = 0; j < 4; ++j) {
          cacc[2 * j] = fadd(fadd(fadd(bflo(wsh[0][j]), bflo(wsh[1][j])), fadd(bflo(wsh[2][j]), bflo(wsh[3][j]))),
                             fadd(fadd(bflo(wsh[4][j]), bflo(wsh[5][j])), fadd(bflo(wsh[6][j]), bflo(wsh[7][j]))));
          cacc[2 * j + 1] = fadd(fadd(fadd(bfhi(wsh[0][j]), bfhi(wsh[1][j])), fadd(bfhi(wsh[2][j]), bfhi(wsh[3][j]))),
                                 fadd(fadd(bfhi(wsh[4][j]), bfhi(wsh[5][j])), fadd(bfhi(wsh[6][j]), bfhi(wsh[7][j]))));
        }
        float c4[4], c2[2];
#pragma unroll
        for (int j = 0; j < 4; ++j) {
          const auto s2 = __builtin_amdgcn_permlane16_swap(__float_as_uint(cacc[j]), __float_as_uint(cacc[4 + j]), false, false);
          c4[j] = fadd(__uint_as_float(s2[0]), __uint_as_float(s2[1]));
        }
#pragma unroll
        for (int j = 0; j < 2; ++j) {
          const auto s2 = __builtin_amdgcn_permlane32_swap(__float_as_uint(c4[j]), __float_as_uint(c4[2 + j]), false, false);
          c2[j] = fadd(__uint_as_float(s2[0]), __uint_as_float(s2[1]));
        }
        __builtin_amdgcn_raw_buffer_store_b64((u2){__float_as_uint(c2[0]), __float_as_uint(c2[1])}, wsr,
                                              WS_COL + ((pn * 16 + cst * 2 + bw) * 256 + (xcd >> 2) * 128 + l15 * 8 + (g & 1) * 4 + (g >> 1) * 2) * 4, 0, 16);
      }
      STAMP(10);
      KBAR();
      rows_stat();
      STAMP(18);
      asm volatile("global_load_dwordx4 %0, %9, %11\n\tglobal_load_dwordx4 %1, %9, %11 offset:1024\n\tglobal_load_dwordx4 %2, %9, %11 offset:2048\n\t"
                   "global_load_dwordx4 %3, %9, %11 offset:3072\n\tglobal_load_dword %4, %10, %11\n\tglobal_load_dword %5, %9, %11 offset:1020\n\t"
                   "global_load_dword %6, %9, %11 offset:2044\n\tglobal_load_dword %7, %9, %11 offset:3068\n\tglobal_load_dword %8, %12, %13"
                   : "=&v"(hc[0]), "=&v"(hc[1]), "=&v"(hc[2]), "=&v"(hc[3]), "=&v"(hp[0]), "=&v"(hp[1]), "=&v"(hp[2]), "=&v"(hp[3]), "=&v"(dcr)
                   : "v"(hoff), "v"(lane ? hoff - 4 : hoff), "s"(ssk), "v"(0u), "s"(sD + b * 4) : "memory");
      KBAR();
      WAITV(9);
      KBAR();
      if (!g1) { ctl_add(C_WSYNC); ctl_spin(C_WSYNC, 2); }
      if (tid == 384 || tid == 385) flag_set(ws, tid == 384 ? WS_FS + rgrp * 128 : WS_FK + (pct * 32 + ptt) * 128, tag);
      STAMP(20);
      unsigned fo = lane < 32 || !g1 ? WS_FK + ((b >> 5) * 32 + (lane & 31)) * 128
                                     : WS_FS + (lane < 48 ? pm * 16 + l15 : (l15 >> 2) * 64 + (xcd & 1) * 32 + (l15 & 3) + 4 * pnl) * 128;
      unsigned fo2 = WS_FS + (rt * 8 + (lane & 7)) * 128;
      OPAQUE(fo); OPAQUE(fo2);
      KBAR();
      asm volatile("s_waitcnt vmcnt(0)" : "+v"(hc[0]), "+v"(hc[1]), "+v"(hc[2]), "+v"(hc[3]), "+v"(hp[0]), "+v"(hp[1]), "+v"(hp[2]), "+v"(hp[3]), "+v"(dcr)::"memory");
      if (lane == 0) hp[0] = 0;
#pragma unroll
      for (int z = 0; z < 4; ++z) {
        c1[z] = (u4){__builtin_amdgcn_alignbit(hc[z][0], hp[z], 16), __builtin_amdgcn_alignbit(hc[z][1], hc[z][0], 16),
                     __builtin_amdgcn_alignbit(hc[z][2], hc[z][1], 16), __builtin_amdgcn_alignbit(hc[z][3], hc[z][2], 16)};
        OPAQUE(c1[z]);
      }
      u64 p0, p1;
      if (!g1) __builtin_amdgcn_s_sleep(70);
      if (PEEK_AT == 5) KBAR();
      asm volatile("global_load_dwordx2 %0, %2, %4 sc1\n\tglobal_load_dwordx2 %1, %3, %4 sc1" : "=&v"(p0), "=&v"(p1) : "v"(fo), "v"(fo2), "s"(ws) : "memory");
      KBAR();
      STAMP(23);
      asm volatile("s_waitcnt vmcnt(0)" : "+v"(p0), "+v"(p1)::"memory");
      if (!__all(p0 == tag && p1 == tag)) poll2(ws, fo, fo2, tag);
      if (!g1) ctl_add(C_TAG);
      STAMP(21);
      u2 rsv, csv[16];
      {
        const unsigned co = WS_COL + (g1 ? pn : 0) * 16384 + t7 * 8;
#define LD4(o, vreg) asm volatile("global_load_dwordx2 %0, %4, %5 sc1\n\tglobal_load_dwordx2 %1, %4, %5 offset:1024 sc1\n\t" \
                                  "global_load_dwordx2 %2, %4, %5 offset:2048 sc1\n\tglobal_load_dwordx2 %3, %4, %5 offset:3072 sc1" \
                                  : "=&v"(csv[o]), "=&v"(csv[o + 1]), "=&v"(csv[o + 2]), "=&v"(csv[o + 3]) : "v"(vreg), "s"(ws) : "memory")
        LD4(0, co); LD4(4, co + 4096); LD4(8, co + 8192); LD4(12, co + 12288);
        asm volatile("global_load_dwordx2 %0, %1, %2 sc1" : "=&v"(rsv) : "v"(WS_ROW + (g1 ? pm : 0) * 1024 + t7 * 8), "s"(ws) : "memory");
      }
      asm volatile("global_load_dwordx4 %0, %4, %5 sc1\n\tglobal_load_dwordx4 %1, %4, %5 offset:1024 sc1\n\tglobal_load_dwordx4 %2, %4, %5 offset:2048 sc1\n\t"
                   "global_load_dwordx4 %3, %4, %5 offset:3072 sc1" : "=&v"(uc[0]), "=&v"(uc[1]), "=&v"(uc[2]), "=&v"(uc[3]) : "v"(WS_KVT + hoff), "s"(ws) : "memory");
      if (PEEK_AT == 4) KBAR();
      const unsigned base = L_CV + bw * CV_CH;
#pragma unroll
      for (int z = 0; z < 4; ++z) {
        sts128(base + 32 + lane * 16 + z * 1024, hc[z]);
        sts128(base + 32 + lane * 16 + z * 1024 + CV_COPY, c1[z]);
      }
      if (lane < 4) sts128(base + (lane >> 1) * CV_COPY + (lane & 1) * 16, (u4){0, 0, 0, 0});
      if (lane >= 32) sts128(base + CV_UR + 4096 + (lane - 32) * 16, (u4){0, 0, 0, 0});
      STAMP(14);
      KBAR();
      asm volatile("s_waitcnt vmcnt(4)" : "+v"(rsv), "+v"(csv[0]), "+v"(csv[1]), "+v"(csv[2]), "+v"(csv[3]), "+v"(csv[4]), "+v"(csv[5]), "+v"(csv[6]), "+v"(csv[7]),
                   "+v"(csv[8]), "+v"(csv[9]), "+v"(csv[10]), "+v"(csv[11]), "+v"(csv[12]), "+v"(csv[13]), "+v"(csv[14]), "+v"(csv[15])::"memory");
      {
        *(LAS u2*)(size_t)(L_RS + t7 * 8) = rsv;
        float s0[4], s1[4];
#pragma unroll
        for (int i = 0; i < 4; ++i) {
          s0[i] = fadd(fadd(__uint_as_float(csv[i][0]), __uint_as_float(csv[4 + i][0])), fadd(__uint_as_float(csv[8 + i][0]), __uint_as_float(csv[12 + i][0])));
          s1[i] = fadd(fadd(__uint_as_float(csv[i][1]), __uint_as_float(csv[4 + i][1])), fadd(__uint_as_float(csv[8 + i][1]), __uint_as_float(csv[12 + i][1])));
        }
        *(LAS u2*)(size_t)(L_CS + t7 * 8) = (u2){__float_as_uint(fadd(fadd(s0[0], s0[1]), fadd(s0[2], s0[3]))), __float_as_uint(fadd(fadd(s1[0], s1[1]), fadd(s1[2], s1[3])))};
      }
      STAMP(16);
      KBAR();
      if (b == 255 && bw == 1) {
#pragma unroll 1
        for (int z = 0; z < 4; ++z) poll(ws, WS_FS + (z * 64 + lane) * 128, tag);
        if (lane == 0) flag_set(ws, WS_EPOCH, tag);
      }
      asm volatile("s_waitcnt vmcnt(0)" : "+v"(uc[0]), "+v"(uc[1]), "+v"(uc[2]), "+v"(uc[3])::"memory");
#pragma unroll
      for (int z = 0; z < 4; ++z) sts128(base + CV_UR + lane * 16 + z * 1024, uc[z]);
      ctl_add(C_CVIN);
      STAMP(15);
    }
    ctl_wait(C_CVIN, 2);
    const int ch = gw >> 1, half = gw & 1;
    const unsigned base = L_CV + ch * CV_CH;
    const unsigned o = 8 * g + l15 + 1, kap = o & 1;
    unsigned aa = base + kap * CV_COPY + 2 * (o + kap) + half * 256, ab = aa + 8;
    unsigned ba = base + CV_UR + 2 * (2032 - 16 * l15 + 8 * g) - 3584 + half * 256;
    OPAQUE(aa); OPAQUE(ab); OPAQUE(ba);
    f4 y[8];
#pragma unroll
    for (int n = 0; n < 8; ++n) y[n] = (f4){0.f, 0.f, 0.f, 0.f};
    bf8 A[4], B[8];
    auto lda = [&](int qi) {
      const int off = (qi & 7) * 512 + (qi >> 3) * 64;
      const u2a lo = *(const LAS u2a*)(size_t)(aa + off), hi = *(const LAS u2a*)(size_t)(ab + off);
      A[qi & 3] = __builtin_bit_cast(bf8, (u4){lo[0], lo[1], hi[0], hi[1]});
    };
#pragma unroll
    for (int i = 0; i < 8; ++i) { B[7 - i] = lds128(ba + i * 512); if (i < 3) lda(i); }
    SB();
    if (g1) ctl_spin(C_EPI, 4);
    STAMP(5);
    __builtin_amdgcn_s_setprio(1);
#pragma unroll 1
    for (int rho = 0; rho < 4; ++rho) {
#pragma unroll
      for (int qi = 0; qi < 8; ++qi) {
#pragma unroll
        for (int p = 7 - qi; p >= 0; --p) {
          y[qi + p] = mfma(A[qi & 3], B[p], y[qi + p]);
          if (p == 7 - qi) { lda(qi + 3); B[p] = lds128(ba + (7 - p) * 512 + 64); }
          SB();
        }
      }
      aa += 64; ab += 64; ba += 64;
      OPAQUE(aa); OPAQUE(ab); OPAQUE(ba);
    }
    STAMP(6);
    ctl_add(C_PAIR + ch * 4); ctl_spin(C_PAIR + ch * 4, 2);
    const unsigned ex0 = base + half * 4096, ex1 = base + (half ^ 1) * 4096;
#pragma unroll
    for (int n = 0; n < 4; ++n) {
      if (half) { const f4 t = y[n]; y[n] = y[4 + n]; y[4 + n] = t; }
      sts128f(ex0 + n * 1024 + lane * 16, y[4 + n]);
    }
    u2 uu[4];
#pragma unroll
    for (int n = 0; n < 4; ++n) uu[n] = *(const LAS u2*)(size_t)(base + CV_UR + 2 * (2044 - (256 * (4 * half + n) + 16 * l15 + 4 * g)));
    ctl_add(C_PAIR + ch * 4);
    const float dc = ch ? bfhi(dcr) : bflo(dcr);
#pragma unroll
    for (int n = 0; n < 4; ++n) {
      y[n][0] = ffma(bfhi(uu[n][1]), dc, y[n][0]); y[n][1] = ffma(bflo(uu[n][1]), dc, y[n][1]);
      y[n][2] = ffma(bfhi(uu[n][0]), dc, y[n][2]); y[n][3] = ffma(bflo(uu[n][0]), dc, y[n][3]);
    }
    ctl_spin(C_PAIR + ch * 4, 4);
    STAMP(11);
    u2 pk[4];
#pragma unroll
    for (int n = 0; n < 4; ++n) {
      const f4 t = lds128f(ex1 + n * 1024 + lane * 16);
      pk[n] = (u2){pk2(fadd(t[0], y[n][0]), fadd(t[1], y[n][1])), pk2(fadd(t[2], y[n][2]), fadd(t[3], y[n][3]))};
    }
#pragma unroll
    for (int n = 0; n < 4; n += 2) {
      const auto s0 = __builtin_amdgcn_permlane16_swap(pk[n][0], pk[n + 1][0], false, false);
      const auto s1 = __builtin_amdgcn_permlane16_swap(pk[n][1], pk[n + 1][1], false, false);
      const u4 o4 = {s0[0], s1[0], s0[1], s1[1]};
      __builtin_amdgcn_raw_buffer_store_b128(o4, wsr, WS_YT + (2 * b + ch) * 4096 + 2 * (256 * (4 * half + n + (g & 1)) + 16 * l15 + 8 * (g >> 1)), 0, 16);
    }
    STAMP(22);
    WAITV(0);
    ctl_add(C_BSYNC);
    ctl_spin(C_BSYNC, 4);
    if (tid == 256) flag_set(ws, WS_FY + b * 128, tag);
    STAMP(7);
    tail_load();
    if (gw == 3) {
      poll(ws, WS_FY + (ct2 * 32 + (lane & 31)) * 128, tag);
      ctl_add(C_GO);
    } else ctl_wait(C_GO, 1);
    STAMP(8);
    u4 yq[2];
#pragma unroll
    for (int z = 0; z < 2; ++z) {
      const int id = t8 + 256 * z;
      yq[z] = __builtin_amdgcn_raw_buffer_load_b128(wsr, WS_YT + (ct2 * 64 + (id >> 3)) * 4096 + rt * 128 + (id & 7) * 16, 0, 16);
    }
#pragma unroll
    for (int z = 0; z < 2; ++z) {
      const int id = t8 + 256 * z, c = id >> 3;
      sts128(L_YI + c * 128 + (((id & 7) ^ (c >> 3)) << 4), yq[z]);
    }
    __builtin_amdgcn_s_setprio(0);
    STAMP(9);
  }
  f2 xn[4];
#pragma unroll
  for (int j = 0; j < 4; ++j) {
    const float m = __uint_as_float(st2[0]), rr = __uint_as_float(st2[1]);
    const unsigned d = pk2(bflo(xq[j]) - m, bfhi(xq[j]) - m);
    const unsigned n = pk2(bflo(d) * rr, bfhi(d) * rr);
    xn[j] = (f2){bflo(n), bfhi(n)};
    OPAQUE(xn[j]);
  }
  BAR();
  if (w < 4) STAMP(10);
  {
    int ln3 = __builtin_amdgcn_mbcnt_hi(-1, __builtin_amdgcn_mbcnt_lo(-1, 0));
    OPAQUE(ln3);
    const int tk = w * 8 + (ln3 >> 3), c8 = ln3 & 7;
    const unsigned pb = (w >> 2) * SLOT + (tk & 31) * 256;
    f4 o0 = lds128f(pb + (((2 * c8) ^ (tk & 7)) << 4)), o1 = lds128f(pb + (((2 * c8 + 1) ^ (tk & 7)) << 4));
#pragma unroll
    for (int k4 = 1; k4 < 4; ++k4) {
      o0 += lds128f(pb + k4 * 12288 + (((2 * c8) ^ (tk & 7)) << 4));
      o1 += lds128f(pb + k4 * 12288 + (((2 * c8 + 1) ^ (tk & 7)) << 4));
    }
    const unsigned ya = L_YI + c8 * 1024 + ((w ^ c8) << 4) + (tk & 7) * 2;
    unsigned yv[8];
#pragma unroll
    for (int j = 0; j < 8; ++j) yv[j] = *(const LAS unsigned short*)(size_t)(ya + j * 128);
    f2 o[4] = {{o0[0], o0[1]}, {o0[2], o0[3]}, {o1[0], o1[1]}, {o1[2], o1[3]}}, e[4], sg[4];
#pragma unroll
    for (int j = 0; j < 4; ++j) {
      const f2 z = o[j] * -1.4426950408889634f;
      e[j] = (f2){__builtin_amdgcn_exp2f(z[0]), __builtin_amdgcn_exp2f(z[1])} + 1.0f;
      sg[j] = xn[j] * (f2){bflo(yv[2 * j]), bflo(yv[2 * j + 1])} * o[j];
    }
    u4 res;
#pragma unroll
    for (int j = 0; j < 4; ++j) {
      const f2 t = __builtin_elementwise_fma(sg[j], (f2){__builtin_amdgcn_rcpf(e[j][0]), __builtin_amdgcn_rcpf(e[j][1])}, (f2){bflo(xq[j]), bfhi(xq[j])});
      res[j] = pk2(t[0], t[1]);
    }
    *(GAS u4*)(out + (rt * 64 + tk) * 1024 + ct2 * 128 + c8 * 16) = res;
  }
  if (w < 4) STAMP(11);
}
}

extern "C" void kernel_launch(void* const* d_in, const int* in_sizes, int n_in, void* d_out, int out_size, void* d_ws, size_t ws_size,
                              hipStream_t stream) {
  (void)in_sizes; (void)n_in; (void)out_size; (void)ws_size;
  (void)hipFuncSetAttribute((const void*)h3_geglu_fused, hipFuncAttributeMaxDynamicSharedMemorySize, LDS_BYTES);
  h3_geglu_fused<<<256, 512, LDS_BYTES, stream>>>((const char*)d_in[0], (const char*)d_in[1], (const char*)d_in[2], (const char*)d_in[3],
                                                  (const char*)d_in[4], (const char*)d_in[5], (const char*)d_in[7], (const char*)d_in[8],
                                                  (const char*)d_in[9], (char*)d_out, (char*)d_ws);
}
```

```cpp
#define PROF
#define PROBE_B 9
#define PROBE_W 0
#define PROBE_I 18
#include <hip/hip_runtime.h>
#include <stdint.h>

#define LAS __attribute__((address_space(3)))
#define GAS __attribute__((address_space(1)))
#define DI __device__ __forceinline__
typedef __bf16 bf8 __attribute__((ext_vector_type(8)));
typedef __bf16 bf2 __attribute__((ext_vector_type(2)));
typedef short s4 __attribute__((ext_vector_type(4)));
typedef float f4 __attribute__((ext_vector_type(4)));
typedef float f2 __attribute__((ext_vector_type(2)));
typedef unsigned u4 __attribute__((ext_vector_type(4)));
typedef unsigned u2 __attribute__((ext_vector_type(2)));
typedef unsigned u2a __attribute__((ext_vector_type(2), aligned(4)));
typedef unsigned long long u64;

namespace {
constexpr unsigned WS_EPOCH = 0;
constexpr unsigned WS_FK = 4096;
constexpr unsigned WS_FS = WS_FK + 256 * 128;
constexpr unsigned WS_FH = WS_FS + 256 * 128;
constexpr unsigned WS_FY = WS_FH + 256 * 128;
constexpr unsigned WS_ROW = WS_FY + 256 * 128;
constexpr unsigned WS_COL = WS_ROW + 2048 * 8;
constexpr unsigned WS_KVT = 1u << 20;
constexpr unsigned WS_YT = 3u << 20;
constexpr unsigned WS_H = 5u << 20;
constexpr unsigned WS_PROF = 32u << 20;
constexpr unsigned SLOT = 49152;
constexpr unsigned L_MISC = 3 * SLOT;
constexpr unsigned L_RS = 159744;
constexpr unsigned L_CS = L_RS + 1024;
constexpr unsigned L_CTL = L_CS + 1024;
constexpr unsigned C_LAND = L_CTL, C_CONS = L_CTL + 40, C_BSYNC = L_CTL + 72, C_PRE = L_CTL + 76, C_WSYNC = L_CTL + 80, C_EPI = L_CTL + 84,
                   C_HPUB = L_CTL + 88, C_GO = L_CTL + 92, C_WD = L_CTL + 96, C_CVIN = L_CTL + 104, C_TAG = L_CTL + 108, C_PAIR = L_CTL + 112,
                   C_RD = L_CTL + 128, C_LD = L_CTL + 144, C_HOK = L_CTL + 160, L_TAG = L_CTL + 192;
constexpr unsigned CV_COPY = 4160, CV_UR = 2 * CV_COPY, CV_CH = CV_UR + 4608;
constexpr unsigned L_CV = L_RS - 2 * CV_CH;
static_assert(L_CV >= 131072, "W_down ring");
constexpr unsigned L_YI = L_CV;
constexpr unsigned LDS_BYTES = 163840;
constexpr int SPIN_CAP = 1 << 18;

DI float bflo(unsigned u) { return __uint_as_float(u << 16); }
DI float bfhi(unsigned u) { return __uint_as_float(u & 0xffff0000u); }
DI unsigned pk2(float a, float b) { f2 v = {a, b}; return __builtin_bit_cast(unsigned, __builtin_convertvector(v, bf2)); }
DI float dot2(unsigned a, bf2 b, float c) { return __builtin_amdgcn_fdot2_f32_bf16(__builtin_bit_cast(bf2, a), b, c, false); }
DI float rbf(float a) { return bflo(pk2(a, a)); }
DI f4 mfma(bf8 a, bf8 b, f4 c) { return __builtin_amdgcn_mfma_f32_16x16x32_bf16(a, b, c, 0, 0, 0); }
DI bf8 lds128(unsigned a) { return *(const LAS bf8*)(size_t)a; }
DI u4 lds128u(unsigned a) { return *(const LAS u4*)(size_t)a; }
DI f4 lds128f(unsigned a) { return *(const LAS f4*)(size_t)a; }
DI void sts128(unsigned a, u4 v) { *(LAS u4*)(size_t)a = v; }
DI void sts128f(unsigned a, f4 v) { *(LAS f4*)(size_t)a = v; }
DI bf8 ldtr(unsigned a0, unsigned a1) {
  s4 lo = __builtin_amdgcn_ds_read_tr16_b64_v4i16((LAS s4*)(size_t)a0);
  s4 hi = __builtin_amdgcn_ds_read_tr16_b64_v4i16((LAS s4*)(size_t)a1);
  return __builtin_bit_cast(bf8, __builtin_shufflevector(lo, hi, 0, 1, 2, 3, 4, 5, 6, 7));
}
#define WAITV(n) asm volatile("s_waitcnt vmcnt(%0)" ::"i"(n) : "memory")
#define WAITL(n) asm volatile("s_waitcnt lgkmcnt(%0)" ::"i"(n) : "memory")
#define SB() __builtin_amdgcn_sched_barrier(0)
#define OPAQUE(x) asm volatile("" : "+v"(x))
#define BAR() do { asm volatile("s_waitcnt lgkmcnt(0)" ::: "memory"); __builtin_amdgcn_s_barrier(); asm volatile("" ::: "memory"); } while (0)

DI void glds2(unsigned v0, unsigned v1, const char* sb, unsigned l) {
  asm volatile("s_mov_b32 m0, %3\n\ts_nop 0\n\tglobal_load_lds_dwordx4 %0, %2\n\tglobal_load_lds_dwordx4 %1, %2 offset:1024"
               ::"v"(v0), "v"(v1), "s"(sb), "s"(l) : "memory");
}
DI void glds4(unsigned v0, unsigned v1, unsigned v2, unsigned v3, const char* sb, unsigned l) {
  asm volatile("s_mov_b32 m0, %5\n\ts_nop 0\n\tglobal_load_lds_dwordx4 %0, %4\n\tglobal_load_lds_dwordx4 %1, %4 offset:1024\n\t"
               "global_load_lds_dwordx4 %2, %4 offset:2048\n\tglobal_load_lds_dwordx4 %3, %4 offset:3072"
               ::"v"(v0), "v"(v1), "v"(v2), "v"(v3), "s"(sb), "s"(l) : "memory");
}
DI void flag_set(char* ws, unsigned off, u64 tag) {
  __hip_atomic_store((GAS u64*)(ws + off), tag, __ATOMIC_RELAXED, __HIP_MEMORY_SCOPE_AGENT);
}
DI void poll(const char* ws, unsigned off, u64 tag) {
#pragma unroll 1
  for (int it = 0; it < SPIN_CAP; ++it) {
    u64 x = __hip_atomic_load((GAS u64*)(ws + off), __ATOMIC_RELAXED, __HIP_MEMORY_SCOPE_AGENT);
    if (__all(x == tag)) break;
    __builtin_amdgcn_s_sleep(4);
  }
  asm volatile("" ::: "memory");
}
DI float fadd(float a, float b) { float r; asm("v_add_f32 %0, %1, %2" : "=v"(r) : "v"(a), "v"(b)); return r; }
DI float fsub(float a, float b) { float r; asm("v_sub_f32 %0, %1, %2" : "=v"(r) : "v"(a), "v"(b)); return r; }
DI float fmul(float a, float b) { float r; asm("v_mul_f32 %0, %1, %2" : "=v"(r) : "v"(a), "v"(b)); return r; }
DI float ffma(float a, float b, float c) { float r; asm("v_fma_f32 %0, %1, %2, %3" : "=v"(r) : "v"(a), "v"(b), "v"(c)); return r; }
DI void poll2(const char* ws, unsigned off0, unsigned off1, u64 tag) {
#pragma unroll 1
  for (int it = 0; it < SPIN_CAP; ++it) {
    const u64 x = __hip_atomic_load((GAS u64*)(ws + off0), __ATOMIC_RELAXED, __HIP_MEMORY_SCOPE_AGENT);
    const u64 y = __hip_atomic_load((GAS u64*)(ws + off1), __ATOMIC_RELAXED, __HIP_MEMORY_SCOPE_AGENT);
    if (__all(x == tag && y == tag)) break;
    __builtin_amdgcn_s_sleep(8);
  }
  asm volatile("" ::: "memory");
}
template <int CTRL> DI float dpp_add(float v) {
  return v + __int_as_float(__builtin_amdgcn_update_dpp(0, __float_as_int(v), CTRL, 0xf, 0xf, true));
}
DI float wave_sum(float v) {
  v = dpp_add<0xB1>(v); v = dpp_add<0x4E>(v); v = dpp_add<0x141>(v); v = dpp_add<0x140>(v);
  const int i = __float_as_int(v);
  return (__int_as_float(__builtin_amdgcn_readlane(i, 0)) + __int_as_float(__builtin_amdgcn_readlane(i, 16))) +
         (__int_as_float(__builtin_amdgcn_readlane(i, 32)) + __int_as_float(__builtin_amdgcn_readlane(i, 48)));
}
DI void ctl_add(unsigned addr) {
  asm volatile("s_waitcnt lgkmcnt(0)\n\ts_mov_b64 exec, 1\n\tds_add_u32 %0, %1\n\ts_mov_b64 exec, -1" ::"v"(addr), "v"(1u) : "memory");
}
DI unsigned ctl_get(unsigned addr) {
  unsigned s, t;
  asm volatile("ds_read_b32 %1, %2\n\ts_waitcnt lgkmcnt(0)\n\tv_readfirstlane_b32 %0, %1\n\ts_nop 1" : "=s"(s), "=&v"(t) : "v"(addr) : "memory");
  return s;
}
DI void ctl_spin(unsigned addr, unsigned target) {
#pragma unroll 1
  for (int it = 0; it < SPIN_CAP; ++it)
    if (ctl_get(addr) >= target) break;
}
DI void ctl_wait(unsigned addr, unsigned target) {
#pragma unroll 1
  for (int it = 0; it < SPIN_CAP; ++it) {
    if (ctl_get(addr) >= target) break;
    __builtin_amdgcn_s_sleep(1);
  }
}
#define CSTAMP(i) do { u64 t_; asm volatile("s_memtime %0\n\ts_waitcnt lgkmcnt(0)" : "=s"(t_)::"memory"); tt[i] = (unsigned)t_; } while (0)
#define CNOW(v) do { u64 t_; asm volatile("s_memtime %0\n\ts_waitcnt lgkmcnt(0)" : "=s"(t_)::"memory"); v = (unsigned)t_; } while (0)
#define STAMP(i) do { if ((i) != 0 && (i) != 12 && (i) != PROBE_I) break; u64 t_; asm volatile("s_memrealtime %0\n\ts_waitcnt lgkmcnt(0)" : "=s"(t_)::"memory"); tt[i] = (unsigned)t_; } while (0)

#define HWIN 4
#define WREG 6
#define SFIRST 0
#define PEEK_AT 5
#define SIDE_SLEEP 0
#define PF_WAVES 4
#define S1_EARLY 1
#define BAR_AT 32
#define KBAR() do { if (g1) BAR(); } while (0)
struct Frag { bf8 x[4]; bf8 w[2][4]; };

__global__ __launch_bounds__(512, 2) void h3_geglu_fused(const char* q, const char* kk, const char* vv, const char* shk, const char* ssk,
                                                         const char* sD, const char* wg, const char* wu, const char* wd, char* out, char* ws) {
  extern __shared__ __attribute__((aligned(16))) char smem[];
  const int lane = threadIdx.x & 63, w = __builtin_amdgcn_readfirstlane(threadIdx.x >> 6) ^ (SFIRST ? 4 : 0), gw = w & 3;
  const int tid = w * 64 + lane, t8 = tid & 255;
  const int b = blockIdx.x, xcd = b & 7, r = b >> 3;
  const bool g1 = r < 28;
  const int l15 = lane & 15, g = lane >> 4;
  const __amdgpu_buffer_rsrc_t wsr = __builtin_amdgcn_make_buffer_rsrc(ws, 0, 16 << 20, 0x00020000);
  const int pnl = r >> 2;
  const int pm = (xcd >> 1) * 4 + (r & 3), pn = (xcd & 1) * 7 + pnl;
  const int rt = xcd * 4 + (r & 3), ct2 = r >> 2;
  unsigned tt[24] = {0};
  STAMP(0);
  auto wslot = [](int i) -> unsigned { const int s = i & 7; return s < 2 ? 2 * SLOT + s * 4096 : s < 5 ? (s - 2) * 4096 : SLOT + (s - 5) * 4096; };
  auto wdstage = [&](unsigned dv0, int kq, int i) {
    glds4(dv0, dv0 + 7168, dv0 + 2 * 7168, dv0 + 3 * 7168, wd + ct2 * 128 + (kq * 14 + i) * 32768, wslot(i) + kq * ((i & 7) < 2 ? 8192 : 12288));
  };
  u64 tag;
  auto tag_get = [&]() {
    const u2 t = *(const volatile LAS u2*)(size_t)L_TAG;
    tag = ((u64)(unsigned)__builtin_amdgcn_readfirstlane(t[1]) << 32) | (unsigned)__builtin_amdgcn_readfirstlane(t[0]);
  };

  u4 xq; u2 st2;
  auto tail_load = [&]() {
    int ln = __builtin_amdgcn_mbcnt_hi(-1, __builtin_amdgcn_mbcnt_lo(-1, 0));
    OPAQUE(ln);
    const int tk = w * 8 + (ln >> 3), c8 = ln & 7;
    xq = *(const GAS u4*)(q + (rt * 64 + tk) * 1024 + ct2 * 128 + c8 * 16);
    st2 = __builtin_amdgcn_raw_buffer_load_b64(wsr, WS_ROW + (rt * 64 + tk) * 8, 0, 16);
  };

  if (w < 4) {
    {
      const int ptt = b >> 3, pct = b & 7;
      const unsigned lk = 2 * SLOT + gw * 5120;
      const unsigned kvo = (lane >> 3) * 1024 + (((lane & 7) ^ ((g & 1) << 2)) << 4);
      const bool top = ptt == 0 && gw == 0;
      const char* kb = kk + (ptt * 64 + gw * 16) * 1024 + pct * 128;
      u2 sw;
      asm volatile("s_mov_b32 m0, %7\n\ts_nop 0\n\tglobal_load_lds_dwordx4 %1, %4\n\tglobal_load_lds_dwordx4 %1, %5 offset:1024\n\t"
                   "global_load_lds_dwordx4 %2, %5 offset:2048\n\tglobal_load_lds_dwordx4 %1, %6 offset:3072\n\ts_add_u32 m0, m0, 4096\n\t"
                   "global_load_dwordx2 %0, %3, %8\n\tglobal_load_lds_dwordx4 %2, %9"
                   : "=&v"(sw)
                   : "v"(kvo), "v"(kvo + 7168), "v"(lane * 8), "s"(top ? kb : kb - 8192), "s"(kb - 1024), "s"(vv + (ptt * 64 + gw * 16) * 1024 + pct * 128 - 3072),
                     "s"(lk), "s"(shk + pct * 512), "s"(vv + (ptt * 64 + gw * 16) * 1024 + pct * 128 + 1024)
                   : "memory", "scc");
      const int qq = l15 >> 2, pp = lane & 3;
      unsigned ra = lk + qq * 128 + (((2 * g + (pp >> 1)) ^ ((qq >> 1) << 2)) << 4) + (pp & 1) * 8;
      OPAQUE(ra);
      asm volatile("s_waitcnt vmcnt(0)" : "+v"(sw)::"memory");
      STAMP(18);
      u2 kr[5], vr[4];
#pragma unroll
      for (int i = 0; i < 5; ++i) kr[i] = __builtin_bit_cast(u2, __builtin_amdgcn_ds_read_tr16_b64_v4i16((LAS s4*)(size_t)(ra + 512 + i * 512)));
#pragma unroll
      for (int i = 0; i < 4; ++i) vr[i] = __builtin_bit_cast(u2, __builtin_amdgcn_ds_read_tr16_b64_v4i16((LAS s4*)(size_t)(ra + 3072 + i * 512)));
      if (top) kr[0] = (u2){0, 0};
      const float w0 = bflo(sw[0]), w1 = bfhi(sw[0]), w2 = bflo(sw[1]), w3 = bfhi(sw[1]);
      float kf[20], u[16];
#pragma unroll
      for (int i = 0; i < 5; ++i) { kf[4 * i] = bflo(kr[i][0]); kf[4 * i + 1] = bfhi(kr[i][0]); kf[4 * i + 2] = bflo(kr[i][1]); kf[4 * i + 3] = bfhi(kr[i][1]); }
      SB();
#pragma unroll
      for (int i = 0; i < 16; ++i) u[i] = fmul(kf[i + 1], w3);
      SB();
#pragma unroll
      for (int i = 0; i < 16; ++i) u[i] = ffma(kf[i + 2], w2, u[i]);
      SB();
#pragma unroll
      for (int i = 0; i < 16; ++i) u[i] = ffma(kf[i + 3], w1, u[i]);
      SB();
#pragma unroll
      for (int i = 0; i < 16; ++i) u[i] = ffma(kf[i + 4], w0, u[i]);
      SB();
#pragma unroll
      for (int i = 0; i < 16; ++i) { const unsigned vw = vr[i >> 2][(i >> 1) & 1]; u[i] = fmul(u[i], (i & 1) ? bfhi(vw) : bflo(vw)); }
      SB();
      const unsigned ad = L_MISC + lane * 128;
      sts128(ad + (((7 - 2 * gw) ^ (lane & 7)) << 4), (u4){pk2(u[7], u[6]), pk2(u[5], u[4]), pk2(u[3], u[2]), pk2(u[1], u[0])});
      sts128(ad + (((6 - 2 * gw) ^ (lane & 7)) << 4), (u4){pk2(u[15], u[14]), pk2(u[13], u[12]), pk2(u[11], u[10]), pk2(u[9], u[8])});
      STAMP(1);
      BAR();
    }
    if (g1) {
      const int qq = l15 >> 2, pp = lane & 3;
      const int wm = w >> 1, wn = w & 1;
      f4 acc[2][4][4];
#pragma unroll
      for (int s = 0; s < 2; ++s)
#pragma unroll
        for (int e = 0; e < 4; ++e)
#pragma unroll
          for (int i = 0; i < 4; ++i) acc[s][e][i] = (f4){0.f, 0.f, 0.f, 0.f};
      unsigned xa[2], wa[4];
      xa[0] = (wm * 64 + l15) * 128 + ((g ^ (l15 >> 1)) << 4); xa[1] = xa[0] ^ 64;
#pragma unroll
      for (int e = 0; e < 4; ++e) wa[e] = (8 * g + qq) * 256 + (((4 * wn + e) ^ (qq | ((g & 1) << 2))) << 5) + pp * 8;
      Frag f0, f1;
      s4 lo_;
      auto ldpart = [&](Frag& f, int ks, int j) {
        const unsigned o = 16384 + ks * 8192;
        if (j == 0) f.x[0] = lds128(xa[ks]);
        if (j >= 1 && j <= 16) {
          const int s = (j - 1) >> 3, e = ((j - 1) >> 1) & 3;
          if (j & 1) lo_ = __builtin_amdgcn_ds_read_tr16_b64_v4i16((LAS s4*)(size_t)(wa[e] + o + s * 16384));
          else {
            const s4 hi = __builtin_amdgcn_ds_read_tr16_b64_v4i16((LAS s4*)(size_t)(wa[e] + o + s * 16384 + 1024));
            f.w[s][e] = __builtin_bit_cast(bf8, __builtin_shufflevector(lo_, hi, 0, 1, 2, 3, 4, 5, 6, 7));
          }
        }
        if (j >= 17 && j < 20) f.x[j - 16] = lds128(xa[ks] + (j - 16) * 2048);
      };
      auto mm1 = [&](const Frag& f, int j) { acc[(j >> 2) & 1][j & 3][j >> 3] = mfma(f.w[(j >> 2) & 1][j & 3], f.x[j >> 3], acc[(j >> 2) & 1][j & 3][j >> 3]); };
      auto second = [&](int st) {
        const int d = (st == 2 || st == 5) ? -2 * (int)SLOT : (int)SLOT;
#pragma unroll
        for (int j = 0; j < 32; ++j) {
          if (j == BAR_AT) { BAR(); SB(); }
          mm1(f0, j);
          if (j < 20) ldpart(f1, 1, j);
          if (j == 20) { xa[0] += d; OPAQUE(xa[0]); xa[1] += d; OPAQUE(xa[1]); }
          if (j == 21) { wa[0] += d; OPAQUE(wa[0]); wa[1] += d; OPAQUE(wa[1]); }
          if (j == 22) { wa[2] += d; OPAQUE(wa[2]); wa[3] += d; OPAQUE(wa[3]); }
          SB();
        }
        if (BAR_AT == 32) { BAR(); SB(); }
      };
      OPAQUE(xa[0]); OPAQUE(xa[1]); OPAQUE(wa[0]); OPAQUE(wa[1]); OPAQUE(wa[2]); OPAQUE(wa[3]);
      STAMP(2);
#pragma unroll
      for (int j = 0; j < 20; ++j) ldpart(f0, 0, j);
      SB();
      second(0);
#pragma unroll 1
      for (int st = 1; st < 8; ++st) {
#pragma unroll
        for (int j = 0; j < 32; ++j) { mm1(f1, j); if (j < 20) ldpart(f0, 0, j); SB(); }
        second(st);
      }
      STAMP(16);
      constexpr float C0 = -2.0f * 0.7978845608028654f * 1.4426950408889634f, C1 = C0 * 0.044715f;
      f4 cs[2][4];
      f2 rs[4];
      int ln1 = __builtin_amdgcn_mbcnt_hi(-1, __builtin_amdgcn_mbcnt_lo(-1, 0));
      OPAQUE(ln1);
      const int lane = ln1, l15 = lane & 15, g = lane >> 4;
#pragma unroll
      for (int j = 0; j < 32; ++j) {
        mm1(f1, j);
        if (j < 8) cs[j >> 2][j & 3] = lds128f(L_CS + ((j >> 2) * 128 + wn * 64 + (j & 3) * 16 + 4 * g) * 4);
        if (j >= 8 && j < 12) rs[j - 8] = *(const LAS f2*)(size_t)(L_RS + (wm * 64 + (j - 8) * 16 + l15) * 8);
        SB();
      }
      STAMP(3);
      const unsigned hso = WS_H + ((pm * 8 + wm * 4) * 56 + pn * 4 + wn * 2) * 1024 + lane * 16;
#pragma unroll
      for (int i = 0; i < 4; ++i) {
        const float m = -rs[i][0], rr = rs[i][1];
        const float k0 = C0 * rr, k1 = C1 * rr * rr * rr, i2 = __builtin_amdgcn_rcpf(rr * rr);
        const f2 m2 = {m, m}, k02 = {k0, k0}, k12 = {k1, k1}, i22 = {i2, i2};
        f2 a[8], c[8], z[8];
        SB();
#pragma unroll
        for (int j = 0; j < 8; ++j) {
          const int e = j >> 1, h = (j & 1) * 2;
          a[j] = __builtin_elementwise_fma(m2, (f2){cs[0][e][h], cs[0][e][h + 1]}, (f2){acc[0][e][i][h], acc[0][e][i][h + 1]});
        }
        SB();
#pragma unroll
        for (int j = 0; j < 8; ++j) z[j] = a[j] * a[j];
        SB();
#pragma unroll
        for (int j = 0; j < 8; ++j) z[j] = __builtin_elementwise_fma(z[j], k12, k02);
        SB();
#pragma unroll
        for (int j = 0; j < 8; ++j) z[j] = z[j] * a[j];
        SB();
#pragma unroll
        for (int j = 0; j < 8; ++j) {
          const int e = j >> 1, h = (j & 1) * 2;
          z[j] = (f2){__builtin_amdgcn_exp2f(z[j][0]), __builtin_amdgcn_exp2f(z[j][1])};
          c[j] = __builtin_elementwise_fma(m2, (f2){cs[1][e][h], cs[1][e][h + 1]}, (f2){acc[1][e][i][h], acc[1][e][i][h + 1]});
          SB();
        }
#pragma unroll
        for (int j = 0; j < 8; ++j) z[j] = __builtin_elementwise_fma(z[j], i22, i22);
        SB();
#pragma unroll
        for (int j = 0; j < 8; ++j) {
          z[j] = (f2){__builtin_amdgcn_rcpf(z[j][0]), __builtin_amdgcn_rcpf(z[j][1])};
          c[j] = c[j] * a[j];
          SB();
        }
#pragma unroll
        for (int j = 0; j < 8; ++j) c[j] = c[j] * z[j];
        SB();
        u4 o0, o1;
#pragma unroll
        for (int j = 0; j < 4; ++j) { o0[j] = pk2(c[j][0], c[j][1]); o1[j] = pk2(c[4 + j][0], c[4 + j][1]); }
        __builtin_amdgcn_raw_buffer_store_b128(o0, wsr, hso + i * 56 * 1024, 0, 16);
        __builtin_amdgcn_raw_buffer_store_b128(o1, wsr, hso + i * 56 * 1024 + 1024, 0, 16);
      }
      SB();
      ctl_add(C_EPI);
      tag_get();
      STAMP(4);
    } else if (w < PF_WAVES) {
#pragma unroll 1
      for (int j = (r - 28) * PF_WAVES + w; j < 224; j += 4 * PF_WAVES) {
        if (ctl_get(C_TAG) >= 2) break;
        const unsigned o = ((((j >> 2) & 3) * 14 + (j >> 4)) * 32 + (j & 3) * 8 + (lane >> 3)) * 1024 + (lane & 7) * 128;
        asm volatile("s_waitcnt vmcnt(1)\n\ts_mov_b32 m0, %2\n\ts_nop 0\n\tglobal_load_lds_dword %0, %1" ::"v"(o), "s"(wd), "s"(131072 + w * 256) : "memory");
      }
      WAITV(0);
    }
    int ln2 = __builtin_amdgcn_mbcnt_hi(-1, __builtin_amdgcn_mbcnt_lo(-1, 0));
    OPAQUE(ln2);
    {
      const int lane = ln2, l15 = lane & 15, g = lane >> 4, qq = l15 >> 2, pp = lane & 3;
      const unsigned dv0 = (lane >> 3) * 1024 + (((lane & 7) ^ (g << 1)) * 16);
      f4 acc[4][4];
#pragma unroll
      for (int t = 0; t < 4; ++t)
#pragma unroll
        for (int i = 0; i < 4; ++i) acc[t][i] = (f4){0.f, 0.f, 0.f, 0.f};
      const unsigned da = (4 * g + qq) * 128 + (((2 * g + (qq >> 1)) & 3) << 5) + pp * 8;
      unsigned dlo[4];
#pragma unroll
      for (int t = 0; t < 4; ++t) { dlo[t] = (da ^ (t * 32)) + gw * 12288; OPAQUE(dlo[t]); }
      const unsigned dh = 2 * SLOT - gw * 4096;
      constexpr int WR = 6, HW = HWIN;
      bf8 wf[WR][4];
      auto waddr = [&](int i, int t) -> unsigned {
        if ((i & 7) >= 2) return dlo[t] + wslot(i);
        unsigned d_ = dh;
        asm volatile("" : "+s"(d_));
        unsigned a_ = dlo[t] + d_;
        OPAQUE(a_);
        return a_ + (i & 1) * 4096;
      };
      __builtin_amdgcn_s_setprio(2);
      if (g1) {
        WAITV(0);
        if (lane == 0 && __hip_atomic_fetch_add((LAS unsigned*)(size_t)C_HPUB, 1u, __ATOMIC_RELAXED, __HIP_MEMORY_SCOPE_WORKGROUP) == 3)
          flag_set(ws, WS_FH + (pm * 16 + pn) * 128, tag);
      }
      STAMP(5);
      ctl_wait(C_WD + (gw >> 1) * 4, 2);
      if (!g1) tag_get();
#pragma unroll
      for (int t = 0; t < WR * 4; ++t) { const unsigned a_ = waddr(t >> 2, t & 3); wf[t >> 2][t & 3] = ldtr(a_, a_ + 2048); }
      WAITL(0);
      STAMP(17);
#pragma unroll
      for (int i = 8; i < 14; ++i) wdstage(dv0, gw, i);
      __builtin_amdgcn_s_setprio(0);
      STAMP(9);
      poll(ws, WS_FH + ((rt >> 1) * 16 + ((gw * 7) >> 1) + (lane & 3)) * 128, tag);
      STAMP(6);
      u4 hf[HW][4];
      unsigned ho[4];
#pragma unroll
      for (int t = 0; t < 4; ++t) ho[t] = WS_H + ((rt * 4 + t) * 56 + gw * 14) * 1024 + lane * 16;
      auto ldh1 = [&](int i, int t) { hf[i % HW][t] = __builtin_amdgcn_raw_buffer_load_b128(wsr, ho[t] + (i & 3) * 1024, (i >> 2) * 4096, 16); };
#pragma unroll
      for (int i = 0; i < HW * 4; ++i) { ldh1(i >> 2, i & 3); SB(); }
      s4 lo_;
      unsigned wa_;
#pragma unroll
      for (int i = 0; i < 14; ++i) {
        const int nw = i + WR - 1;
        if (i == 11) { tail_load(); SB(); }
#pragma unroll
        for (int j = 0; j < 16; ++j) {
          const int ti = j >> 2, t = j & 3;
          acc[t][ti] = mfma(wf[i % WR][t], __builtin_bit_cast(bf8, hf[i % HW][ti]), acc[t][ti]);
          if (i >= 1 && nw < 14 && t != 3 && (j - ti) < 8) {
            const int e = j - ti;
            if (!(e & 1)) wa_ = waddr(nw, e >> 1);
            const s4 v_ = __builtin_amdgcn_ds_read_tr16_b64_v4i16((LAS s4*)(size_t)(wa_ + (e & 1) * 2048));
            if (e & 1) wf[nw % WR][e >> 1] = __builtin_bit_cast(bf8, __builtin_shufflevector(lo_, v_, 0, 1, 2, 3, 4, 5, 6, 7)); else lo_ = v_;
          }
          if (t == 3 && i + HW < 14) ldh1(i + HW, ti);
          SB();
        }
        if (i == 0) STAMP(19);
      }
      STAMP(7);
#pragma unroll
      for (int ti = 0; ti < 4; ++ti)
#pragma unroll
        for (int t = 0; t < 4; ++t)
          sts128f((ti >> 1) * SLOT + gw * 12288 + ((ti & 1) * 16 + l15) * 256 + (((4 * t + g) ^ (l15 & 7)) << 4), acc[t][ti]);
    }
    STAMP(8);
  } else {
    const int t7 = tid & 127, bw = w & 1;
    unsigned dcr;
    if (w < 6) {
      unsigned xv[4], wv[4];
#pragma unroll
      for (int p = 0; p < 4; ++p) {
        xv[p] = (p * 8 + (lane >> 3)) * 1024 + (((lane & 7) ^ (((p & 1) << 2) | g)) << 4) - p * 1024;
        wv[p] = (p * 4 + g) * 3584 + ((l15 ^ ((g | ((p >> 1) << 2)) << 1)) << 4) - p * 1024;
      }
      auto quarter = [&](int st, int qt) {
        const unsigned l = (st % 3) * SLOT + qt * 4096;
        glds4(xv[0], xv[1], xv[2], xv[3], q + pm * 131072 + qt * 32768 + st * 128, l);
        glds4(wv[0], wv[1], wv[2], wv[3], wg + pn * 256 + (st * 64 + qt * 16) * 3584, l + 16384);
        glds4(wv[0], wv[1], wv[2], wv[3], wu + pn * 256 + (st * 64 + qt * 16) * 3584, l + 32768);
      };
      auto stage = [&](int st) { quarter(st, 2 * bw); quarter(st, 2 * bw + 1); };
      const unsigned dv0 = (lane >> 3) * 1024 + (((lane & 7) ^ (g << 1)) * 16);
      if (g1) { stage(0); if (S1_EARLY) { quarter(1, 2 * bw); WAITV(12); } else WAITV(0); }
      STAMP(1);
      BAR();
      if (g1) { if (!S1_EARLY) quarter(1, 2 * bw); quarter(1, 2 * bw + 1); stage(2); WAITV(24); }
      KBAR();
#pragma unroll 1
      for (int st = 3; st < 8; ++st) {
        if (g1) { stage(st); WAITV(24); }
        KBAR();
      }
      STAMP(13);
      if (!g1) ctl_wait(C_TAG, 2);
      asm volatile("global_load_dword %0, %1, %2" : "=&v"(dcr) : "v"(0u), "s"(sD + b * 4) : "memory");
#pragma unroll
      for (int i = 0; i < 4; ++i) wdstage(dv0, bw * 2 + (i & 1), i >> 1);
      WAITV(17);
      KBAR();
      STAMP(3);
#pragma unroll
      for (int i = 0; i < 6; ++i) wdstage(dv0, bw * 2 + (i & 1), 2 + (i >> 1));
      KBAR();
      STAMP(19);
      tag_get();
      asm volatile("s_waitcnt vmcnt(32)" : "+v"(dcr)::"memory");
#pragma unroll
      for (int i = 0; i < 6; ++i) wdstage(dv0, bw * 2 + (i & 1), 5 + (i >> 1));
      WAITV(24);
      ctl_add(C_WD + bw * 4);
      STAMP(2);
      WAITV(0);
      ctl_add(C_WD + bw * 4);
      STAMP(4);
    } else {
      if (t7 < 48) *(LAS unsigned*)(size_t)(L_CTL + t7 * 4) = 0;
      const int ptt = b >> 3, pct = b & 7;
      const int rgrp = (xcd >> 1) * 64 + (xcd & 1) * 32 + r;
      const int cst = 2 * ((r + pnl) & 3) + ((xcd >> 1) & 1);
      const unsigned hoff = (2 * b + bw) * 4096 + lane * 16;
      u4 xr[4];
      auto rows_load = [&]() {
        asm volatile("global_load_dwordx4 %0, %4, %5\n\tglobal_load_dwordx4 %1, %4, %5 offset:1024\n\tglobal_load_dwordx4 %2, %4, %5 offset:2048\n\t"
                     "global_load_dwordx4 %3, %4, %5 offset:3072"
                     : "=&v"(xr[0]), "=&v"(xr[1]), "=&v"(xr[2]), "=&v"(xr[3]) : "v"(lane * 16), "s"(q + (rgrp * 8 + (w & 1) * 4) * 1024) : "memory");
      };
      auto rows_stat = [&]() {
        asm volatile("" : "+v"(xr[0]), "+v"(xr[1]), "+v"(xr[2]), "+v"(xr[3])::"memory");
        float e[4][8], mu[4], va[4];
#pragma unroll
        for (int i = 0; i < 4; ++i) {
#pragma unroll
          for (int j = 0; j < 4; ++j) { e[i][2 * j] = bflo(xr[i][j]); e[i][2 * j + 1] = bfhi(xr[i][j]); }
          mu[i] = fadd(fadd(fadd(e[i][0], e[i][1]), fadd(e[i][2], e[i][3])), fadd(fadd(e[i][4], e[i][5]), fadd(e[i][6], e[i][7])));
        }
        auto fold4 = [&](const float (&v)[4]) {
          const auto s0 = __builtin_amdgcn_permlane16_swap(__float_as_uint(v[0]), __float_as_uint(v[1]), false, false);
          const auto s1 = __builtin_amdgcn_permlane16_swap(__float_as_uint(v[2]), __float_as_uint(v[3]), false, false);
          const float p0 = fadd(__uint_as_float(s0[0]), __uint_as_float(s0[1])), p1 = fadd(__uint_as_float(s1[0]), __uint_as_float(s1[1]));
          const auto s2 = __builtin_amdgcn_permlane32_swap(__float_as_uint(p0), __float_as_uint(p1), false, false);
          float t = fadd(__uint_as_float(s2[0]), __uint_as_float(s2[1]));
          t = dpp_add<0xB1>(t); t = dpp_add<0x4E>(t); t = dpp_add<0x141>(t); t = dpp_add<0x140>(t);
          return t * (1.0f / 512.0f);
        };
        const float mg = fold4(mu);
#pragma unroll
        for (int i = 0; i < 4; ++i) {
          const float m = __int_as_float(__builtin_amdgcn_readlane(__float_as_int(mg), 16 * i));
          float s = 0.f;
#pragma unroll
          for (int j = 0; j < 8; ++j) { const float d = e[i][j] - m; s = ffma(d, d, s); }
          va[i] = s;
        }
        const float vg = fold4(va);
        const u2 o = {__float_as_uint(rbf(mg)), __float_as_uint(__builtin_amdgcn_rcpf(rbf(__builtin_amdgcn_sqrtf(rbf(rbf(vg) + 1e-5f)))))};
        if (l15 == 0) __builtin_amdgcn_raw_buffer_store_b64(o, wsr, WS_ROW + (rgrp * 8 + (w & 1) * 4 + g) * 8, 0, 16);
        asm volatile("" ::: "memory");
      };

      u4 wsh[8], hc[4], uc[4], c1[4]; unsigned hp[4];
      {
        const char* wb = ((xcd >> 2) ? wu : wg) + (g1 ? pn * 256 + (cst * 64 + bw * 32) * 3584 : 0);
        const unsigned o = g * 3584 + l15 * 16;
        if (SIDE_SLEEP) __builtin_amdgcn_s_sleep(SIDE_SLEEP);
        asm volatile("global_load_dwordx4 %0, %8, %12\n\tglobal_load_dwordx4 %1, %9, %12\n\tglobal_load_dwordx4 %2, %10, %12\n\tglobal_load_dwordx4 %3, %11, %12\n\t"
                     "global_load_dwordx4 %4, %8, %13\n\tglobal_load_dwordx4 %5, %9, %13\n\tglobal_load_dwordx4 %6, %10, %13\n\tglobal_load_dwordx4 %7, %11, %13"
                     : "=&v"(wsh[0]), "=&v"(wsh[1]), "=&v"(wsh[2]), "=&v"(wsh[3]), "=&v"(wsh[4]), "=&v"(wsh[5]), "=&v"(wsh[6]), "=&v"(wsh[7])
                     : "v"(o), "v"(o + 4 * 3584), "v"(o + 8 * 3584), "v"(o + 12 * 3584), "s"(wb), "s"(wb + 16 * 3584) : "memory");
        rows_load();
      }
      asm volatile("s_load_dwordx2 %0, %1, 0x0\n\ts_waitcnt lgkmcnt(0)" : "=s"(tag) : "s"(ws) : "memory");
      tag += 1;
      *(LAS u64*)(size_t)L_TAG = tag;
      BAR();
#pragma unroll
      for (int z = 0; z < 4; ++z) {
        const int id = t7 + 128 * z, c2 = id >> 3, ch = id & 7;
        const u4 t = lds128u(L_MISC + c2 * 128 + ((ch ^ (c2 & 7)) << 4));
        __builtin_amdgcn_raw_buffer_store_b128(t, wsr, WS_KVT + (pct * 64 + c2) * 4096 + 3968 - ptt * 128 + ch * 16, 0, 16);
      }
      asm volatile("s_waitcnt vmcnt(4)" : "+v"(wsh[0]), "+v"(wsh[1]), "+v"(wsh[2]), "+v"(wsh[3]), "+v"(wsh[4]), "+v"(wsh[5]), "+v"(wsh[6]), "+v"(wsh[7])::"memory");
      STAMP(17);
      if (g1) {
        float cacc[8];
#pragma unroll
        for (int j = 0; j < 4; ++j) {
          cacc[2 * j] = fadd(fadd(fadd(bflo(wsh[0][j]), bflo(wsh[1][j])), fadd(bflo(wsh[2][j]), bflo(wsh[3][j]))),
                             fadd(fadd(bflo(wsh[4][j]), bflo(wsh[5][j])), fadd(bflo(wsh[6][j]), bflo(wsh[7][j]))));
          cacc[2 * j + 1] = fadd(fadd(fadd(bfhi(wsh[0][j]), bfhi(wsh[1][j])), fadd(bfhi(wsh[2][j]), bfhi(wsh[3][j]))),
                                 fadd(fadd(bfhi(wsh[4][j]), bfhi(wsh[5][j])), fadd(bfhi(wsh[6][j]), bfhi(wsh[7][j]))));
        }
        float c4[4], c2[2];
#pragma unroll
        for (int j = 0; j < 4; ++j) {
          const auto s2 = __builtin_amdgcn_permlane16_swap(__float_as_uint(cacc[j]), __float_as_uint(cacc[4 + j]), false, false);
          c4[j] = fadd(__uint_as_float(s2[0]), __uint_as_float(s2[1]));
        }
#pragma unroll
        for (int j = 0; j < 2; ++j) {
          const auto s2 = __builtin_amdgcn_permlane32_swap(__float_as_uint(c4[j]), __float_as_uint(c4[2 + j]), false, false);
          c2[j] = fadd(__uint_as_float(s2[0]), __uint_as_float(s2[1]));
        }
        __builtin_amdgcn_raw_buffer_store_b64((u2){__float_as_uint(c2[0]), __float_as_uint(c2[1])}, wsr,
                                              WS_COL + ((pn * 16 + cst * 2 + bw) * 256 + (xcd >> 2) * 128 + l15 * 8 + (g & 1) * 4 + (g >> 1) * 2) * 4, 0, 16);
      }
      STAMP(10);
      KBAR();
      rows_stat();
      STAMP(18);
      asm volatile("global_load_dwordx4 %0, %9, %11\n\tglobal_load_dwordx4 %1, %9, %11 offset:1024\n\tglobal_load_dwordx4 %2, %9, %11 offset:2048\n\t"
                   "global_load_dwordx4 %3, %9, %11 offset:3072\n\tglobal_load_dword %4, %10, %11\n\tglobal_load_dword %5, %9, %11 offset:1020\n\t"
                   "global_load_dword %6, %9, %11 offset:2044\n\tglobal_load_dword %7, %9, %11 offset:3068\n\tglobal_load_dword %8, %12, %13"
                   : "=&v"(hc[0]), "=&v"(hc[1]), "=&v"(hc[2]), "=&v"(hc[3]), "=&v"(hp[0]), "=&v"(hp[1]), "=&v"(hp[2]), "=&v"(hp[3]), "=&v"(dcr)
                   : "v"(hoff), "v"(lane ? hoff - 4 : hoff), "s"(ssk), "v"(0u), "s"(sD + b * 4) : "memory");
      KBAR();
      WAITV(9);
      KBAR();
      if (!g1) { ctl_add(C_WSYNC); ctl_spin(C_WSYNC, 2); }
      if (tid == 384 || tid == 385) flag_set(ws, tid == 384 ? WS_FS + rgrp * 128 : WS_FK + (pct * 32 + ptt) * 128, tag);
      STAMP(20);
      unsigned fo = lane < 32 || !g1 ? WS_FK + ((b >> 5) * 32 + (lane & 31)) * 128
                                     : WS_FS + (lane < 48 ? pm * 16 + l15 : (l15 >> 2) * 64 + (xcd & 1) * 32 + (l15 & 3) + 4 * pnl) * 128;
      unsigned fo2 = WS_FS + (rt * 8 + (lane & 7)) * 128;
      OPAQUE(fo); OPAQUE(fo2);
      KBAR();
      asm volatile("s_waitcnt vmcnt(0)" : "+v"(hc[0]), "+v"(hc[1]), "+v"(hc[2]), "+v"(hc[3]), "+v"(hp[0]), "+v"(hp[1]), "+v"(hp[2]), "+v"(hp[3]), "+v"(dcr)::"memory");
      if (lane == 0) hp[0] = 0;
#pragma unroll
      for (int z = 0; z < 4; ++z) {
        c1[z] = (u4){__builtin_amdgcn_alignbit(hc[z][0], hp[z], 16), __builtin_amdgcn_alignbit(hc[z][1], hc[z][0], 16),
                     __builtin_amdgcn_alignbit(hc[z][2], hc[z][1], 16), __builtin_amdgcn_alignbit(hc[z][3], hc[z][2], 16)};
        OPAQUE(c1[z]);
      }
      u64 p0, p1;
      if (!g1) __builtin_amdgcn_s_sleep(70);
      if (PEEK_AT == 5) KBAR();
      asm volatile("global_load_dwordx2 %0, %2, %4 sc1\n\tglobal_load_dwordx2 %1, %3, %4 sc1" : "=&v"(p0), "=&v"(p1) : "v"(fo), "v"(fo2), "s"(ws) : "memory");
      KBAR();
      STAMP(23);
      asm volatile("s_waitcnt vmcnt(0)" : "+v"(p0), "+v"(p1)::"memory");
      if (!__all(p0 == tag && p1 == tag)) poll2(ws, fo, fo2, tag);
      if (!g1) ctl_add(C_TAG);
      STAMP(21);
      u2 rsv, csv[16];
      {
        const unsigned co = WS_COL + (g1 ? pn : 0) * 16384 + t7 * 8;
#define LD4(o, vreg) asm volatile("global_load_dwordx2 %0, %4, %5 sc1\n\tglobal_load_dwordx2 %1, %4, %5 offset:1024 sc1\n\t" \
                                  "global_load_dwordx2 %2, %4, %5 offset:2048 sc1\n\tglobal_load_dwordx2 %3, %4, %5 offset:3072 sc1" \
                                  : "=&v"(csv[o]), "=&v"(csv[o + 1]), "=&v"(csv[o + 2]), "=&v"(csv[o + 3]) : "v"(vreg), "s"(ws) : "memory")
        LD4(0, co); LD4(4, co + 4096); LD4(8, co + 8192); LD4(12, co + 12288);
        asm volatile("global_load_dwordx2 %0, %1, %2 sc1" : "=&v"(rsv) : "v"(WS_ROW + (g1 ? pm : 0) * 1024 + t7 * 8), "s"(ws) : "memory");
      }
      asm volatile("global_load_dwordx4 %0, %4, %5 sc1\n\tglobal_load_dwordx4 %1, %4, %5 offset:1024 sc1\n\tglobal_load_dwordx4 %2, %4, %5 offset:2048 sc1\n\t"
                   "global_load_dwordx4 %3, %4, %5 offset:3072 sc1" : "=&v"(uc[0]), "=&v"(uc[1]), "=&v"(uc[2]), "=&v"(uc[3]) : "v"(WS_KVT + hoff), "s"(ws) : "memory");
      if (PEEK_AT == 4) KBAR();
      const unsigned base = L_CV + bw * CV_CH;
#pragma unroll
      for (int z = 0; z < 4; ++z) {
        sts128(base + 32 + lane * 16 + z * 1024, hc[z]);
        sts128(base + 32 + lane * 16 + z * 1024 + CV_COPY, c1[z]);
      }
      if (lane < 4) sts128(base + (lane >> 1) * CV_COPY + (lane & 1) * 16, (u4){0, 0, 0, 0});
      if (lane >= 32) sts128(base + CV_UR + 4096 + (lane - 32) * 16, (u4){0, 0, 0, 0});
      STAMP(14);
      KBAR();
      asm volatile("s_waitcnt vmcnt(4)" : "+v"(rsv), "+v"(csv[0]), "+v"(csv[1]), "+v"(csv[2]), "+v"(csv[3]), "+v"(csv[4]), "+v"(csv[5]), "+v"(csv[6]), "+v"(csv[7]),
                   "+v"(csv[8]), "+v"(csv[9]), "+v"(csv[10]), "+v"(csv[11]), "+v"(csv[12]), "+v"(csv[13]), "+v"(csv[14]), "+v"(csv[15])::"memory");
      {
        *(LAS u2*)(size_t)(L_RS + t7 * 8) = rsv;
        float s0[4], s1[4];
#pragma unroll
        for (int i = 0; i < 4; ++i) {
          s0[i] = fadd(fadd(__uint_as_float(csv[i][0]), __uint_as_float(csv[4 + i][0])), fadd(__uint_as_float(csv[8 + i][0]), __uint_as_float(csv[12 + i][0])));
          s1[i] = fadd(fadd(__uint_as_float(csv[i][1]), __uint_as_float(csv[4 + i][1])), fadd(__uint_as_float(csv[8 + i][1]), __uint_as_float(csv[12 + i][1])));
        }
        *(LAS u2*)(size_t)(L_CS + t7 * 8) = (u2){__float_as_uint(fadd(fadd(s0[0], s0[1]), fadd(s0[2], s0[3]))), __float_as_uint(fadd(fadd(s1[0], s1[1]), fadd(s1[2], s1[3])))};
      }
      STAMP(16);
      KBAR();
      if (b == 255 && bw == 1) {
#pragma unroll 1
        for (int z = 0; z < 4; ++z) poll(ws, WS_FS + (z * 64 + lane) * 128, tag);
        if (lane == 0) flag_set(ws, WS_EPOCH, tag);
      }
      asm volatile("s_waitcnt vmcnt(0)" : "+v"(uc[0]), "+v"(uc[1]), "+v"(uc[2]), "+v"(uc[3])::"memory");
#pragma unroll
      for (int z = 0; z < 4; ++z) sts128(base + CV_UR + lane * 16 + z * 1024, uc[z]);
      ctl_add(C_CVIN);
      STAMP(15);
    }
    ctl_wait(C_CVIN, 2);
    const int ch = gw >> 1, half = gw & 1;
    const unsigned base = L_CV + ch * CV_CH;
    const unsigned o = 8 * g + l15 + 1, kap = o & 1;
    unsigned aa = base + kap * CV_COPY + 2 * (o + kap) + half * 256, ab = aa + 8;
    unsigned ba = base + CV_UR + 2 * (2032 - 16 * l15 + 8 * g) - 3584 + half * 256;
    OPAQUE(aa); OPAQUE(ab); OPAQUE(ba);
    f4 y[8];
#pragma unroll
    for (int n = 0; n < 8; ++n) y[n] = (f4){0.f, 0.f, 0.f, 0.f};
    bf8 A[4], B[8];
    auto lda = [&](int qi) {
      const int off = (qi & 7) * 512 + (qi >> 3) * 64;
      const u2a lo = *(const LAS u2a*)(size_t)(aa + off), hi = *(const LAS u2a*)(size_t)(ab + off);
      A[qi & 3] = __builtin_bit_cast(bf8, (u4){lo[0], lo[1], hi[0], hi[1]});
    };
#pragma unroll
    for (int i = 0; i < 8; ++i) { B[7 - i] = lds128(ba + i * 512); if (i < 3) lda(i); }
    SB();
    if (g1) ctl_spin(C_EPI, 4);
    STAMP(5);
    __builtin_amdgcn_s_setprio(1);
#pragma unroll 1
    for (int rho = 0; rho < 4; ++rho) {
#pragma unroll
      for (int qi = 0; qi < 8; ++qi) {
#pragma unroll
        for (int p = 7 - qi; p >= 0; --p) {
          y[qi + p] = mfma(A[qi & 3], B[p], y[qi + p]);
          if (p == 7 - qi) { lda(qi + 3); B[p] = lds128(ba + (7 - p) * 512 + 64); }
          SB();
        }
      }
      aa += 64; ab += 64; ba += 64;
      OPAQUE(aa); OPAQUE(ab); OPAQUE(ba);
    }
    STAMP(6);
    ctl_add(C_PAIR + ch * 4); ctl_spin(C_PAIR + ch * 4, 2);
    const unsigned ex0 = base + half * 4096, ex1 = base + (half ^ 1) * 4096;
#pragma unroll
    for (int n = 0; n < 4; ++n) {
      if (half) { const f4 t = y[n]; y[n] = y[4 + n]; y[4 + n] = t; }
      sts128f(ex0 + n * 1024 + lane * 16, y[4 + n]);
    }
    u2 uu[4];
#pragma unroll
    for (int n = 0; n < 4; ++n) uu[n] = *(const LAS u2*)(size_t)(base + CV_UR + 2 * (2044 - (256 * (4 * half + n) + 16 * l15 + 4 * g)));
    ctl_add(C_PAIR + ch * 4);
    const float dc = ch ? bfhi(dcr) : bflo(dcr);
#pragma unroll
    for (int n = 0; n < 4; ++n) {
      y[n][0] = ffma(bfhi(uu[n][1]), dc, y[n][0]); y[n][1] = ffma(bflo(uu[n][1]), dc, y[n][1]);
      y[n][2] = ffma(bfhi(uu[n][0]), dc, y[n][2]); y[n][3] = ffma(bflo(uu[n][0]), dc, y[n][3]);
    }
    ctl_spin(C_PAIR + ch * 4, 4);
    STAMP(11);
    u2 pk[4];
#pragma unroll
    for (int n = 0; n < 4; ++n) {
      const f4 t = lds128f(ex1 + n * 1024 + lane * 16);
      pk[n] = (u2){pk2(fadd(t[0], y[n][0]), fadd(t[1], y[n][1])), pk2(fadd(t[2], y[n][2]), fadd(t[3], y[n][3]))};
    }
#pragma unroll
    for (int n = 0; n < 4; n += 2) {
      const auto s0 = __builtin_amdgcn_permlane16_swap(pk[n][0], pk[n + 1][0], false, false);
      const auto s1 = __builtin_amdgcn_permlane16_swap(pk[n][1], pk[n + 1][1], false, false);
      const u4 o4 = {s0[0], s1[0], s0[1], s1[1]};
      __builtin_amdgcn_raw_buffer_store_b128(o4, wsr, WS_YT + (2 * b + ch) * 4096 + 2 * (256 * (4 * half + n + (g & 1)) + 16 * l15 + 8 * (g >> 1)), 0, 16);
    }
    STAMP(22);
    WAITV(0);
    ctl_add(C_BSYNC);
    ctl_spin(C_BSYNC, 4);
    if (tid == 256) flag_set(ws, WS_FY + b * 128, tag);
    STAMP(7);
    tail_load();
    if (gw == 3) {
      poll(ws, WS_FY + (ct2 * 32 + (lane & 31)) * 128, tag);
      ctl_add(C_GO);
    } else ctl_wait(C_GO, 1);
    STAMP(8);
    u4 yq[2];
#pragma unroll
    for (int z = 0; z < 2; ++z) {
      const int id = t8 + 256 * z;
      yq[z] = __builtin_amdgcn_raw_buffer_load_b128(wsr, WS_YT + (ct2 * 64 + (id >> 3)) * 4096 + rt * 128 + (id & 7) * 16, 0, 16);
    }
#pragma unroll
    for (int z = 0; z < 2; ++z) {
      const int id = t8 + 256 * z, c = id >> 3;
      sts128(L_YI + c * 128 + (((id & 7) ^ (c >> 3)) << 4), yq[z]);
    }
    __builtin_amdgcn_s_setprio(0);
    STAMP(9);
  }
  f2 xn[4];
#pragma unroll
  for (int j = 0; j < 4; ++j) {
    const float m = __uint_as_float(st2[0]), rr = __uint_as_float(st2[1]);
    const unsigned d = pk2(bflo(xq[j]) - m, bfhi(xq[j]) - m);
    const unsigned n = pk2(bflo(d) * rr, bfhi(d) * rr);
    xn[j] = (f2){bflo(n), bfhi(n)};
    OPAQUE(xn[j]);
  }
  BAR();
  if (w < 4) STAMP(10);
  {
    int ln3 = __builtin_amdgcn_mbcnt_hi(-1, __builtin_amdgcn_mbcnt_lo(-1, 0));
    OPAQUE(ln3);
    const int tk = w * 8 + (ln3 >> 3), c8 = ln3 & 7;
    const unsigned pb = (w >> 2) * SLOT + (tk & 31) * 256;
    f4 o0 = lds128f(pb + (((2 * c8) ^ (tk & 7)) << 4)), o1 = lds128f(pb + (((2 * c8 + 1) ^ (tk & 7)) << 4));
#pragma unroll
    for (int k4 = 1; k4 < 4; ++k4) {
      o0 += lds128f(pb + k4 * 12288 + (((2 * c8) ^ (tk & 7)) << 4));
      o1 += lds128f(pb + k4 * 12288 + (((2 * c8 + 1) ^ (tk & 7)) << 4));
    }
    const unsigned ya = L_YI + c8 * 1024 + ((w ^ c8) << 4) + (tk & 7) * 2;
    unsigned yv[8];
#pragma unroll
    for (int j = 0; j < 8; ++j) yv[j] = *(const LAS unsigned short*)(size_t)(ya + j * 128);
    f2 o[4] = {{o0[0], o0[1]}, {o0[2], o0[3]}, {o1[0], o1[1]}, {o1[2], o1[3]}}, e[4], sg[4];
#pragma unroll
    for (int j = 0; j < 4; ++j) {
      const f2 z = o[j] * -1.4426950408889634f;
      e[j] = (f2){__builtin_amdgcn_exp2f(z[0]), __builtin_amdgcn_exp2f(z[1])} + 1.0f;
      sg[j] = xn[j] * (f2){bflo(yv[2 * j]), bflo(yv[2 * j + 1])} * o[j];
    }
    u4 res;
#pragma unroll
    for (int j = 0; j < 4; ++j) {
      const f2 t = __builtin_elementwise_fma(sg[j], (f2){__builtin_amdgcn_rcpf(e[j][0]), __builtin_amdgcn_rcpf(e[j][1])}, (f2){bflo(xq[j]), bfhi(xq[j])});
      res[j] = pk2(t[0], t[1]);
    }
    *(GAS u4*)(out + (rt * 64 + tk) * 1024 + ct2 * 128 + c8 * 16) = res;
  }
  if (w < 4) STAMP(11);
  asm volatile("s_waitcnt vmcnt(0)" ::: "memory");
  STAMP(12);
  if (b == PROBE_B && w == PROBE_W) {
    const unsigned until = tt[12] + 8u * (tt[PROBE_I] - tt[0]);
    for (int it = 0; it < (1 << 20); ++it) { u64 t_; asm volatile("s_memrealtime %0\n\ts_waitcnt lgkmcnt(0)" : "=s"(t_)::"memory"); if ((int)((unsigned)t_ - until) >= 0) break; }
  }
}
}

extern "C" void kernel_launch(void* const* d_in, const int* in_sizes, int n_in, void* d_out, int out_size, void* d_ws, size_t ws_size,
                              hipStream_t stream) {
  (void)in_sizes; (void)n_in; (void)out_size; (void)ws_size;
  (void)hipFuncSetAttribute((const void*)h3_geglu_fused, hipFuncAttributeMaxDynamicSharedMemorySize, LDS_BYTES);
  h3_geglu_fused<<<256, 512, LDS_BYTES, stream>>>((const char*)d_in[0], (const char*)d_in[1], (const char*)d_in[2], (const char*)d_in[3],
                                                  (const char*)d_in[4], (const char*)d_in[5], (const char*)d_in[7], (const char*)d_in[8],
                                                  (const char*)d_in[9], (char*)d_out, (char*)d_ws);
}
```

```cpp
#include <hip/hip_runtime.h>
#include <stdint.h>

#define LAS __attribute__((address_space(3)))
#define GAS __attribute__((address_space(1)))
#define DI __device__ __forceinline__
typedef __bf16 bf8 __attribute__((ext_vector_type(8)));
typedef __bf16 bf2 __attribute__((ext_vector_type(2)));
typedef short s4 __attribute__((ext_vector_type(4)));
typedef float f4 __attribute__((ext_vector_type(4)));
typedef float f2 __attribute__((ext_vector_type(2)));
typedef unsigned u4 __attribute__((ext_vector_type(4)));
typedef unsigned u2 __attribute__((ext_vector_type(2)));
typedef unsigned u2a __attribute__((ext_vector_type(2), aligned(4)));
typedef unsigned long long u64;

namespace {
constexpr unsigned WS_EPOCH = 0;
constexpr unsigned WS_FK = 4096;
constexpr unsigned WS_FS = WS_FK + 256 * 128;
constexpr unsigned WS_FH = WS_FS + 256 * 128;
constexpr unsigned WS_FY = WS_FH + 256 * 128;
constexpr unsigned WS_ROW = WS_FY + 256 * 128;
constexpr unsigned WS_COL = WS_ROW + 2048 * 8;
constexpr unsigned WS_KVT = 1u << 20;
constexpr unsigned WS_YT = 3u << 20;
constexpr unsigned WS_H = 5u << 20;
constexpr unsigned WS_PROF = 32u << 20;
constexpr unsigned SLOT = 49152;
constexpr unsigned L_MISC = 3 * SLOT;
constexpr unsigned L_RS = 159744;
constexpr unsigned L_CS = L_RS + 1024;
constexpr unsigned L_CTL = L_CS + 1024;
constexpr unsigned C_LAND = L_CTL, C_CONS = L_CTL + 40, C_BSYNC = L_CTL + 72, C_PRE = L_CTL + 76, C_WSYNC = L_CTL + 80, C_EPI = L_CTL + 84,
                   C_HPUB = L_CTL + 88, C_GO = L_CTL + 92, C_WD = L_CTL + 96, C_CVIN = L_CTL + 104, C_TAG = L_CTL + 108, C_PAIR = L_CTL + 112,
                   C_RD = L_CTL + 128, C_LD = L_CTL + 144, C_HOK = L_CTL + 160, L_TAG = L_CTL + 192;
constexpr unsigned CV_COPY = 4160, CV_UR = 2 * CV_COPY, CV_CH = CV_UR + 4608;
constexpr unsigned L_CV = L_RS - 2 * CV_CH;
static_assert(L_CV >= 131072, "W_down ring");
constexpr unsigned L_YI = L_CV;
constexpr unsigned LDS_BYTES = 163840;
constexpr int SPIN_CAP = 1 << 18;

DI float bflo(unsigned u) { return __uint_as_float(u << 16); }
DI float bfhi(unsigned u) { return __uint_as_float(u & 0xffff0000u); }
DI unsigned pk2(float a, float b) { f2 v = {a, b}; return __builtin_bit_cast(unsigned, __builtin_convertvector(v, bf2)); }
DI float dot2(unsigned a, bf2 b, float c) { return __builtin_amdgcn_fdot2_f32_bf16(__builtin_bit_cast(bf2, a), b, c, false); }
DI float rbf(float a) { return bflo(pk2(a, a)); }
DI f4 mfma(bf8 a, bf8 b, f4 c) { return __builtin_amdgcn_mfma_f32_16x16x32_bf16(a, b, c, 0, 0, 0); }
DI bf8 lds128(unsigned a) { return *(const LAS bf8*)(size_t)a; }
DI u4 lds128u(unsigned a) { return *(const LAS u4*)(size_t)a; }
DI f4 lds128f(unsigned a) { return *(const LAS f4*)(size_t)a; }
DI void sts128(unsigned a, u4 v) { *(LAS u4*)(size_t)a = v; }
DI void sts128f(unsigned a, f4 v) { *(LAS f4*)(size_t)a = v; }
DI bf8 ldtr(unsigned a0, unsigned a1) {
  s4 lo = __builtin_amdgcn_ds_read_tr16_b64_v4i16((LAS s4*)(size_t)a0);
  s4 hi = __builtin_amdgcn_ds_read_tr16_b64_v4i16((LAS s4*)(size_t)a1);
  return __builtin_bit_cast(bf8, __builtin_shufflevector(lo, hi, 0, 1, 2, 3, 4, 5, 6, 7));
}
#define WAITV(n) asm volatile("s_waitcnt vmcnt(%0)" ::"i"(n) : "memory")
#define WAITL(n) asm volatile("s_waitcnt lgkmcnt(%0)" ::"i"(n) : "memory")
#define SB() __builtin_amdgcn_sched_barrier(0)
#define OPAQUE(x) asm volatile("" : "+v"(x))
#define BAR() do { asm volatile("s_waitcnt lgkmcnt(0)" ::: "memory"); __builtin_amdgcn_s_barrier(); asm volatile("" ::: "memory"); } while (0)

DI void glds2(unsigned v0, unsigned v1, const char* sb, unsigned l) {
  asm volatile("s_mov_b32 m0, %3\n\ts_nop 0\n\tglobal_load_lds_dwordx4 %0, %2\n\tglobal_load_lds_dwordx4 %1, %2 offset:1024"
               ::"v"(v0), "v"(v1), "s"(sb), "s"(l) : "memory");
}
DI void glds4(unsigned v0, unsigned v1, unsigned v2, unsigned v3, const char* sb, unsigned l) {
  asm volatile("s_mov_b32 m0, %5\n\ts_nop 0\n\tglobal_load_lds_dwordx4 %0, %4\n\tglobal_load_lds_dwordx4 %1, %4 offset:1024\n\t"
               "global_load_lds_dwordx4 %2, %4 offset:2048\n\tglobal_load_lds_dwordx4 %3, %4 offset:3072"
               ::"v"(v0), "v"(v1), "v"(v2), "v"(v3), "s"(sb), "s"(l) : "memory");
}
DI void flag_set(char* ws, unsigned off, u64 tag) {
  __hip_atomic_store((GAS u64*)(ws + off), tag, __ATOMIC_RELAXED, __HIP_MEMORY_SCOPE_AGENT);
}
DI void poll(const char* ws, unsigned off, u64 tag) {
#pragma unroll 1
  for (int it = 0; it < SPIN_CAP; ++it) {
    u64 x = __hip_atomic_load((GAS u64*)(ws + off), __ATOMIC_RELAXED, __HIP_MEMORY_SCOPE_AGENT);
    if (__all(x == tag)) break;
    __builtin_amdgcn_s_sleep(4);
  }
  asm volatile("" ::: "memory");
}
DI float fadd(float a, float b) { float r; asm("v_add_f32 %0, %1, %2" : "=v"(r) : "v"(a), "v"(b)); return r; }
DI float fsub(float a, float b) { float r; asm("v_sub_f32 %0, %1, %2" : "=v"(r) : "v"(a), "v"(b)); return r; }
DI float fmul(float a, float b) { float r; asm("v_mul_f32 %0, %1, %2" : "=v"(r) : "v"(a), "v"(b)); return r; }
DI float ffma(float a, float b, float c) { float r; asm("v_fma_f32 %0, %1, %2, %3" : "=v"(r) : "v"(a), "v"(b), "v"(c)); return r; }
DI void poll2(const char* ws, unsigned off0, unsigned off1, u64 tag) {
#pragma unroll 1
  for (int it = 0; it < SPIN_CAP; ++it) {
    const u64 x = __hip_atomic_load((GAS u64*)(ws + off0), __ATOMIC_RELAXED, __HIP_MEMORY_SCOPE_AGENT);
    const u64 y = __hip_atomic_load((GAS u64*)(ws + off1), __ATOMIC_RELAXED, __HIP_MEMORY_SCOPE_AGENT);
    if (__all(x == tag && y == tag)) break;
    __builtin_amdgcn_s_sleep(8);
  }
  asm volatile("" ::: "memory");
}
template <int CTRL> DI float dpp_add(float v) {
  return v + __int_as_float(__builtin_amdgcn_update_dpp(0, __float_as_int(v), CTRL, 0xf, 0xf, true));
}
DI float wave_sum(float v) {
  v = dpp_add<0xB1>(v); v = dpp_add<0x4E>(v); v = dpp_add<0x141>(v); v = dpp_add<0x140>(v);
  const int i = __float_as_int(v);
  return (__int_as_float(__builtin_amdgcn_readlane(i, 0)) + __int_as_float(__builtin_amdgcn_readlane(i, 16))) +
         (__int_as_float(__builtin_amdgcn_readlane(i, 32)) + __int_as_float(__builtin_amdgcn_readlane(i, 48)));
}
DI void ctl_add(unsigned addr) {
  asm volatile("s_waitcnt lgkmcnt(0)\n\ts_mov_b64 exec, 1\n\tds_add_u32 %0, %1\n\ts_mov_b64 exec, -1" ::"v"(addr), "v"(1u) : "memory");
}
DI unsigned ctl_get(unsigned addr) {
  unsigned s, t;
  asm volatile("ds_read_b32 %1, %2\n\ts_waitcnt lgkmcnt(0)\n\tv_readfirstlane_b32 %0, %1\n\ts_nop 1" : "=s"(s), "=&v"(t) : "v"(addr) : "memory");
  return s;
}
DI void ctl_spin(unsigned addr, unsigned target) {
#pragma unroll 1
  for (int it = 0; it < SPIN_CAP; ++it)
    if (ctl_get(addr) >= target) break;
}
DI void ctl_wait(unsigned addr, unsigned target) {
#pragma unroll 1
  for (int it = 0; it < SPIN_CAP; ++it) {
    if (ctl_get(addr) >= target) break;
    __builtin_amdgcn_s_sleep(1);
  }
}
#define STAMP(i)
#define CNOW(v)
#define CSTAMP(i)

#define HWIN 4
#define WREG 6
#define SFIRST 0
#define PEEK_AT 5
#define SIDE_SLEEP 0
#define PF_W 1
#define PF_CODE 1
#define PF_L2 0
#define S1_EARLY 1
#define BAR_AT 32
#define KBAR() do { if (g1) BAR(); } while (0)
struct Frag { bf8 x[4]; bf8 w[2][4]; };

__global__ __launch_bounds__(512, 2) void h3_geglu_fused(const char* q, const char* kk, const char* vv, const char* shk, const char* ssk,
                                                         const char* sD, const char* wg, const char* wu, const char* wd, char* out, char* ws) {
  extern __shared__ __attribute__((aligned(16))) char smem[];
  u64 kbase;
  asm volatile("s_getpc_b64 %0" : "=s"(kbase));
  const int lane = threadIdx.x & 63, w = __builtin_amdgcn_readfirstlane(threadIdx.x >> 6) ^ (SFIRST ? 4 : 0), gw = w & 3;
  const int tid = w * 64 + lane, t8 = tid & 255;
  const int b = blockIdx.x, xcd = b & 7, r = b >> 3;
  const bool g1 = r < 28;
  const int l15 = lane & 15, g = lane >> 4;
  const __amdgpu_buffer_rsrc_t wsr = __builtin_amdgcn_make_buffer_rsrc(ws, 0, 16 << 20, 0x00020000);
  const int pnl = r >> 2;
  const int pm = (xcd >> 1) * 4 + (r & 3), pn = (xcd & 1) * 7 + pnl;
  const int rt = xcd * 4 + (r & 3), ct2 = r >> 2;
  auto wslot = [](int i) -> unsigned { const int s = i & 7; return s < 2 ? 2 * SLOT + s * 4096 : s < 5 ? (s - 2) * 4096 : SLOT + (s - 5) * 4096; };
  auto wdstage = [&](unsigned dv0, int kq, int i) {
    glds4(dv0, dv0 + 7168, dv0 + 2 * 7168, dv0 + 3 * 7168, wd + ct2 * 128 + (kq * 14 + i) * 32768, wslot(i) + kq * ((i & 7) < 2 ? 8192 : 12288));
  };
  u64 tag;
  auto tag_get = [&]() {
    const u2 t = *(const volatile LAS u2*)(size_t)L_TAG;
    tag = ((u64)(unsigned)__builtin_amdgcn_readfirstlane(t[1]) << 32) | (unsigned)__builtin_amdgcn_readfirstlane(t[0]);
  };

  u4 xq; u2 st2;
  auto tail_load = [&]() {
    int ln = __builtin_amdgcn_mbcnt_hi(-1, __builtin_amdgcn_mbcnt_lo(-1, 0));
    OPAQUE(ln);
    const int tk = w * 8 + (ln >> 3), c8 = ln & 7;
    xq = *(const GAS u4*)(q + (rt * 64 + tk) * 1024 + ct2 * 128 + c8 * 16);
    st2 = __builtin_amdgcn_raw_buffer_load_b64(wsr, WS_ROW + (rt * 64 + tk) * 8, 0, 16);
  };

  if (w < 4) {
    {
      const int ptt = b >> 3, pct = b & 7;
      const unsigned lk = 2 * SLOT + gw * 5120;
      const unsigned kvo = (lane >> 3) * 1024 + (((lane & 7) ^ ((g & 1) << 2)) << 4);
      const bool top = ptt == 0 && gw == 0;
      const char* kb = kk + (ptt * 64 + gw * 16) * 1024 + pct * 128;
      u2 sw;
      asm volatile("s_mov_b32 m0, %7\n\ts_nop 0\n\tglobal_load_lds_dwordx4 %1, %4\n\tglobal_load_lds_dwordx4 %1, %5 offset:1024\n\t"
                   "global_load_lds_dwordx4 %2, %5 offset:2048\n\tglobal_load_lds_dwordx4 %1, %6 offset:3072\n\ts_add_u32 m0, m0, 4096\n\t"
                   "global_load_dwordx2 %0, %3, %8\n\tglobal_load_lds_dwordx4 %2, %9"
                   : "=&v"(sw)
                   : "v"(kvo), "v"(kvo + 7168), "v"(lane * 8), "s"(top ? kb : kb - 8192), "s"(kb - 1024), "s"(vv + (ptt * 64 + gw * 16) * 1024 + pct * 128 - 3072),
                     "s"(lk), "s"(shk + pct * 512), "s"(vv + (ptt * 64 + gw * 16) * 1024 + pct * 128 + 1024)
                   : "memory", "scc");
      const int qq = l15 >> 2, pp = lane & 3;
      unsigned ra = lk + qq * 128 + (((2 * g + (pp >> 1)) ^ ((qq >> 1) << 2)) << 4) + (pp & 1) * 8;
      OPAQUE(ra);
      asm volatile("s_waitcnt vmcnt(0)" : "+v"(sw)::"memory");
      STAMP(18);
      u2 kr[5], vr[4];
#pragma unroll
      for (int i = 0; i < 5; ++i) kr[i] = __builtin_bit_cast(u2, __builtin_amdgcn_ds_read_tr16_b64_v4i16((LAS s4*)(size_t)(ra + 512 + i * 512)));
#pragma unroll
      for (int i = 0; i < 4; ++i) vr[i] = __builtin_bit_cast(u2, __builtin_amdgcn_ds_read_tr16_b64_v4i16((LAS s4*)(size_t)(ra + 3072 + i * 512)));
      if (top) kr[0] = (u2){0, 0};
      const float w0 = bflo(sw[0]), w1 = bfhi(sw[0]), w2 = bflo(sw[1]), w3 = bfhi(sw[1]);
      float kf[20], u[16];
#pragma unroll
      for (int i = 0; i < 5; ++i) { kf[4 * i] = bflo(kr[i][0]); kf[4 * i + 1] = bfhi(kr[i][0]); kf[4 * i + 2] = bflo(kr[i][1]); kf[4 * i + 3] = bfhi(kr[i][1]); }
      SB();
#pragma unroll
      for (int i = 0; i < 16; ++i) u[i] = fmul(kf[i + 1], w3);
      SB();
#pragma unroll
      for (int i = 0; i < 16; ++i) u[i] = ffma(kf[i + 2], w2, u[i]);
      SB();
#pragma unroll
      for (int i = 0; i < 16; ++i) u[i] = ffma(kf[i + 3], w1, u[i]);
      SB();
#pragma unroll
      for (int i = 0; i < 16; ++i) u[i] = ffma(kf[i + 4], w0, u[i]);
      SB();
#pragma unroll
      for (int i = 0; i < 16; ++i) { const unsigned vw = vr[i >> 2][(i >> 1) & 1]; u[i] = fmul(u[i], (i & 1) ? bfhi(vw) : bflo(vw)); }
      SB();
      const unsigned ad = L_MISC + lane * 128;
      sts128(ad + (((7 - 2 * gw) ^ (lane & 7)) << 4), (u4){pk2(u[7], u[6]), pk2(u[5], u[4]), pk2(u[3], u[2]), pk2(u[1], u[0])});
      sts128(ad + (((6 - 2 * gw) ^ (lane & 7)) << 4), (u4){pk2(u[15], u[14]), pk2(u[13], u[12]), pk2(u[11], u[10]), pk2(u[9], u[8])});
      STAMP(1);
      BAR();
    }
    if (g1) {
      const int qq = l15 >> 2, pp = lane & 3;
      const int wm = w >> 1, wn = w & 1;
      f4 acc[2][4][4];
#pragma unroll
      for (int s = 0; s < 2; ++s)
#pragma unroll
        for (int e = 0; e < 4; ++e)
#pragma unroll
          for (int i = 0; i < 4; ++i) acc[s][e][i] = (f4){0.f, 0.f, 0.f, 0.f};
      unsigned xa[2], wa[4];
      xa[0] = (wm * 64 + l15) * 128 + ((g ^ (l15 >> 1)) << 4); xa[1] = xa[0] ^ 64;
#pragma unroll
      for (int e = 0; e < 4; ++e) wa[e] = (8 * g + qq) * 256 + (((4 * wn + e) ^ (qq | ((g & 1) << 2))) << 5) + pp * 8;
      Frag f0, f1;
      s4 lo_;
      auto ldpart = [&](Frag& f, int ks, int j) {
        const unsigned o = 16384 + ks * 8192;
        if (j == 0) f.x[0] = lds128(xa[ks]);
        if (j >= 1 && j <= 16) {
          const int s = (j - 1) >> 3, e = ((j - 1) >> 1) & 3;
          if (j & 1) lo_ = __builtin_amdgcn_ds_read_tr16_b64_v4i16((LAS s4*)(size_t)(wa[e] + o + s * 16384));
          else {
            const s4 hi = __builtin_amdgcn_ds_read_tr16_b64_v4i16((LAS s4*)(size_t)(wa[e] + o + s * 16384 + 1024));
            f.w[s][e] = __builtin_bit_cast(bf8, __builtin_shufflevector(lo_, hi, 0, 1, 2, 3, 4, 5, 6, 7));
          }
        }
        if (j >= 17 && j < 20) f.x[j - 16] = lds128(xa[ks] + (j - 16) * 2048);
      };
      auto mm1 = [&](const Frag& f, int j) { acc[(j >> 2) & 1][j & 3][j >> 3] = mfma(f.w[(j >> 2) & 1][j & 3], f.x[j >> 3], acc[(j >> 2) & 1][j & 3][j >> 3]); };
      auto second = [&](int st) {
        const int d = (st == 2 || st == 5) ? -2 * (int)SLOT : (int)SLOT;
#pragma unroll
        for (int j = 0; j < 32; ++j) {
          if (j == BAR_AT) { BAR(); SB(); }
          mm1(f0, j);
          if (j < 20) ldpart(f1, 1, j);
          if (j == 20) { xa[0] += d; OPAQUE(xa[0]); xa[1] += d; OPAQUE(xa[1]); }
          if (j == 21) { wa[0] += d; OPAQUE(wa[0]); wa[1] += d; OPAQUE(wa[1]); }
          if (j == 22) { wa[2] += d; OPAQUE(wa[2]); wa[3] += d; OPAQUE(wa[3]); }
          SB();
        }
        if (BAR_AT == 32) { BAR(); SB(); }
      };
      OPAQUE(xa[0]); OPAQUE(xa[1]); OPAQUE(wa[0]); OPAQUE(wa[1]); OPAQUE(wa[2]); OPAQUE(wa[3]);
      STAMP(2); CSTAMP(20);
#pragma unroll
      for (int j = 0; j < 20; ++j) ldpart(f0, 0, j);
      SB();
      second(0);
#pragma unroll 1
      for (int st = 1; st < 8; ++st) {
#pragma unroll
        for (int j = 0; j < 32; ++j) { mm1(f1, j); if (j < 20) ldpart(f0, 0, j); SB(); }
        second(st);
      }
      STAMP(16); CSTAMP(21);
      constexpr float C0 = -2.0f * 0.7978845608028654f * 1.4426950408889634f, C1 = C0 * 0.044715f;
      f4 cs[2][4];
      f2 rs[4];
      int ln1 = __builtin_amdgcn_mbcnt_hi(-1, __builtin_amdgcn_mbcnt_lo(-1, 0));
      OPAQUE(ln1);
      const int lane = ln1, l15 = lane & 15, g = lane >> 4;
#pragma unroll
      for (int j = 0; j < 32; ++j) {
        mm1(f1, j);
        if (j < 8) cs[j >> 2][j & 3] = lds128f(L_CS + ((j >> 2) * 128 + wn * 64 + (j & 3) * 16 + 4 * g) * 4);
        if (j >= 8 && j < 12) rs[j - 8] = *(const LAS f2*)(size_t)(L_RS + (wm * 64 + (j - 8) * 16 + l15) * 8);
        SB();
      }
      STAMP(3);
      const unsigned hso = WS_H + ((pm * 8 + wm * 4) * 56 + pn * 4 + wn * 2) * 1024 + lane * 16;
#pragma unroll
      for (int i = 0; i < 4; ++i) {
        const float m = -rs[i][0], rr = rs[i][1];
        const float k0 = C0 * rr, k1 = C1 * rr * rr * rr, i2 = __builtin_amdgcn_rcpf(rr * rr);
        const f2 m2 = {m, m}, k02 = {k0, k0}, k12 = {k1, k1}, i22 = {i2, i2};
        f2 a[8], c[8], z[8];
        SB();
#pragma unroll
        for (int j = 0; j < 8; ++j) {
          const int e = j >> 1, h = (j & 1) * 2;
          a[j] = __builtin_elementwise_fma(m2, (f2){cs[0][e][h], cs[0][e][h + 1]}, (f2){acc[0][e][i][h], acc[0][e][i][h + 1]});
        }
        SB();
#pragma unroll
        for (int j = 0; j < 8; ++j) z[j] = a[j] * a[j];
        SB();
#pragma unroll
        for (int j = 0; j < 8; ++j) z[j] = __builtin_elementwise_fma(z[j], k12, k02);
        SB();
#pragma unroll
        for (int j = 0; j < 8; ++j) z[j] = z[j] * a[j];
        SB();
#pragma unroll
        for (int j = 0; j < 8; ++j) {
          const int e = j >> 1, h = (j & 1) * 2;
          z[j] = (f2){__builtin_amdgcn_exp2f(z[j][0]), __builtin_amdgcn_exp2f(z[j][1])};
          c[j] = __builtin_elementwise_fma(m2, (f2){cs[1][e][h], cs[1][e][h + 1]}, (f2){acc[1][e][i][h], acc[1][e][i][h + 1]});
          SB();
        }
#pragma unroll
        for (int j = 0; j < 8; ++j) z[j] = __builtin_elementwise_fma(z[j], i22, i22);
        SB();
#pragma unroll
        for (int j = 0; j < 8; ++j) {
          z[j] = (f2){__builtin_amdgcn_rcpf(z[j][0]), __builtin_amdgcn_rcpf(z[j][1])};
          c[j] = c[j] * a[j];
          SB();
        }
#pragma unroll
        for (int j = 0; j < 8; ++j) c[j] = c[j] * z[j];
        SB();
        u4 o0, o1;
#pragma unroll
        for (int j = 0; j < 4; ++j) { o0[j] = pk2(c[j][0], c[j][1]); o1[j] = pk2(c[4 + j][0], c[4 + j][1]); }
        __builtin_amdgcn_raw_buffer_store_b128(o0, wsr, hso + i * 56 * 1024, 0, 16);
        __builtin_amdgcn_raw_buffer_store_b128(o1, wsr, hso + i * 56 * 1024 + 1024, 0, 16);
      }
      SB();
      ctl_add(C_EPI);
      tag_get();
      STAMP(4);
    } else {
      const unsigned o = ((xcd * 4 + r - 28) * 448 + gw * 112 + lane) * 128;
      if (PF_W) asm volatile("s_mov_b32 m0, %3\n\ts_nop 0\n\tglobal_load_lds_dword %0, %2\n\tglobal_load_lds_dword %1, %2"
                             ::"v"(o), "v"(o + (lane < 48 ? 8192 : 0)), "s"(wd), "s"(131072 + gw * 256) : "memory");
      if (PF_CODE && lane < 15)
        asm volatile("s_mov_b32 m0, %2\n\ts_nop 0\n\tglobal_load_lds_dword %0, %1" ::"v"((((r - 28) * 4 + gw) * 15 + lane) * 128), "s"(kbase), "s"(131072 + gw * 256) : "memory");
      if (PF_L2) {
#pragma unroll 1
        for (int j = (r - 28) * 4 + gw; j < PF_L2 * 16; j += 16) {
          const unsigned o2 = ((((j >> 2) & 3) * 14 + (j >> 4)) * 32 + (j & 3) * 8 + (lane >> 3)) * 1024 + (lane & 7) * 128;
          asm volatile("s_waitcnt vmcnt(0)\n\ts_mov_b32 m0, %2\n\ts_nop 0\n\tglobal_load_lds_dword %0, %1" ::"v"(o2), "s"(wd), "s"(131072 + gw * 256) : "memory");
        }
      }
      WAITV(0);
    }
    int ln2 = __builtin_amdgcn_mbcnt_hi(-1, __builtin_amdgcn_mbcnt_lo(-1, 0));
    OPAQUE(ln2);
    {
      const int lane = ln2, l15 = lane & 15, g = lane >> 4, qq = l15 >> 2, pp = lane & 3;
      const unsigned dv0 = (lane >> 3) * 1024 + (((lane & 7) ^ (g << 1)) * 16);
      f4 acc[4][4];
#pragma unroll
      for (int t = 0; t < 4; ++t)
#pragma unroll
        for (int i = 0; i < 4; ++i) acc[t][i] = (f4){0.f, 0.f, 0.f, 0.f};
      const unsigned da = (4 * g + qq) * 128 + (((2 * g + (qq >> 1)) & 3) << 5) + pp * 8;
      unsigned dlo[4];
#pragma unroll
      for (int t = 0; t < 4; ++t) { dlo[t] = (da ^ (t * 32)) + gw * 12288; OPAQUE(dlo[t]); }
      const unsigned dh = 2 * SLOT - gw * 4096;
      constexpr int WR = 6, HW = HWIN;
      bf8 wf[WR][4];
      auto waddr = [&](int i, int t) -> unsigned {
        if ((i & 7) >= 2) return dlo[t] + wslot(i);
        unsigned d_ = dh;
        asm volatile("" : "+s"(d_));
        unsigned a_ = dlo[t] + d_;
        OPAQUE(a_);
        return a_ + (i & 1) * 4096;
      };
      __builtin_amdgcn_s_setprio(2);
      if (g1) {
        WAITV(0);
        if (lane == 0 && __hip_atomic_fetch_add((LAS unsigned*)(size_t)C_HPUB, 1u, __ATOMIC_RELAXED, __HIP_MEMORY_SCOPE_WORKGROUP) == 3)
          flag_set(ws, WS_FH + (pm * 16 + pn) * 128, tag);
      }
      STAMP(5);
      ctl_wait(C_WD + (gw >> 1) * 4, 2);
      if (!g1) tag_get();
#pragma unroll
      for (int t = 0; t < WR * 4; ++t) { const unsigned a_ = waddr(t >> 2, t & 3); wf[t >> 2][t & 3] = ldtr(a_, a_ + 2048); }
      WAITL(0);
      STAMP(17);
#pragma unroll
      for (int i = 8; i < 14; ++i) wdstage(dv0, gw, i);
      __builtin_amdgcn_s_setprio(0);
      STAMP(9);
      poll(ws, WS_FH + ((rt >> 1) * 16 + ((gw * 7) >> 1) + (lane & 3)) * 128, tag);
      STAMP(6);
      u4 hf[HW][4];
      unsigned ho[4];
#pragma unroll
      for (int t = 0; t < 4; ++t) ho[t] = WS_H + ((rt * 4 + t) * 56 + gw * 14) * 1024 + lane * 16;
      auto ldh1 = [&](int i, int t) { hf[i % HW][t] = __builtin_amdgcn_raw_buffer_load_b128(wsr, ho[t] + (i & 3) * 1024, (i >> 2) * 4096, 16); };
#pragma unroll
      for (int i = 0; i < HW * 4; ++i) { ldh1(i >> 2, i & 3); SB(); }
      s4 lo_;
      unsigned wa_;
#pragma unroll
      for (int i = 0; i < 14; ++i) {
        const int nw = i + WR - 1;
        if (i == 11) { tail_load(); SB(); }
#pragma unroll
        for (int j = 0; j < 16; ++j) {
          const int ti = j >> 2, t = j & 3;
          acc[t][ti] = mfma(wf[i % WR][t], __builtin_bit_cast(bf8, hf[i % HW][ti]), acc[t][ti]);
          if (i >= 1 && nw < 14 && t != 3 && (j - ti) < 8) {
            const int e = j - ti;
            if (!(e & 1)) wa_ = waddr(nw, e >> 1);
            const s4 v_ = __builtin_amdgcn_ds_read_tr16_b64_v4i16((LAS s4*)(size_t)(wa_ + (e & 1) * 2048));
            if (e & 1) wf[nw % WR][e >> 1] = __builtin_bit_cast(bf8, __builtin_shufflevector(lo_, v_, 0, 1, 2, 3, 4, 5, 6, 7)); else lo_ = v_;
          }
          if (t == 3 && i + HW < 14) ldh1(i + HW, ti);
          SB();
        }
        if (i == 0) STAMP(19);
      }
      STAMP(7);
#pragma unroll
      for (int ti = 0; ti < 4; ++ti)
#pragma unroll
        for (int t = 0; t < 4; ++t)
          sts128f((ti >> 1) * SLOT + gw * 12288 + ((ti & 1) * 16 + l15) * 256 + (((4 * t + g) ^ (l15 & 7)) << 4), acc[t][ti]);
    }
    STAMP(8);
  } else {
    const int t7 = tid & 127, bw = w & 1;
    unsigned dcr;
    if (w < 6) {
      unsigned xv[4], wv[4];
#pragma unroll
      for (int p = 0; p < 4; ++p) {
        xv[p] = (p * 8 + (lane >> 3)) * 1024 + (((lane & 7) ^ (((p & 1) << 2) | g)) << 4) - p * 1024;
        wv[p] = (p * 4 + g) * 3584 + ((l15 ^ ((g | ((p >> 1) << 2)) << 1)) << 4) - p * 1024;
      }
      auto quarter = [&](int st, int qt) {
        const unsigned l = (st % 3) * SLOT + qt * 4096;
        glds4(xv[0], xv[1], xv[2], xv[3], q + pm * 131072 + qt * 32768 + st * 128, l);
        glds4(wv[0], wv[1], wv[2], wv[3], wg + pn * 256 + (st * 64 + qt * 16) * 3584, l + 16384);
        glds4(wv[0], wv[1], wv[2], wv[3], wu + pn * 256 + (st * 64 + qt * 16) * 3584, l + 32768);
      };
      auto stage = [&](int st) { quarter(st, 2 * bw); quarter(st, 2 * bw + 1); };
      const unsigned dv0 = (lane >> 3) * 1024 + (((lane & 7) ^ (g << 1)) * 16);
      if (g1) { stage(0); if (S1_EARLY) { quarter(1, 2 * bw); WAITV(12); } else WAITV(0); }
      STAMP(1);
      BAR();
      if (g1) { if (!S1_EARLY) quarter(1, 2 * bw); quarter(1, 2 * bw + 1); stage(2); WAITV(24); }
      KBAR();
#pragma unroll 1
      for (int st = 3; st < 8; ++st) {
        if (g1) { stage(st); WAITV(24); }
        KBAR();
      }
      STAMP(13);
      if (!g1) ctl_wait(C_TAG, 2);
      asm volatile("global_load_dword %0, %1, %2" : "=&v"(dcr) : "v"(0u), "s"(sD + b * 4) : "memory");
#pragma unroll
      for (int i = 0; i < 4; ++i) wdstage(dv0, bw * 2 + (i & 1), i >> 1);
      WAITV(17);
      KBAR();
      STAMP(3);
#pragma unroll
      for (int i = 0; i < 6; ++i) wdstage(dv0, bw * 2 + (i & 1), 2 + (i >> 1));
      KBAR();
      STAMP(19);
      tag_get();
      asm volatile("s_waitcnt vmcnt(32)" : "+v"(dcr)::"memory");
#pragma unroll
      for (int i = 0; i < 6; ++i) wdstage(dv0, bw * 2 + (i & 1), 5 + (i >> 1));
      WAITV(24);
      ctl_add(C_WD + bw * 4);
      STAMP(2);
      WAITV(0);
      ctl_add(C_WD + bw * 4);
      STAMP(4);
    } else {
      if (t7 < 48) *(LAS unsigned*)(size_t)(L_CTL + t7 * 4) = 0;
      const int ptt = b >> 3, pct = b & 7;
      const int rgrp = (xcd >> 1) * 64 + (xcd & 1) * 32 + r;
      const int cst = 2 * ((r + pnl) & 3) + ((xcd >> 1) & 1);
      const unsigned hoff = (2 * b + bw) * 4096 + lane * 16;
      u4 xr[4];
      auto rows_load = [&]() {
        asm volatile("global_load_dwordx4 %0, %4, %5\n\tglobal_load_dwordx4 %1, %4, %5 offset:1024\n\tglobal_load_dwordx4 %2, %4, %5 offset:2048\n\t"
                     "global_load_dwordx4 %3, %4, %5 offset:3072"
                     : "=&v"(xr[0]), "=&v"(xr[1]), "=&v"(xr[2]), "=&v"(xr[3]) : "v"(lane * 16), "s"(q + (rgrp * 8 + (w & 1) * 4) * 1024) : "memory");
      };
      auto rows_stat = [&]() {
        asm volatile("" : "+v"(xr[0]), "+v"(xr[1]), "+v"(xr[2]), "+v"(xr[3])::"memory");
        float e[4][8], mu[4], va[4];
#pragma unroll
        for (int i = 0; i < 4; ++i) {
#pragma unroll
          for (int j = 0; j < 4; ++j) { e[i][2 * j] = bflo(xr[i][j]); e[i][2 * j + 1] = bfhi(xr[i][j]); }
          mu[i] = fadd(fadd(fadd(e[i][0], e[i][1]), fadd(e[i][2], e[i][3])), fadd(fadd(e[i][4], e[i][5]), fadd(e[i][6], e[i][7])));
        }
        auto fold4 = [&](const float (&v)[4]) {
          const auto s0 = __builtin_amdgcn_permlane16_swap(__float_as_uint(v[0]), __float_as_uint(v[1]), false, false);
          const auto s1 = __builtin_amdgcn_permlane16_swap(__float_as_uint(v[2]), __float_as_uint(v[3]), false, false);
          const float p0 = fadd(__uint_as_float(s0[0]), __uint_as_float(s0[1])), p1 = fadd(__uint_as_float(s1[0]), __uint_as_float(s1[1]));
          const auto s2 = __builtin_amdgcn_permlane32_swap(__float_as_uint(p0), __float_as_uint(p1), false, false);
          float t = fadd(__uint_as_float(s2[0]), __uint_as_float(s2[1]));
          t = dpp_add<0xB1>(t); t = dpp_add<0x4E>(t); t = dpp_add<0x141>(t); t = dpp_add<0x140>(t);
          return t * (1.0f / 512.0f);
        };
        const float mg = fold4(mu);
#pragma unroll
        for (int i = 0; i < 4; ++i) {
          const float m = __int_as_float(__builtin_amdgcn_readlane(__float_as_int(mg), 16 * i));
          float s = 0.f;
#pragma unroll
          for (int j = 0; j < 8; ++j) { const float d = e[i][j] - m; s = ffma(d, d, s); }
          va[i] = s;
        }
        const float vg = fold4(va);
        const u2 o = {__float_as_uint(rbf(mg)), __float_as_uint(__builtin_amdgcn_rcpf(rbf(__builtin_amdgcn_sqrtf(rbf(rbf(vg) + 1e-5f)))))};
        if (l15 == 0) __builtin_amdgcn_raw_buffer_store_b64(o, wsr, WS_ROW + (rgrp * 8 + (w & 1) * 4 + g) * 8, 0, 16);
        asm volatile("" ::: "memory");
      };

      u4 wsh[8], hc[4], uc[4], c1[4]; unsigned hp[4];
      {
        const char* wb = ((xcd >> 2) ? wu : wg) + (g1 ? pn * 256 + (cst * 64 + bw * 32) * 3584 : 0);
        const unsigned o = g * 3584 + l15 * 16;
        if (SIDE_SLEEP) __builtin_amdgcn_s_sleep(SIDE_SLEEP);
        asm volatile("global_load_dwordx4 %0, %8, %12\n\tglobal_load_dwordx4 %1, %9, %12\n\tglobal_load_dwordx4 %2, %10, %12\n\tglobal_load_dwordx4 %3, %11, %12\n\t"
                     "global_load_dwordx4 %4, %8, %13\n\tglobal_load_dwordx4 %5, %9, %13\n\tglobal_load_dwordx4 %6, %10, %13\n\tglobal_load_dwordx4 %7, %11, %13"
                     : "=&v"(wsh[0]), "=&v"(wsh[1]), "=&v"(wsh[2]), "=&v"(wsh[3]), "=&v"(wsh[4]), "=&v"(wsh[5]), "=&v"(wsh[6]), "=&v"(wsh[7])
                     : "v"(o), "v"(o + 4 * 3584), "v"(o + 8 * 3584), "v"(o + 12 * 3584), "s"(wb), "s"(wb + 16 * 3584) : "memory");
        rows_load();
      }
      asm volatile("s_load_dwordx2 %0, %1, 0x0\n\ts_waitcnt lgkmcnt(0)" : "=s"(tag) : "s"(ws) : "memory");
      tag += 1;
      *(LAS u64*)(size_t)L_TAG = tag;
      BAR();
#pragma unroll
      for (int z = 0; z < 4; ++z) {
        const int id = t7 + 128 * z, c2 = id >> 3, ch = id & 7;
        const u4 t = lds128u(L_MISC + c2 * 128 + ((ch ^ (c2 & 7)) << 4));
        __builtin_amdgcn_raw_buffer_store_b128(t, wsr, WS_KVT + (pct * 64 + c2) * 4096 + 3968 - ptt * 128 + ch * 16, 0, 16);
      }
      asm volatile("s_waitcnt vmcnt(4)" : "+v"(wsh[0]), "+v"(wsh[1]), "+v"(wsh[2]), "+v"(wsh[3]), "+v"(wsh[4]), "+v"(wsh[5]), "+v"(wsh[6]), "+v"(wsh[7])::"memory");
      STAMP(17);
      if (g1) {
        float cacc[8];
#pragma unroll
        for (int j = 0; j < 4; ++j) {
          cacc[2 * j] = fadd(fadd(fadd(bflo(wsh[0][j]), bflo(wsh[1][j])), fadd(bflo(wsh[2][j]), bflo(wsh[3][j]))),
                             fadd(fadd(bflo(wsh[4][j]), bflo(wsh[5][j])), fadd(bflo(wsh[6][j]), bflo(wsh[7][j]))));
          cacc[2 * j + 1] = fadd(fadd(fadd(bfhi(wsh[0][j]), bfhi(wsh[1][j])), fadd(bfhi(wsh[2][j]), bfhi(wsh[3][j]))),
                                 fadd(fadd(bfhi(wsh[4][j]), bfhi(wsh[5][j])), fadd(bfhi(wsh[6][j]), bfhi(wsh[7][j]))));
        }
        float c4[4], c2[2];
#pragma unroll
        for (int j = 0; j < 4; ++j) {
          const auto s2 = __builtin_amdgcn_permlane16_swap(__float_as_uint(cacc[j]), __float_as_uint(cacc[4 + j]), false, false);
          c4[j] = fadd(__uint_as_float(s2[0]), __uint_as_float(s2[1]));
        }
#pragma unroll
        for (int j = 0; j < 2; ++j) {
          const auto s2 = __builtin_amdgcn_permlane32_swap(__float_as_uint(c4[j]), __float_as_uint(c4[2 + j]), false, false);
          c2[j] = fadd(__uint_as_float(s2[0]), __uint_as_float(s2[1]));
        }
        __builtin_amdgcn_raw_buffer_store_b64((u2){__float_as_uint(c2[0]), __float_as_uint(c2[1])}, wsr,
                                              WS_COL + ((pn * 16 + cst * 2 + bw) * 256 + (xcd >> 2) * 128 + l15 * 8 + (g & 1) * 4 + (g >> 1) * 2) * 4, 0, 16);
      }
      STAMP(10);
      KBAR();
      rows_stat();
      STAMP(18);
      asm volatile("global_load_dwordx4 %0, %9, %11\n\tglobal_load_dwordx4 %1, %9, %11 offset:1024\n\tglobal_load_dwordx4 %2, %9, %11 offset:2048\n\t"
                   "global_load_dwordx4 %3, %9, %11 offset:3072\n\tglobal_load_dword %4, %10, %11\n\tglobal_load_dword %5, %9, %11 offset:1020\n\t"
                   "global_load_dword %6, %9, %11 offset:2044\n\tglobal_load_dword %7, %9, %11 offset:3068\n\tglobal_load_dword %8, %12, %13"
                   : "=&v"(hc[0]), "=&v"(hc[1]), "=&v"(hc[2]), "=&v"(hc[3]), "=&v"(hp[0]), "=&v"(hp[1]), "=&v"(hp[2]), "=&v"(hp[3]), "=&v"(dcr)
                   : "v"(hoff), "v"(lane ? hoff - 4 : hoff), "s"(ssk), "v"(0u), "s"(sD + b * 4) : "memory");
      KBAR();
      WAITV(9);
      KBAR();
      if (!g1) { ctl_add(C_WSYNC); ctl_spin(C_WSYNC, 2); }
      if (tid == 384 || tid == 385) flag_set(ws, tid == 384 ? WS_FS + rgrp * 128 : WS_FK + (pct * 32 + ptt) * 128, tag);
      STAMP(20);
      unsigned fo = lane < 32 || !g1 ? WS_FK + ((b >> 5) * 32 + (lane & 31)) * 128
                                     : WS_FS + (lane < 48 ? pm * 16 + l15 : (l15 >> 2) * 64 + (xcd & 1) * 32 + (l15 & 3) + 4 * pnl) * 128;
      unsigned fo2 = WS_FS + (rt * 8 + (lane & 7)) * 128;
      OPAQUE(fo); OPAQUE(fo2);
      KBAR();
      asm volatile("s_waitcnt vmcnt(0)" : "+v"(hc[0]), "+v"(hc[1]), "+v"(hc[2]), "+v"(hc[3]), "+v"(hp[0]), "+v"(hp[1]), "+v"(hp[2]), "+v"(hp[3]), "+v"(dcr)::"memory");
      if (lane == 0) hp[0] = 0;
#pragma unroll
      for (int z = 0; z < 4; ++z) {
        c1[z] = (u4){__builtin_amdgcn_alignbit(hc[z][0], hp[z], 16), __builtin_amdgcn_alignbit(hc[z][1], hc[z][0], 16),
                     __builtin_amdgcn_alignbit(hc[z][2], hc[z][1], 16), __builtin_amdgcn_alignbit(hc[z][3], hc[z][2], 16)};
        OPAQUE(c1[z]);
      }
      u64 p0, p1;
      if (!g1) __builtin_amdgcn_s_sleep(70);
      if (PEEK_AT == 5) KBAR();
      asm volatile("global_load_dwordx2 %0, %2, %4 sc1\n\tglobal_load_dwordx2 %1, %3, %4 sc1" : "=&v"(p0), "=&v"(p1) : "v"(fo), "v"(fo2), "s"(ws) : "memory");
      KBAR();
      STAMP(23);
      asm volatile("s_waitcnt vmcnt(0)" : "+v"(p0), "+v"(p1)::"memory");
      if (!__all(p0 == tag && p1 == tag)) poll2(ws, fo, fo2, tag);
      if (!g1) ctl_add(C_TAG);
      STAMP(21);
      u2 rsv, csv[16];
      {
        const unsigned co = WS_COL + (g1 ? pn : 0) * 16384 + t7 * 8;
#define LD4(o, vreg) asm volatile("global_load_dwordx2 %0, %4, %5 sc1\n\tglobal_load_dwordx2 %1, %4, %5 offset:1024 sc1\n\t" \
                                  "global_load_dwordx2 %2, %4, %5 offset:2048 sc1\n\tglobal_load_dwordx2 %3, %4, %5 offset:3072 sc1" \
                                  : "=&v"(csv[o]), "=&v"(csv[o + 1]), "=&v"(csv[o + 2]), "=&v"(csv[o + 3]) : "v"(vreg), "s"(ws) : "memory")
        LD4(0, co); LD4(4, co + 4096); LD4(8, co + 8192); LD4(12, co + 12288);
        asm volatile("global_load_dwordx2 %0, %1, %2 sc1" : "=&v"(rsv) : "v"(WS_ROW + (g1 ? pm : 0) * 1024 + t7 * 8), "s"(ws) : "memory");
      }
      asm volatile("global_load_dwordx4 %0, %4, %5 sc1\n\tglobal_load_dwordx4 %1, %4, %5 offset:1024 sc1\n\tglobal_load_dwordx4 %2, %4, %5 offset:2048 sc1\n\t"
                   "global_load_dwordx4 %3, %4, %5 offset:3072 sc1" : "=&v"(uc[0]), "=&v"(uc[1]), "=&v"(uc[2]), "=&v"(uc[3]) : "v"(WS_KVT + hoff), "s"(ws) : "memory");
      if (PEEK_AT == 4) KBAR();
      const unsigned base = L_CV + bw * CV_CH;
#pragma unroll
      for (int z = 0; z < 4; ++z) {
        sts128(base + 32 + lane * 16 + z * 1024, hc[z]);
        sts128(base + 32 + lane * 16 + z * 1024 + CV_COPY, c1[z]);
      }
      if (lane < 4) sts128(base + (lane >> 1) * CV_COPY + (lane & 1) * 16, (u4){0, 0, 0, 0});
      if (lane >= 32) sts128(base + CV_UR + 4096 + (lane - 32) * 16, (u4){0, 0, 0, 0});
      STAMP(14);
      KBAR();
      asm volatile("s_waitcnt vmcnt(4)" : "+v"(rsv), "+v"(csv[0]), "+v"(csv[1]), "+v"(csv[2]), "+v"(csv[3]), "+v"(csv[4]), "+v"(csv[5]), "+v"(csv[6]), "+v"(csv[7]),
                   "+v"(csv[8]), "+v"(csv[9]), "+v"(csv[10]), "+v"(csv[11]), "+v"(csv[12]), "+v"(csv[13]), "+v"(csv[14]), "+v"(csv[15])::"memory");
      {
        *(LAS u2*)(size_t)(L_RS + t7 * 8) = rsv;
        float s0[4], s1[4];
#pragma unroll
        for (int i = 0; i < 4; ++i) {
          s0[i] = fadd(fadd(__uint_as_float(csv[i][0]), __uint_as_float(csv[4 + i][0])), fadd(__uint_as_float(csv[8 + i][0]), __uint_as_float(csv[12 + i][0])));
          s1[i] = fadd(fadd(__uint_as_float(csv[i][1]), __uint_as_float(csv[4 + i][1])), fadd(__uint_as_float(csv[8 + i][1]), __uint_as_float(csv[12 + i][1])));
        }
        *(LAS u2*)(size_t)(L_CS + t7 * 8) = (u2){__float_as_uint(fadd(fadd(s0[0], s0[1]), fadd(s0[2], s0[3]))), __float_as_uint(fadd(fadd(s1[0], s1[1]), fadd(s1[2], s1[3])))};
      }
      STAMP(16);
      KBAR();
      if (b == 255 && bw == 1) {
#pragma unroll 1
        for (int z = 0; z < 4; ++z) poll(ws, WS_FS + (z * 64 + lane) * 128, tag);
        if (lane == 0) flag_set(ws, WS_EPOCH, tag);
      }
      asm volatile("s_waitcnt vmcnt(0)" : "+v"(uc[0]), "+v"(uc[1]), "+v"(uc[2]), "+v"(uc[3])::"memory");
#pragma unroll
      for (int z = 0; z < 4; ++z) sts128(base + CV_UR + lane * 16 + z * 1024, uc[z]);
      ctl_add(C_CVIN);
      STAMP(15);
    }
    ctl_wait(C_CVIN, 2);
    const int ch = gw >> 1, half = gw & 1;
    const unsigned base = L_CV + ch * CV_CH;
    const unsigned o = 8 * g + l15 + 1, kap = o & 1;
    unsigned aa = base + kap * CV_COPY + 2 * (o + kap) + half * 256, ab = aa + 8;
    unsigned ba = base + CV_UR + 2 * (2032 - 16 * l15 + 8 * g) - 3584 + half * 256;
    OPAQUE(aa); OPAQUE(ab); OPAQUE(ba);
    f4 y[8];
#pragma unroll
    for (int n = 0; n < 8; ++n) y[n] = (f4){0.f, 0.f, 0.f, 0.f};
    bf8 A[4], B[8];
    auto lda = [&](int qi) {
      const int off = (qi & 7) * 512 + (qi >> 3) * 64;
      const u2a lo = *(const LAS u2a*)(size_t)(aa + off), hi = *(const LAS u2a*)(size_t)(ab + off);
      A[qi & 3] = __builtin_bit_cast(bf8, (u4){lo[0], lo[1], hi[0], hi[1]});
    };
#pragma unroll
    for (int i = 0; i < 8; ++i) { B[7 - i] = lds128(ba + i * 512); if (i < 3) lda(i); }
    SB();
    if (g1) ctl_spin(C_EPI, 4);
    STAMP(5);
    __builtin_amdgcn_s_setprio(1);
#pragma unroll 1
    for (int rho = 0; rho < 4; ++rho) {
#pragma unroll
      for (int qi = 0; qi < 8; ++qi) {
#pragma unroll
        for (int p = 7 - qi; p >= 0; --p) {
          y[qi + p] = mfma(A[qi & 3], B[p], y[qi + p]);
          if (p == 7 - qi) { lda(qi + 3); B[p] = lds128(ba + (7 - p) * 512 + 64); }
          SB();
        }
      }
      aa += 64; ab += 64; ba += 64;
      OPAQUE(aa); OPAQUE(ab); OPAQUE(ba);
    }
    STAMP(6);
    ctl_add(C_PAIR + ch * 4); ctl_spin(C_PAIR + ch * 4, 2);
    const unsigned ex0 = base + half * 4096, ex1 = base + (half ^ 1) * 4096;
#pragma unroll
    for (int n = 0; n < 4; ++n) {
      if (half) { const f4 t = y[n]; y[n] = y[4 + n]; y[4 + n] = t; }
      sts128f(ex0 + n * 1024 + lane * 16, y[4 + n]);
    }
    u2 uu[4];
#pragma unroll
    for (int n = 0; n < 4; ++n) uu[n] = *(const LAS u2*)(size_t)(base + CV_UR + 2 * (2044 - (256 * (4 * half + n) + 16 * l15 + 4 * g)));
    ctl_add(C_PAIR + ch * 4);
    const float dc = ch ? bfhi(dcr) : bflo(dcr);
#pragma unroll
    for (int n = 0; n < 4; ++n) {
      y[n][0] = ffma(bfhi(uu[n][1]), dc, y[n][0]); y[n][1] = ffma(bflo(uu[n][1]), dc, y[n][1]);
      y[n][2] = ffma(bfhi(uu[n][0]), dc, y[n][2]); y[n][3] = ffma(bflo(uu[n][0]), dc, y[n][3]);
    }
    ctl_spin(C_PAIR + ch * 4, 4);
    STAMP(11);
    u2 pk[4];
#pragma unroll
    for (int n = 0; n < 4; ++n) {
      const f4 t = lds128f(ex1 + n * 1024 + lane * 16);
      pk[n] = (u2){pk2(fadd(t[0], y[n][0]), fadd(t[1], y[n][1])), pk2(fadd(t[2], y[n][2]), fadd(t[3], y[n][3]))};
    }
#pragma unroll
    for (int n = 0; n < 4; n += 2) {
      const auto s0 = __builtin_amdgcn_permlane16_swap(pk[n][0], pk[n + 1][0], false, false);
      const auto s1 = __builtin_amdgcn_permlane16_swap(pk[n][1], pk[n + 1][1], false, false);
      const u4 o4 = {s0[0], s1[0], s0[1], s1[1]};
      __builtin_amdgcn_raw_buffer_store_b128(o4, wsr, WS_YT + (2 * b + ch) * 4096 + 2 * (256 * (4 * half + n + (g & 1)) + 16 * l15 + 8 * (g >> 1)), 0, 16);
    }
    STAMP(22);
    WAITV(0);
    ctl_add(C_BSYNC);
    ctl_spin(C_BSYNC, 4);
    if (tid == 256) flag_set(ws, WS_FY + b * 128, tag);
    STAMP(7);
    tail_load();
    if (gw == 3) {
      poll(ws, WS_FY + (ct2 * 32 + (lane & 31)) * 128, tag);
      ctl_add(C_GO);
    } else ctl_wait(C_GO, 1);
    STAMP(8);
    u4 yq[2];
#pragma unroll
    for (int z = 0; z < 2; ++z) {
      const int id = t8 + 256 * z;
      yq[z] = __builtin_amdgcn_raw_buffer_load_b128(wsr, WS_YT + (ct2 * 64 + (id >> 3)) * 4096 + rt * 128 + (id & 7) * 16, 0, 16);
    }
#pragma unroll
    for (int z = 0; z < 2; ++z) {
      const int id = t8 + 256 * z, c = id >> 3;
      sts128(L_YI + c * 128 + (((id & 7) ^ (c >> 3)) << 4), yq[z]);
    }
    __builtin_amdgcn_s_setprio(0);
    STAMP(9);
  }
  f2 xn[4];
#pragma unroll
  for (int j = 0; j < 4; ++j) {
    const float m = __uint_as_float(st2[0]), rr = __uint_as_float(st2[1]);
    const unsigned d = pk2(bflo(xq[j]) - m, bfhi(xq[j]) - m);
    const unsigned n = pk2(bflo(d) * rr, bfhi(d) * rr);
    xn[j] = (f2){bflo(n), bfhi(n)};
    OPAQUE(xn[j]);
  }
  BAR();
  if (w < 4) STAMP(10);
  {
    int ln3 = __builtin_amdgcn_mbcnt_hi(-1, __builtin_amdgcn_mbcnt_lo(-1, 0));
    OPAQUE(ln3);
    const int tk = w * 8 + (ln3 >> 3), c8 = ln3 & 7;
    const unsigned pb = (w >> 2) * SLOT + (tk & 31) * 256;
    f4 o0 = lds128f(pb + (((2 * c8) ^ (tk & 7)) << 4)), o1 = lds128f(pb + (((2 * c8 + 1) ^ (tk & 7)) << 4));
#pragma unroll
    for (int k4 = 1; k4 < 4; ++k4) {
      o0 += lds128f(pb + k4 * 12288 + (((2 * c8) ^ (tk & 7)) << 4));
      o1 += lds128f(pb + k4 * 12288 + (((2 * c8 + 1) ^ (tk & 7)) << 4));
    }
    const unsigned ya = L_YI + c8 * 1024 + ((w ^ c8) << 4) + (tk & 7) * 2;
    unsigned yv[8];
#pragma unroll
    for (int j = 0; j < 8; ++j) yv[j] = *(const LAS unsigned short*)(size_t)(ya + j * 128);
    f2 o[4] = {{o0[0], o0[1]}, {o0[2], o0[3]}, {o1[0], o1[1]}, {o1[2], o1[3]}}, e[4], sg[4];
#pragma unroll
    for (int j = 0; j < 4; ++j) {
      const f2 z = o[j] * -1.4426950408889634f;
      e[j] = (f2){__builtin_amdgcn_exp2f(z[0]), __builtin_amdgcn_exp2f(z[1])} + 1.0f;
      sg[j] = xn[j] * (f2){bflo(yv[2 * j]), bflo(yv[2 * j + 1])} * o[j];
    }
    u4 res;
#pragma unroll
    for (int j = 0; j < 4; ++j) {
      const f2 t = __builtin_elementwise_fma(sg[j], (f2){__builtin_amdgcn_rcpf(e[j][0]), __builtin_amdgcn_rcpf(e[j][1])}, (f2){bflo(xq[j]), bfhi(xq[j])});
      res[j] = pk2(t[0], t[1]);
    }
    *(GAS u4*)(out + (rt * 64 + tk) * 1024 + ct2 * 128 + c8 * 16) = res;
  }
  if (w < 4) STAMP(11);
}
}

extern "C" void kernel_launch(void* const* d_in, const int* in_sizes, int n_in, void* d_out, int out_size, void* d_ws, size_t ws_size,
                              hipStream_t stream) {
  (void)in_sizes; (void)n_in; (void)out_size; (void)ws_size;
  (void)hipFuncSetAttribute((const void*)h3_geglu_fused, hipFuncAttributeMaxDynamicSharedMemorySize, LDS_BYTES);
  h3_geglu_fused<<<256, 512, LDS_BYTES, stream>>>((const char*)d_in[0], (const char*)d_in[1], (const char*)d_in[2], (const char*)d_in[3],
                                                  (const char*)d_in[4], (const char*)d_in[5], (const char*)d_in[7], (const char*)d_in[8],
                                                  (const char*)d_in[9], (char*)d_out, (char*)d_ws);
}
```

```cpp
#include <hip/hip_runtime.h>
#include <stdint.h>

#define LAS __attribute__((address_space(3)))
#define GAS __attribute__((address_space(1)))
#define DI __device__ __forceinline__
typedef __bf16 bf8 __attribute__((ext_vector_type(8)));
typedef __bf16 bf2 __attribute__((ext_vector_type(2)));
typedef short s4 __attribute__((ext_vector_type(4)));
typedef float f4 __attribute__((ext_vector_type(4)));
typedef float f2 __attribute__((ext_vector_type(2)));
typedef unsigned u4 __attribute__((ext_vector_type(4)));
typedef unsigned u2 __attribute__((ext_vector_type(2)));
typedef unsigned u2a __attribute__((ext_vector_type(2), aligned(4)));
typedef unsigned long long u64;

namespace {
constexpr unsigned WS_EPOCH = 0;
constexpr unsigned WS_FK = 4096;
constexpr unsigned WS_FS = WS_FK + 256 * 128;
constexpr unsigned WS_FH = WS_FS + 256 * 128;
constexpr unsigned WS_FY = WS_FH + 256 * 128;
constexpr unsigned WS_ROW = WS_FY + 256 * 128;
constexpr unsigned WS_COL = WS_ROW + 2048 * 8;
constexpr unsigned WS_KVT = 1u << 20;
constexpr unsigned WS_YT = 3u << 20;
constexpr unsigned WS_H = 5u << 20;
constexpr unsigned WS_PROF = 32u << 20;
constexpr unsigned SLOT = 49152;
constexpr unsigned L_MISC = 3 * SLOT;
constexpr unsigned L_RS = 159744;
constexpr unsigned L_CS = L_RS + 1024;
constexpr unsigned L_CTL = L_CS + 1024;
constexpr unsigned C_LAND = L_CTL, C_CONS = L_CTL + 40, C_BSYNC = L_CTL + 72, C_PRE = L_CTL + 76, C_WSYNC = L_CTL + 80, C_EPI = L_CTL + 84,
                   C_HPUB = L_CTL + 88, C_GO = L_CTL + 92, C_WD = L_CTL + 96, C_CVIN = L_CTL + 104, C_TAG = L_CTL + 108, C_PAIR = L_CTL + 112,
                   C_RD = L_CTL + 128, C_LD = L_CTL + 144, C_HOK = L_CTL + 160, L_TAG = L_CTL + 192;
constexpr unsigned CV_COPY = 4160, CV_UR = 2 * CV_COPY, CV_CH = CV_UR + 4608;
constexpr unsigned L_CV = L_RS - 2 * CV_CH;
static_assert(L_CV >= 131072, "W_down ring");
constexpr unsigned L_YI = L_CV;
constexpr unsigned LDS_BYTES = 163840;
constexpr int SPIN_CAP = 1 << 18;

DI float bflo(unsigned u) { return __uint_as_float(u << 16); }
DI float bfhi(unsigned u) { return __uint_as_float(u & 0xffff0000u); }
DI unsigned pk2(float a, float b) { f2 v = {a, b}; return __builtin_bit_cast(unsigned, __builtin_convertvector(v, bf2)); }
DI float dot2(unsigned a, bf2 b, float c) { return __builtin_amdgcn_fdot2_f32_bf16(__builtin_bit_cast(bf2, a), b, c, false); }
DI float rbf(float a) { return bflo(pk2(a, a)); }
DI f4 mfma(bf8 a, bf8 b, f4 c) { return __builtin_amdgcn_mfma_f32_16x16x32_bf16(a, b, c, 0, 0, 0); }
DI bf8 lds128(unsigned a) { return *(const LAS bf8*)(size_t)a; }
DI u4 lds128u(unsigned a) { return *(const LAS u4*)(size_t)a; }
DI f4 lds128f(unsigned a) { return *(const LAS f4*)(size_t)a; }
DI void sts128(unsigned a, u4 v) { *(LAS u4*)(size_t)a = v; }
DI void sts128f(unsigned a, f4 v) { *(LAS f4*)(size_t)a = v; }
DI bf8 ldtr(unsigned a0, unsigned a1) {
  s4 lo = __builtin_amdgcn_ds_read_tr16_b64_v4i16((LAS s4*)(size_t)a0);
  s4 hi = __builtin_amdgcn_ds_read_tr16_b64_v4i16((LAS s4*)(size_t)a1);
  return __builtin_bit_cast(bf8, __builtin_shufflevector(lo, hi, 0, 1, 2, 3, 4, 5, 6, 7));
}
#define WAITV(n) asm volatile("s_waitcnt vmcnt(%0)" ::"i"(n) : "memory")
#define WAITL(n) asm volatile("s_waitcnt lgkmcnt(%0)" ::"i"(n) : "memory")
#define SB() __builtin_amdgcn_sched_barrier(0)
#define OPAQUE(x) asm volatile("" : "+v"(x))
#define BAR() do { asm volatile("s_waitcnt lgkmcnt(0)" ::: "memory"); __builtin_amdgcn_s_barrier(); asm volatile("" ::: "memory"); } while (0)

DI void glds2(unsigned v0, unsigned v1, const char* sb, unsigned l) {
  asm volatile("s_mov_b32 m0, %3\n\ts_nop 0\n\tglobal_load_lds_dwordx4 %0, %2\n\tglobal_load_lds_dwordx4 %1, %2 offset:1024"
               ::"v"(v0), "v"(v1), "s"(sb), "s"(l) : "memory");
}
DI void glds4(unsigned v0, unsigned v1, unsigned v2, unsigned v3, const char* sb, unsigned l) {
  asm volatile("s_mov_b32 m0, %5\n\ts_nop 0\n\tglobal_load_lds_dwordx4 %0, %4\n\tglobal_load_lds_dwordx4 %1, %4 offset:1024\n\t"
               "global_load_lds_dwordx4 %2, %4 offset:2048\n\tglobal_load_lds_dwordx4 %3, %4 offset:3072"
               ::"v"(v0), "v"(v1), "v"(v2), "v"(v3), "s"(sb), "s"(l) : "memory");
}
DI void flag_set(char* ws, unsigned off, u64 tag) {
  __hip_atomic_store((GAS u64*)(ws + off), tag, __ATOMIC_RELAXED, __HIP_MEMORY_SCOPE_AGENT);
}
DI void poll(const char* ws, unsigned off, u64 tag) {
#pragma unroll 1
  for (int it = 0; it < SPIN_CAP; ++it) {
    u64 x = __hip_atomic_load((GAS u64*)(ws + off), __ATOMIC_RELAXED, __HIP_MEMORY_SCOPE_AGENT);
    if (__all(x == tag)) break;
    __builtin_amdgcn_s_sleep(4);
  }
  asm volatile("" ::: "memory");
}
DI float fadd(float a, float b) { float r; asm("v_add_f32 %0, %1, %2" : "=v"(r) : "v"(a), "v"(b)); return r; }
DI float fsub(float a, float b) { float r; asm("v_sub_f32 %0, %1, %2" : "=v"(r) : "v"(a), "v"(b)); return r; }
DI float fmul(float a, float b) { float r; asm("v_mul_f32 %0, %1, %2" : "=v"(r) : "v"(a), "v"(b)); return r; }
DI float ffma(float a, float b, float c) { float r; asm("v_fma_f32 %0, %1, %2, %3" : "=v"(r) : "v"(a), "v"(b), "v"(c)); return r; }
DI void poll2(const char* ws, unsigned off0, unsigned off1, u64 tag) {
#pragma unroll 1
  for (int it = 0; it < SPIN_CAP; ++it) {
    const u64 x = __hip_atomic_load((GAS u64*)(ws + off0), __ATOMIC_RELAXED, __HIP_MEMORY_SCOPE_AGENT);
    const u64 y = __hip_atomic_load((GAS u64*)(ws + off1), __ATOMIC_RELAXED, __HIP_MEMORY_SCOPE_AGENT);
    if (__all(x == tag && y == tag)) break;
    __builtin_amdgcn_s_sleep(8);
  }
  asm volatile("" ::: "memory");
}
template <int CTRL> DI float dpp_add(float v) {
  return v + __int_as_float(__builtin_amdgcn_update_dpp(0, __float_as_int(v), CTRL, 0xf, 0xf, true));
}
DI float wave_sum(float v) {
  v = dpp_add<0xB1>(v); v = dpp_add<0x4E>(v); v = dpp_add<0x141>(v); v = dpp_add<0x140>(v);
  const int i = __float_as_int(v);
  return (__int_as_float(__builtin_amdgcn_readlane(i, 0)) + __int_as_float(__builtin_amdgcn_readlane(i, 16))) +
         (__int_as_float(__builtin_amdgcn_readlane(i, 32)) + __int_as_float(__builtin_amdgcn_readlane(i, 48)));
}
DI void ctl_add(unsigned addr) {
  asm volatile("s_waitcnt lgkmcnt(0)\n\ts_mov_b64 exec, 1\n\tds_add_u32 %0, %1\n\ts_mov_b64 exec, -1" ::"v"(addr), "v"(1u) : "memory");
}
DI unsigned ctl_get(unsigned addr) {
  unsigned s, t;
  asm volatile("ds_read_b32 %1, %2\n\ts_waitcnt lgkmcnt(0)\n\tv_readfirstlane_b32 %0, %1\n\ts_nop 1" : "=s"(s), "=&v"(t) : "v"(addr) : "memory");
  return s;
}
DI void ctl_spin(unsigned addr, unsigned target) {
#pragma unroll 1
  for (int it = 0; it < SPIN_CAP; ++it)
    if (ctl_get(addr) >= target) break;
}
DI void ctl_wait(unsigned addr, unsigned target) {
#pragma unroll 1
  for (int it = 0; it < SPIN_CAP; ++it) {
    if (ctl_get(addr) >= target) break;
    __builtin_amdgcn_s_sleep(1);
  }
}
#define STAMP(i)
#define CNOW(v)
#define CSTAMP(i)

#define HWIN 4
#define WREG 6
#define SFIRST 0
#define PEEK_AT 5
#define SIDE_SLEEP 0
#define CODE_LINES 240
#define PF_W 1
#define PF_CODE 1
#define PF_L2 14
#define S1_EARLY 1
#define BAR_AT 32
#define KBAR() do { if (g1) BAR(); } while (0)
struct Frag { bf8 x[4]; bf8 w[2][4]; };

__global__ __launch_bounds__(512, 2) void h3_geglu_fused(const char* q, const char* kk, const char* vv, const char* shk, const char* ssk,
                                                         const char* sD, const char* wg, const char* wu, const char* wd, char* out, char* ws) {
  extern __shared__ __attribute__((aligned(16))) char smem[];
  u64 kbase;
  asm volatile("s_getpc_b64 %0" : "=s"(kbase));
  const int lane = threadIdx.x & 63, w0 = __builtin_amdgcn_readfirstlane(threadIdx.x >> 6) ^ (SFIRST ? 4 : 0);
  const int b = blockIdx.x, xcd = b & 7, r = b >> 3;
  const bool g1 = r < 28;
  const int l15 = lane & 15, g = lane >> 4;
  const __amdgpu_buffer_rsrc_t wsr = __builtin_amdgcn_make_buffer_rsrc(ws, 0, 16 << 20, 0x00020000);
  const int pnl = r >> 2;
  const int pm = (xcd >> 1) * 4 + (r & 3), pn = (xcd & 1) * 7 + pnl;
  const int rt = xcd * 4 + (r & 3), ct2 = r >> 2;
  auto stage_offsets = [&](unsigned (&xv)[4], unsigned (&wv)[4]) {
#pragma unroll
    for (int p = 0; p < 4; ++p) {
      xv[p] = (p * 8 + (lane >> 3)) * 1024 + (((lane & 7) ^ (((p & 1) << 2) | g)) << 4) - p * 1024;
      wv[p] = (p * 4 + g) * 3584 + ((l15 ^ ((g | ((p >> 1) << 2)) << 1)) << 4) - p * 1024;
    }
  };
  auto quarter = [&](const unsigned (&xv)[4], const unsigned (&wv)[4], int st, int qt) {
    const unsigned l = (st % 3) * SLOT + qt * 4096;
    glds4(xv[0], xv[1], xv[2], xv[3], q + pm * 131072 + qt * 32768 + st * 128, l);
    glds4(wv[0], wv[1], wv[2], wv[3], wg + pn * 256 + (st * 64 + qt * 16) * 3584, l + 16384);
    glds4(wv[0], wv[1], wv[2], wv[3], wu + pn * 256 + (st * 64 + qt * 16) * 3584, l + 32768);
  };
  if (w0 < 4) {
    const unsigned kvo = (lane >> 3) * 1024 + (((lane & 7) ^ ((g & 1) << 2)) << 4);
    const char* kb = kk + (r * 64 + w0 * 16) * 1024 + xcd * 128;
    const char* vb = vv + (r * 64 + w0 * 16) * 1024 + xcd * 128;
    asm volatile("s_mov_b32 m0, %6\n\ts_nop 0\n\tglobal_load_lds_dwordx4 %0, %3\n\tglobal_load_lds_dwordx4 %0, %4 offset:1024\n\t"
                 "global_load_lds_dwordx4 %1, %4 offset:2048\n\tglobal_load_lds_dwordx4 %0, %5 offset:3072\n\ts_add_u32 m0, m0, 4096\n\ts_nop 0\n\t"
                 "global_load_lds_dwordx4 %1, %8\n\tglobal_load_lds_dwordx4 %2, %7 offset:1024"
                 ::"v"(kvo), "v"(kvo + 7168), "v"((lane & 31) * 16), "s"(r == 0 && w0 == 0 ? kb : kb - 8192), "s"(kb - 1024), "s"(vb - 3072),
                   "s"(2 * SLOT + w0 * 6144), "s"(shk + xcd * 512 - 1024), "s"(vb + 1024)
                 : "memory", "scc");
  } else if (w0 < 6) {
    if (g1) {
      unsigned xv[4], wv[4];
      stage_offsets(xv, wv);
      quarter(xv, wv, 0, 2 * (w0 & 1)); quarter(xv, wv, 0, 2 * (w0 & 1) + 1); quarter(xv, wv, 1, 2 * (w0 & 1));
    }
  } else if (PF_CODE && w0 == 7 && lane < 8 && r * 8 + lane < CODE_LINES) {
    asm volatile("s_mov_b32 m0, %2\n\ts_nop 0\n\tglobal_load_lds_dword %0, %1" ::"v"((r * 8 + lane) * 128), "s"(kbase), "s"(L_RS) : "memory");
  }
  int w_ = w0;
  asm volatile("" : "+s"(w_));
  const int w = w_, gw = w & 3, tid = w * 64 + lane, t8 = tid & 255;
  auto wslot = [](int i) -> unsigned { const int s = i & 7; return s < 2 ? 2 * SLOT + s * 4096 : s < 5 ? (s - 2) * 4096 : SLOT + (s - 5) * 4096; };
  auto wdstage = [&](unsigned dv0, int kq, int i) {
    glds4(dv0, dv0 + 7168, dv0 + 2 * 7168, dv0 + 3 * 7168, wd + ct2 * 128 + (kq * 14 + i) * 32768, wslot(i) + kq * ((i & 7) < 2 ? 8192 : 12288));
  };
  u64 tag;
  auto tag_get = [&]() {
    const u2 t = *(const volatile LAS u2*)(size_t)L_TAG;
    tag = ((u64)(unsigned)__builtin_amdgcn_readfirstlane(t[1]) << 32) | (unsigned)__builtin_amdgcn_readfirstlane(t[0]);
  };

  u4 xq; u2 st2;
  auto tail_load = [&]() {
    int ln = __builtin_amdgcn_mbcnt_hi(-1, __builtin_amdgcn_mbcnt_lo(-1, 0));
    OPAQUE(ln);
    const int tk = w * 8 + (ln >> 3), c8 = ln & 7;
    xq = *(const GAS u4*)(q + (rt * 64 + tk) * 1024 + ct2 * 128 + c8 * 16);
    st2 = __builtin_amdgcn_raw_buffer_load_b64(wsr, WS_ROW + (rt * 64 + tk) * 8, 0, 16);
  };

  if (w < 4) {
    {
      const unsigned lk = 2 * SLOT + gw * 6144;
      const bool top = r == 0 && gw == 0;
      const int qq = l15 >> 2, pp = lane & 3;
      unsigned ra = lk + qq * 128 + (((2 * g + (pp >> 1)) ^ ((qq >> 1) << 2)) << 4) + (pp & 1) * 8;
      OPAQUE(ra);
      WAITV(0);
      STAMP(18);
      const u2 sw = *(const LAS u2*)(size_t)(lk + 5120 + lane * 8);
      u2 kr[5], vr[4];
#pragma unroll
      for (int i = 0; i < 5; ++i) kr[i] = __builtin_bit_cast(u2, __builtin_amdgcn_ds_read_tr16_b64_v4i16((LAS s4*)(size_t)(ra + 512 + i * 512)));
#pragma unroll
      for (int i = 0; i < 4; ++i) vr[i] = __builtin_bit_cast(u2, __builtin_amdgcn_ds_read_tr16_b64_v4i16((LAS s4*)(size_t)(ra + 3072 + i * 512)));
      if (top) kr[0] = (u2){0, 0};
      const float w0 = bflo(sw[0]), w1 = bfhi(sw[0]), w2 = bflo(sw[1]), w3 = bfhi(sw[1]);
      float kf[20], u[16];
#pragma unroll
      for (int i = 0; i < 5; ++i) { kf[4 * i] = bflo(kr[i][0]); kf[4 * i + 1] = bfhi(kr[i][0]); kf[4 * i + 2] = bflo(kr[i][1]); kf[4 * i + 3] = bfhi(kr[i][1]); }
      SB();
#pragma unroll
      for (int i = 0; i < 16; ++i) u[i] = fmul(kf[i + 1], w3);
      SB();
#pragma unroll
      for (int i = 0; i < 16; ++i) u[i] = ffma(kf[i + 2], w2, u[i]);
      SB();
#pragma unroll
      for (int i = 0; i < 16; ++i) u[i] = ffma(kf[i + 3], w1, u[i]);
      SB();
#pragma unroll
      for (int i = 0; i < 16; ++i) u[i] = ffma(kf[i + 4], w0, u[i]);
      SB();
#pragma unroll
      for (int i = 0; i < 16; ++i) { const unsigned vw = vr[i >> 2][(i >> 1) & 1]; u[i] = fmul(u[i], (i & 1) ? bfhi(vw) : bflo(vw)); }
      SB();
      const unsigned ad = L_MISC + lane * 128;
      sts128(ad + (((7 - 2 * gw) ^ (lane & 7)) << 4), (u4){pk2(u[7], u[6]), pk2(u[5], u[4]), pk2(u[3], u[2]), pk2(u[1], u[0])});
      sts128(ad + (((6 - 2 * gw) ^ (lane & 7)) << 4), (u4){pk2(u[15], u[14]), pk2(u[13], u[12]), pk2(u[11], u[10]), pk2(u[9], u[8])});
      STAMP(1);
      BAR();
    }
    if (g1) {
      const int qq = l15 >> 2, pp = lane & 3;
      const int wm = w >> 1, wn = w & 1;
      f4 acc[2][4][4];
#pragma unroll
      for (int s = 0; s < 2; ++s)
#pragma unroll
        for (int e = 0; e < 4; ++e)
#pragma unroll
          for (int i = 0; i < 4; ++i) acc[s][e][i] = (f4){0.f, 0.f, 0.f, 0.f};
      unsigned xa[2], wa[4];
      xa[0] = (wm * 64 + l15) * 128 + ((g ^ (l15 >> 1)) << 4); xa[1] = xa[0] ^ 64;
#pragma unroll
      for (int e = 0; e < 4; ++e) wa[e] = (8 * g + qq) * 256 + (((4 * wn + e) ^ (qq | ((g & 1) << 2))) << 5) + pp * 8;
      Frag f0, f1;
      s4 lo_;
      auto ldpart = [&](Frag& f, int ks, int j) {
        const unsigned o = 16384 + ks * 8192;
        if (j == 0) f.x[0] = lds128(xa[ks]);
        if (j >= 1 && j <= 16) {
          const int s = (j - 1) >> 3, e = ((j - 1) >> 1) & 3;
          if (j & 1) lo_ = __builtin_amdgcn_ds_read_tr16_b64_v4i16((LAS s4*)(size_t)(wa[e] + o + s * 16384));
          else {
            const s4 hi = __builtin_amdgcn_ds_read_tr16_b64_v4i16((LAS s4*)(size_t)(wa[e] + o + s * 16384 + 1024));
            f.w[s][e] = __builtin_bit_cast(bf8, __builtin_shufflevector(lo_, hi, 0, 1, 2, 3, 4, 5, 6, 7));
          }
        }
        if (j >= 17 && j < 20) f.x[j - 16] = lds128(xa[ks] + (j - 16) * 2048);
      };
      auto mm1 = [&](const Frag& f, int j) { acc[(j >> 2) & 1][j & 3][j >> 3] = mfma(f.w[(j >> 2) & 1][j & 3], f.x[j >> 3], acc[(j >> 2) & 1][j & 3][j >> 3]); };
      auto second = [&](int st) {
        const int d = (st == 2 || st == 5) ? -2 * (int)SLOT : (int)SLOT;
#pragma unroll
        for (int j = 0; j < 32; ++j) {
          if (j == BAR_AT) { BAR(); SB(); }
          mm1(f0, j);
          if (j < 20) ldpart(f1, 1, j);
          if (j == 20) { xa[0] += d; OPAQUE(xa[0]); xa[1] += d; OPAQUE(xa[1]); }
          if (j == 21) { wa[0] += d; OPAQUE(wa[0]); wa[1] += d; OPAQUE(wa[1]); }
          if (j == 22) { wa[2] += d; OPAQUE(wa[2]); wa[3] += d; OPAQUE(wa[3]); }
          SB();
        }
        if (BAR_AT == 32) { BAR(); SB(); }
      };
      OPAQUE(xa[0]); OPAQUE(xa[1]); OPAQUE(wa[0]); OPAQUE(wa[1]); OPAQUE(wa[2]); OPAQUE(wa[3]);
      STAMP(2); CSTAMP(20);
#pragma unroll
      for (int j = 0; j < 20; ++j) ldpart(f0, 0, j);
      SB();
      second(0);
#pragma unroll 1
      for (int st = 1; st < 8; ++st) {
#pragma unroll
        for (int j = 0; j < 32; ++j) { mm1(f1, j); if (j < 20) ldpart(f0, 0, j); SB(); }
        second(st);
      }
      STAMP(16); CSTAMP(21);
      constexpr float C0 = -2.0f * 0.7978845608028654f * 1.4426950408889634f, C1 = C0 * 0.044715f;
      f4 cs[2][4];
      f2 rs[4];
      int ln1 = __builtin_amdgcn_mbcnt_hi(-1, __builtin_amdgcn_mbcnt_lo(-1, 0));
      OPAQUE(ln1);
      const int lane = ln1, l15 = lane & 15, g = lane >> 4;
#pragma unroll
      for (int j = 0; j < 32; ++j) {
        mm1(f1, j);
        if (j < 8) cs[j >> 2][j & 3] = lds128f(L_CS + ((j >> 2) * 128 + wn * 64 + (j & 3) * 16 + 4 * g) * 4);
        if (j >= 8 && j < 12) rs[j - 8] = *(const LAS f2*)(size_t)(L_RS + (wm * 64 + (j - 8) * 16 + l15) * 8);
        SB();
      }
      STAMP(3);
      const unsigned hso = WS_H + ((pm * 8 + wm * 4) * 56 + pn * 4 + wn * 2) * 1024 + lane * 16;
#pragma unroll
      for (int i = 0; i < 4; ++i) {
        const float m = -rs[i][0], rr = rs[i][1];
        const float k0 = C0 * rr, k1 = C1 * rr * rr * rr, i2 = __builtin_amdgcn_rcpf(rr * rr);
        const f2 m2 = {m, m}, k02 = {k0, k0}, k12 = {k1, k1}, i22 = {i2, i2};
        f2 a[8], c[8], z[8];
        SB();
#pragma unroll
        for (int j = 0; j < 8; ++j) {
          const int e = j >> 1, h = (j & 1) * 2;
          a[j] = __builtin_elementwise_fma(m2, (f2){cs[0][e][h], cs[0][e][h + 1]}, (f2){acc[0][e][i][h], acc[0][e][i][h + 1]});
        }
        SB();
#pragma unroll
        for (int j = 0; j < 8; ++j) z[j] = a[j] * a[j];
        SB();
#pragma unroll
        for (int j = 0; j < 8; ++j) z[j] = __builtin_elementwise_fma(z[j], k12, k02);
        SB();
#pragma unroll
        for (int j = 0; j < 8; ++j) z[j] = z[j] * a[j];
        SB();
#pragma unroll
        for (int j = 0; j < 8; ++j) {
          const int e = j >> 1, h = (j & 1) * 2;
          z[j] = (f2){__builtin_amdgcn_exp2f(z[j][0]), __builtin_amdgcn_exp2f(z[j][1])};
          c[j] = __builtin_elementwise_fma(m2, (f2){cs[1][e][h], cs[1][e][h + 1]}, (f2){acc[1][e][i][h], acc[1][e][i][h + 1]});
          SB();
        }
#pragma unroll
        for (int j = 0; j < 8; ++j) z[j] = __builtin_elementwise_fma(z[j], i22, i22);
        SB();
#pragma unroll
        for (int j = 0; j < 8; ++j) {
          z[j] = (f2){__builtin_amdgcn_rcpf(z[j][0]), __builtin_amdgcn_rcpf(z[j][1])};
          c[j] = c[j] * a[j];
          SB();
        }
#pragma unroll
        for (int j = 0; j < 8; ++j) c[j] = c[j] * z[j];
        SB();
        u4 o0, o1;
#pragma unroll
        for (int j = 0; j < 4; ++j) { o0[j] = pk2(c[j][0], c[j][1]); o1[j] = pk2(c[4 + j][0], c[4 + j][1]); }
        __builtin_amdgcn_raw_buffer_store_b128(o0, wsr, hso + i * 56 * 1024, 0, 16);
        __builtin_amdgcn_raw_buffer_store_b128(o1, wsr, hso + i * 56 * 1024 + 1024, 0, 16);
      }
      SB();
      ctl_add(C_EPI);
      tag_get();
      STAMP(4);
    } else {
      const unsigned o = ((xcd * 4 + r - 28) * 448 + gw * 112 + lane) * 128;
      if (PF_W) asm volatile("s_mov_b32 m0, %3\n\ts_nop 0\n\tglobal_load_lds_dword %0, %2\n\tglobal_load_lds_dword %1, %2"
                             ::"v"(o), "v"(o + (lane < 48 ? 8192 : 0)), "s"(wd), "s"(131072 + gw * 256) : "memory");
      if (PF_L2) {
#pragma unroll 1
        for (int j = (r - 28) * 4 + gw; j < PF_L2 * 16; j += 16) {
          const unsigned o2 = ((((j >> 2) & 3) * 14 + (j >> 4)) * 32 + (j & 3) * 8 + (lane >> 3)) * 1024 + (lane & 7) * 128;
          asm volatile("s_waitcnt vmcnt(0)\n\ts_mov_b32 m0, %2\n\ts_nop 0\n\tglobal_load_lds_dword %0, %1" ::"v"(o2), "s"(wd), "s"(131072 + gw * 256) : "memory");
        }
      }
      WAITV(0);
    }
    int ln2 = __builtin_amdgcn_mbcnt_hi(-1, __builtin_amdgcn_mbcnt_lo(-1, 0));
    OPAQUE(ln2);
    {
      const int lane = ln2, l15 = lane & 15, g = lane >> 4, qq = l15 >> 2, pp = lane & 3;
      const unsigned dv0 = (lane >> 3) * 1024 + (((lane & 7) ^ (g << 1)) * 16);
      f4 acc[4][4];
#pragma unroll
      for (int t = 0; t < 4; ++t)
#pragma unroll
        for (int i = 0; i < 4; ++i) acc[t][i] = (f4){0.f, 0.f, 0.f, 0.f};
      const unsigned da = (4 * g + qq) * 128 + (((2 * g + (qq >> 1)) & 3) << 5) + pp * 8;
      unsigned dlo[4];
#pragma unroll
      for (int t = 0; t < 4; ++t) { dlo[t] = (da ^ (t * 32)) + gw * 12288; OPAQUE(dlo[t]); }
      const unsigned dh = 2 * SLOT - gw * 4096;
      constexpr int WR = 6, HW = HWIN;
      bf8 wf[WR][4];
      auto waddr = [&](int i, int t) -> unsigned {
        if ((i & 7) >= 2) return dlo[t] + wslot(i);
        unsigned d_ = dh;
        asm volatile("" : "+s"(d_));
        unsigned a_ = dlo[t] + d_;
        OPAQUE(a_);
        return a_ + (i & 1) * 4096;
      };
      __builtin_amdgcn_s_setprio(2);
      if (g1) {
        WAITV(0);
        if (lane == 0 && __hip_atomic_fetch_add((LAS unsigned*)(size_t)C_HPUB, 1u, __ATOMIC_RELAXED, __HIP_MEMORY_SCOPE_WORKGROUP) == 3)
          flag_set(ws, WS_FH + (pm * 16 + pn) * 128, tag);
      }
      STAMP(5);
      ctl_wait(C_WD + (gw >> 1) * 4, 2);
      if (!g1) tag_get();
#pragma unroll
      for (int t = 0; t < WR * 4; ++t) { const unsigned a_ = waddr(t >> 2, t & 3); wf[t >> 2][t & 3] = ldtr(a_, a_ + 2048); }
      WAITL(0);
      STAMP(17);
#pragma unroll
      for (int i = 8; i < 14; ++i) wdstage(dv0, gw, i);
      __builtin_amdgcn_s_setprio(0);
      STAMP(9);
      poll(ws, WS_FH + ((rt >> 1) * 16 + ((gw * 7) >> 1) + (lane & 3)) * 128, tag);
      STAMP(6);
      u4 hf[HW][4];
      unsigned ho[4];
#pragma unroll
      for (int t = 0; t < 4; ++t) ho[t] = WS_H + ((rt * 4 + t) * 56 + gw * 14) * 1024 + lane * 16;
      auto ldh1 = [&](int i, int t) { hf[i % HW][t] = __builtin_amdgcn_raw_buffer_load_b128(wsr, ho[t] + (i & 3) * 1024, (i >> 2) * 4096, 16); };
#pragma unroll
      for (int i = 0; i < HW * 4; ++i) { ldh1(i >> 2, i & 3); SB(); }
      s4 lo_;
      unsigned wa_;
#pragma unroll
      for (int i = 0; i < 14; ++i) {
        const int nw = i + WR - 1;
        if (i == 11) { tail_load(); SB(); }
#pragma unroll
        for (int j = 0; j < 16; ++j) {
          const int ti = j >> 2, t = j & 3;
          acc[t][ti] = mfma(wf[i % WR][t], __builtin_bit_cast(bf8, hf[i % HW][ti]), acc[t][ti]);
          if (i >= 1 && nw < 14 && t != 3 && (j - ti) < 8) {
            const int e = j - ti;
            if (!(e & 1)) wa_ = waddr(nw, e >> 1);
            const s4 v_ = __builtin_amdgcn_ds_read_tr16_b64_v4i16((LAS s4*)(size_t)(wa_ + (e & 1) * 2048));
            if (e & 1) wf[nw % WR][e >> 1] = __builtin_bit_cast(bf8, __builtin_shufflevector(lo_, v_, 0, 1, 2, 3, 4, 5, 6, 7)); else lo_ = v_;
          }
          if (t == 3 && i + HW < 14) ldh1(i + HW, ti);
          SB();
        }
        if (i == 0) STAMP(19);
      }
      STAMP(7);
#pragma unroll
      for (int ti = 0; ti < 4; ++ti)
#pragma unroll
        for (int t = 0; t < 4; ++t)
          sts128f((ti >> 1) * SLOT + gw * 12288 + ((ti & 1) * 16 + l15) * 256 + (((4 * t + g) ^ (l15 & 7)) << 4), acc[t][ti]);
    }
    STAMP(8);
  } else {
    const int t7 = tid & 127, bw = w & 1;
    unsigned dcr;
    if (w < 6) {
      unsigned xv[4], wv[4];
      stage_offsets(xv, wv);
      auto stage = [&](int st) { quarter(xv, wv, st, 2 * bw); quarter(xv, wv, st, 2 * bw + 1); };
      const unsigned dv0 = (lane >> 3) * 1024 + (((lane & 7) ^ (g << 1)) * 16);
      WAITV(12);
      STAMP(1);
      BAR();
      if (g1) { quarter(xv, wv, 1, 2 * bw + 1); stage(2); WAITV(24); }
      KBAR();
#pragma unroll 1
      for (int st = 3; st < 8; ++st) {
        if (g1) { stage(st); WAITV(24); }
        KBAR();
      }
      STAMP(13);
      if (!g1) ctl_wait(C_TAG, 2);
      asm volatile("global_load_dword %0, %1, %2" : "=&v"(dcr) : "v"(0u), "s"(sD + b * 4) : "memory");
#pragma unroll
      for (int i = 0; i < 4; ++i) wdstage(dv0, bw * 2 + (i & 1), i >> 1);
      WAITV(17);
      KBAR();
      STAMP(3);
#pragma unroll
      for (int i = 0; i < 6; ++i) wdstage(dv0, bw * 2 + (i & 1), 2 + (i >> 1));
      KBAR();
      STAMP(19);
      tag_get();
      asm volatile("s_waitcnt vmcnt(32)" : "+v"(dcr)::"memory");
#pragma unroll
      for (int i = 0; i < 6; ++i) wdstage(dv0, bw * 2 + (i & 1), 5 + (i >> 1));
      WAITV(24);
      ctl_add(C_WD + bw * 4);
      STAMP(2);
      WAITV(0);
      ctl_add(C_WD + bw * 4);
      STAMP(4);
    } else {
      if (t7 < 48) *(LAS unsigned*)(size_t)(L_CTL + t7 * 4) = 0;
      const int ptt = b >> 3, pct = b & 7;
      const int rgrp = (xcd >> 1) * 64 + (xcd & 1) * 32 + r;
      const int cst = 2 * ((r + pnl) & 3) + ((xcd >> 1) & 1);
      const unsigned hoff = (2 * b + bw) * 4096 + lane * 16;
      u4 xr[4];
      auto rows_load = [&]() {
        asm volatile("global_load_dwordx4 %0, %4, %5\n\tglobal_load_dwordx4 %1, %4, %5 offset:1024\n\tglobal_load_dwordx4 %2, %4, %5 offset:2048\n\t"
                     "global_load_dwordx4 %3, %4, %5 offset:3072"
                     : "=&v"(xr[0]), "=&v"(xr[1]), "=&v"(xr[2]), "=&v"(xr[3]) : "v"(lane * 16), "s"(q + (rgrp * 8 + (w & 1) * 4) * 1024) : "memory");
      };
      auto rows_stat = [&]() {
        asm volatile("" : "+v"(xr[0]), "+v"(xr[1]), "+v"(xr[2]), "+v"(xr[3])::"memory");
        float e[4][8], mu[4], va[4];
#pragma unroll
        for (int i = 0; i < 4; ++i) {
#pragma unroll
          for (int j = 0; j < 4; ++j) { e[i][2 * j] = bflo(xr[i][j]); e[i][2 * j + 1] = bfhi(xr[i][j]); }
          mu[i] = fadd(fadd(fadd(e[i][0], e[i][1]), fadd(e[i][2], e[i][3])), fadd(fadd(e[i][4], e[i][5]), fadd(e[i][6], e[i][7])));
        }
        auto fold4 = [&](const float (&v)[4]) {
          const auto s0 = __builtin_amdgcn_permlane16_swap(__float_as_uint(v[0]), __float_as_uint(v[1]), false, false);
          const auto s1 = __builtin_amdgcn_permlane16_swap(__float_as_uint(v[2]), __float_as_uint(v[3]), false, false);
          const float p0 = fadd(__uint_as_float(s0[0]), __uint_as_float(s0[1])), p1 = fadd(__uint_as_float(s1[0]), __uint_as_float(s1[1]));
          const auto s2 = __builtin_amdgcn_permlane32_swap(__float_as_uint(p0), __float_as_uint(p1), false, false);
          float t = fadd(__uint_as_float(s2[0]), __uint_as_float(s2[1]));
          t = dpp_add<0xB1>(t); t = dpp_add<0x4E>(t); t = dpp_add<0x141>(t); t = dpp_add<0x140>(t);
          return t * (1.0f / 512.0f);
        };
        const float mg = fold4(mu);
#pragma unroll
        for (int i = 0; i < 4; ++i) {
          const float m = __int_as_float(__builtin_amdgcn_readlane(__float_as_int(mg), 16 * i));
          float s = 0.f;
#pragma unroll
          for (int j = 0; j < 8; ++j) { const float d = e[i][j] - m; s = ffma(d, d, s); }
          va[i] = s;
        }
        const float vg = fold4(va);
        const u2 o = {__float_as_uint(rbf(mg)), __float_as_uint(__builtin_amdgcn_rcpf(rbf(__builtin_amdgcn_sqrtf(rbf(rbf(vg) + 1e-5f)))))};
        if (l15 == 0) __builtin_amdgcn_raw_buffer_store_b64(o, wsr, WS_ROW + (rgrp * 8 + (w & 1) * 4 + g) * 8, 0, 16);
        asm volatile("" ::: "memory");
      };

      u4 wsh[8], hc[4], uc[4], c1[4]; unsigned hp[4];
      {
        const char* wb = ((xcd >> 2) ? wu : wg) + (g1 ? pn * 256 + (cst * 64 + bw * 32) * 3584 : 0);
        const unsigned o = g * 3584 + l15 * 16;
        if (SIDE_SLEEP) __builtin_amdgcn_s_sleep(SIDE_SLEEP);
        asm volatile("global_load_dwordx4 %0, %8, %12\n\tglobal_load_dwordx4 %1, %9, %12\n\tglobal_load_dwordx4 %2, %10, %12\n\tglobal_load_dwordx4 %3, %11, %12\n\t"
                     "global_load_dwordx4 %4, %8, %13\n\tglobal_load_dwordx4 %5, %9, %13\n\tglobal_load_dwordx4 %6, %10, %13\n\tglobal_load_dwordx4 %7, %11, %13"
                     : "=&v"(wsh[0]), "=&v"(wsh[1]), "=&v"(wsh[2]), "=&v"(wsh[3]), "=&v"(wsh[4]), "=&v"(wsh[5]), "=&v"(wsh[6]), "=&v"(wsh[7])
                     : "v"(o), "v"(o + 4 * 3584), "v"(o + 8 * 3584), "v"(o + 12 * 3584), "s"(wb), "s"(wb + 16 * 3584) : "memory");
        rows_load();
      }
      asm volatile("s_load_dwordx2 %0, %1, 0x0\n\ts_waitcnt lgkmcnt(0)" : "=s"(tag) : "s"(ws) : "memory");
      tag += 1;
      *(LAS u64*)(size_t)L_TAG = tag;
      BAR();
#pragma unroll
      for (int z = 0; z < 4; ++z) {
        const int id = t7 + 128 * z, c2 = id >> 3, ch = id & 7;
        const u4 t = lds128u(L_MISC + c2 * 128 + ((ch ^ (c2 & 7)) << 4));
        __builtin_amdgcn_raw_buffer_store_b128(t, wsr, WS_KVT + (pct * 64 + c2) * 4096 + 3968 - ptt * 128 + ch * 16, 0, 16);
      }
      asm volatile("s_waitcnt vmcnt(4)" : "+v"(wsh[0]), "+v"(wsh[1]), "+v"(wsh[2]), "+v"(wsh[3]), "+v"(wsh[4]), "+v"(wsh[5]), "+v"(wsh[6]), "+v"(wsh[7])::"memory");
      STAMP(17);
      if (g1) {
        float cacc[8];
#pragma unroll
        for (int j = 0; j < 4; ++j) {
          cacc[2 * j] = fadd(fadd(fadd(bflo(wsh[0][j]), bflo(wsh[1][j])), fadd(bflo(wsh[2][j]), bflo(wsh[3][j]))),
                             fadd(fadd(bflo(wsh[4][j]), bflo(wsh[5][j])), fadd(bflo(wsh[6][j]), bflo(wsh[7][j]))));
          cacc[2 * j + 1] = fadd(fadd(fadd(bfhi(wsh[0][j]), bfhi(wsh[1][j])), fadd(bfhi(wsh[2][j]), bfhi(wsh[3][j]))),
                                 fadd(fadd(bfhi(wsh[4][j]), bfhi(wsh[5][j])), fadd(bfhi(wsh[6][j]), bfhi(wsh[7][j]))));
        }
        float c4[4], c2[2];
#pragma unroll
        for (int j = 0; j < 4; ++j) {
          const auto s2 = __builtin_amdgcn_permlane16_swap(__float_as_uint(cacc[j]), __float_as_uint(cacc[4 + j]), false, false);
          c4[j] = fadd(__uint_as_float(s2[0]), __uint_as_float(s2[1]));
        }
#pragma unroll
        for (int j = 0; j < 2; ++j) {
          const auto s2 = __builtin_amdgcn_permlane32_swap(__float_as_uint(c4[j]), __float_as_uint(c4[2 + j]), false, false);
          c2[j] = fadd(__uint_as_float(s2[0]), __uint_as_float(s2[1]));
        }
        __builtin_amdgcn_raw_buffer_store_b64((u2){__float_as_uint(c2[0]), __float_as_uint(c2[1])}, wsr,
                                              WS_COL + ((pn * 16 + cst * 2 + bw) * 256 + (xcd >> 2) * 128 + l15 * 8 + (g & 1) * 4 + (g >> 1) * 2) * 4, 0, 16);
      }
      STAMP(10);
      KBAR();
      rows_stat();
      STAMP(18);
      asm volatile("global_load_dwordx4 %0, %9, %11\n\tglobal_load_dwordx4 %1, %9, %11 offset:1024\n\tglobal_load_dwordx4 %2, %9, %11 offset:2048\n\t"
                   "global_load_dwordx4 %3, %9, %11 offset:3072\n\tglobal_load_dword %4, %10, %11\n\tglobal_load_dword %5, %9, %11 offset:1020\n\t"
                   "global_load_dword %6, %9, %11 offset:2044\n\tglobal_load_dword %7, %9, %11 offset:3068\n\tglobal_load_dword %8, %12, %13"
                   : "=&v"(hc[0]), "=&v"(hc[1]), "=&v"(hc[2]), "=&v"(hc[3]), "=&v"(hp[0]), "=&v"(hp[1]), "=&v"(hp[2]), "=&v"(hp[3]), "=&v"(dcr)
                   : "v"(hoff), "v"(lane ? hoff - 4 : hoff), "s"(ssk), "v"(0u), "s"(sD + b * 4) : "memory");
      KBAR();
      WAITV(9);
      KBAR();
      if (!g1) { ctl_add(C_WSYNC); ctl_spin(C_WSYNC, 2); }
      if (tid == 384 || tid == 385) flag_set(ws, tid == 384 ? WS_FS + rgrp * 128 : WS_FK + (pct * 32 + ptt) * 128, tag);
      STAMP(20);
      unsigned fo = lane < 32 || !g1 ? WS_FK + ((b >> 5) * 32 + (lane & 31)) * 128
                                     : WS_FS + (lane < 48 ? pm * 16 + l15 : (l15 >> 2) * 64 + (xcd & 1) * 32 + (l15 & 3) + 4 * pnl) * 128;
      unsigned fo2 = WS_FS + (rt * 8 + (lane & 7)) * 128;
      OPAQUE(fo); OPAQUE(fo2);
      KBAR();
      asm volatile("s_waitcnt vmcnt(0)" : "+v"(hc[0]), "+v"(hc[1]), "+v"(hc[2]), "+v"(hc[3]), "+v"(hp[0]), "+v"(hp[1]), "+v"(hp[2]), "+v"(hp[3]), "+v"(dcr)::"memory");
      if (lane == 0) hp[0] = 0;
#pragma unroll
      for (int z = 0; z < 4; ++z) {
        c1[z] = (u4){__builtin_amdgcn_alignbit(hc[z][0], hp[z], 16), __builtin_amdgcn_alignbit(hc[z][1], hc[z][0], 16),
                     __builtin_amdgcn_alignbit(hc[z][2], hc[z][1], 16), __builtin_amdgcn_alignbit(hc[z][3], hc[z][2], 16)};
        OPAQUE(c1[z]);
      }
      u64 p0, p1;
      if (!g1) __builtin_amdgcn_s_sleep(70);
      if (PEEK_AT == 5) KBAR();
      asm volatile("global_load_dwordx2 %0, %2, %4 sc1\n\tglobal_load_dwordx2 %1, %3, %4 sc1" : "=&v"(p0), "=&v"(p1) : "v"(fo), "v"(fo2), "s"(ws) : "memory");
      KBAR();
      STAMP(23);
      asm volatile("s_waitcnt vmcnt(0)" : "+v"(p0), "+v"(p1)::"memory");
      if (!__all(p0 == tag && p1 == tag)) poll2(ws, fo, fo2, tag);
      if (!g1) ctl_add(C_TAG);
      STAMP(21);
      u2 rsv, csv[16];
      {
        const unsigned co = WS_COL + (g1 ? pn : 0) * 16384 + t7 * 8;
#define LD4(o, vreg) asm volatile("global_load_dwordx2 %0, %4, %5 sc1\n\tglobal_load_dwordx2 %1, %4, %5 offset:1024 sc1\n\t" \
                                  "global_load_dwordx2 %2, %4, %5 offset:2048 sc1\n\tglobal_load_dwordx2 %3, %4, %5 offset:3072 sc1" \
                                  : "=&v"(csv[o]), "=&v"(csv[o + 1]), "=&v"(csv[o + 2]), "=&v"(csv[o + 3]) : "v"(vreg), "s"(ws) : "memory")
        LD4(0, co); LD4(4, co + 4096); LD4(8, co + 8192); LD4(12, co + 12288);
        asm volatile("global_load_dwordx2 %0, %1, %2 sc1" : "=&v"(rsv) : "v"(WS_ROW + (g1 ? pm : 0) * 1024 + t7 * 8), "s"(ws) : "memory");
      }
      asm volatile("global_load_dwordx4 %0, %4, %5 sc1\n\tglobal_load_dwordx4 %1, %4, %5 offset:1024 sc1\n\tglobal_load_dwordx4 %2, %4, %5 offset:2048 sc1\n\t"
                   "global_load_dwordx4 %3, %4, %5 offset:3072 sc1" : "=&v"(uc[0]), "=&v"(uc[1]), "=&v"(uc[2]), "=&v"(uc[3]) : "v"(WS_KVT + hoff), "s"(ws) : "memory");
      if (PEEK_AT == 4) KBAR();
      const unsigned base = L_CV + bw * CV_CH;
#pragma unroll
      for (int z = 0; z < 4; ++z) {
        sts128(base + 32 + lane * 16 + z * 1024, hc[z]);
        sts128(base + 32 + lane * 16 + z * 1024 + CV_COPY, c1[z]);
      }
      if (lane < 4) sts128(base + (lane >> 1) * CV_COPY + (lane & 1) * 16, (u4){0, 0, 0, 0});
      if (lane >= 32) sts128(base + CV_UR + 4096 + (lane - 32) * 16, (u4){0, 0, 0, 0});
      STAMP(14);
      KBAR();
      asm volatile("s_waitcnt vmcnt(4)" : "+v"(rsv), "+v"(csv[0]), "+v"(csv[1]), "+v"(csv[2]), "+v"(csv[3]), "+v"(csv[4]), "+v"(csv[5]), "+v"(csv[6]), "+v"(csv[7]),
                   "+v"(csv[8]), "+v"(csv[9]), "+v"(csv[10]), "+v"(csv[11]), "+v"(csv[12]), "+v"(csv[13]), "+v"(csv[14]), "+v"(csv[15])::"memory");
      {
        *(LAS u2*)(size_t)(L_RS + t7 * 8) = rsv;
        float s0[4], s1[4];
#pragma unroll
        for (int i = 0; i < 4; ++i) {
          s0[i] = fadd(fadd(__uint_as_float(csv[i][0]), __uint_as_float(csv[4 + i][0])), fadd(__uint_as_float(csv[8 + i][0]), __uint_as_float(csv[12 + i][0])));
          s1[i] = fadd(fadd(__uint_as_float(csv[i][1]), __uint_as_float(csv[4 + i][1])), fadd(__uint_as_float(csv[8 + i][1]), __uint_as_float(csv[12 + i][1])));
        }
        *(LAS u2*)(size_t)(L_CS + t7 * 8) = (u2){__float_as_uint(fadd(fadd(s0[0], s0[1]), fadd(s0[2], s0[3]))), __float_as_uint(fadd(fadd(s1[0], s1[1]), fadd(s1[2], s1[3])))};
      }
      STAMP(16);
      KBAR();
      if (b == 255 && bw == 1) {
#pragma unroll 1
        for (int z = 0; z < 4; ++z) poll(ws, WS_FS + (z * 64 + lane) * 128, tag);
        if (lane == 0) flag_set(ws, WS_EPOCH, tag);
      }
      asm volatile("s_waitcnt vmcnt(0)" : "+v"(uc[0]), "+v"(uc[1]), "+v"(uc[2]), "+v"(uc[3])::"memory");
#pragma unroll
      for (int z = 0; z < 4; ++z) sts128(base + CV_UR + lane * 16 + z * 1024, uc[z]);
      ctl_add(C_CVIN);
      STAMP(15);
    }
    ctl_wait(C_CVIN, 2);
    const int ch = gw >> 1, half = gw & 1;
    const unsigned base = L_CV + ch * CV_CH;
    const unsigned o = 8 * g + l15 + 1, kap = o & 1;
    unsigned aa = base + kap * CV_COPY + 2 * (o + kap) + half * 256, ab = aa + 8;
    unsigned ba = base + CV_UR + 2 * (2032 - 16 * l15 + 8 * g) - 3584 + half * 256;
    OPAQUE(aa); OPAQUE(ab); OPAQUE(ba);
    f4 y[8];
#pragma unroll
    for (int n = 0; n < 8; ++n) y[n] = (f4){0.f, 0.f, 0.f, 0.f};
    bf8 A[4], B[8];
    auto lda = [&](int qi) {
      const int off = (qi & 7) * 512 + (qi >> 3) * 64;
      const u2a lo = *(const LAS u2a*)(size_t)(aa + off), hi = *(const LAS u2a*)(size_t)(ab + off);
      A[qi & 3] = __builtin_bit_cast(bf8, (u4){lo[0], lo[1], hi[0], hi[1]});
    };
#pragma unroll
    for (int i = 0; i < 8; ++i) { B[7 - i] = lds128(ba + i * 512); if (i < 3) lda(i); }
    SB();
    if (g1) ctl_spin(C_EPI, 4);
    STAMP(5);
    __builtin_amdgcn_s_setprio(1);
#pragma unroll 1
    for (int rho = 0; rho < 4; ++rho) {
#pragma unroll
      for (int qi = 0; qi < 8; ++qi) {
#pragma unroll
        for (int p = 7 - qi; p >= 0; --p) {
          y[qi + p] = mfma(A[qi & 3], B[p], y[qi + p]);
          if (p == 7 - qi) { lda(qi + 3); B[p] = lds128(ba + (7 - p) * 512 + 64); }
          SB();
        }
      }
      aa += 64; ab += 64; ba += 64;
      OPAQUE(aa); OPAQUE(ab); OPAQUE(ba);
    }
    STAMP(6);
    ctl_add(C_PAIR + ch * 4); ctl_spin(C_PAIR + ch * 4, 2);
    const unsigned ex0 = base + half * 4096, ex1 = base + (half ^ 1) * 4096;
#pragma unroll
    for (int n = 0; n < 4; ++n) {
      if (half) { const f4 t = y[n]; y[n] = y[4 + n]; y[4 + n] = t; }
      sts128f(ex0 + n * 1024 + lane * 16, y[4 + n]);
    }
    u2 uu[4];
#pragma unroll
    for (int n = 0; n < 4; ++n) uu[n] = *(const LAS u2*)(size_t)(base + CV_UR + 2 * (2044 - (256 * (4 * half + n) + 16 * l15 + 4 * g)));
    ctl_add(C_PAIR + ch * 4);
    const float dc = ch ? bfhi(dcr) : bflo(dcr);
#pragma unroll
    for (int n = 0; n < 4; ++n) {
      y[n][0] = ffma(bfhi(uu[n][1]), dc, y[n][0]); y[n][1] = ffma(bflo(uu[n][1]), dc, y[n][1]);
      y[n][2] = ffma(bfhi(uu[n][0]), dc, y[n][2]); y[n][3] = ffma(bflo(uu[n][0]), dc, y[n][3]);
    }
    ctl_spin(C_PAIR + ch * 4, 4);
    STAMP(11);
    u2 pk[4];
#pragma unroll
    for (int n = 0; n < 4; ++n) {
      const f4 t = lds128f(ex1 + n * 1024 + lane * 16);
      pk[n] = (u2){pk2(fadd(t[0], y[n][0]), fadd(t[1], y[n][1])), pk2(fadd(t[2], y[n][2]), fadd(t[3], y[n][3]))};
    }
#pragma unroll
    for (int n = 0; n < 4; n += 2) {
      const auto s0 = __builtin_amdgcn_permlane16_swap(pk[n][0], pk[n + 1][0], false, false);
      const auto s1 = __builtin_amdgcn_permlane16_swap(pk[n][1], pk[n + 1][1], false, false);
      const u4 o4 = {s0[0], s1[0], s0[1], s1[1]};
      __builtin_amdgcn_raw_buffer_store_b128(o4, wsr, WS_YT + (2 * b + ch) * 4096 + 2 * (256 * (4 * half + n + (g & 1)) + 16 * l15 + 8 * (g >> 1)), 0, 16);
    }
    STAMP(22);
    WAITV(0);
    ctl_add(C_BSYNC);
    ctl_spin(C_BSYNC, 4);
    if (tid == 256) flag_set(ws, WS_FY + b * 128, tag);
    STAMP(7);
    tail_load();
    if (gw == 3) {
      poll(ws, WS_FY + (ct2 * 32 + (lane & 31)) * 128, tag);
      ctl_add(C_GO);
    } else ctl_wait(C_GO, 1);
    STAMP(8);
    u4 yq[2];
#pragma unroll
    for (int z = 0; z < 2; ++z) {
      const int id = t8 + 256 * z;
      yq[z] = __builtin_amdgcn_raw_buffer_load_b128(wsr, WS_YT + (ct2 * 64 + (id >> 3)) * 4096 + rt * 128 + (id & 7) * 16, 0, 16);
    }
#pragma unroll
    for (int z = 0; z < 2; ++z) {
      const int id = t8 + 256 * z, c = id >> 3;
      sts128(L_YI + c * 128 + (((id & 7) ^ (c >> 3)) << 4), yq[z]);
    }
    __builtin_amdgcn_s_setprio(0);
    STAMP(9);
  }
  f2 xn[4];
#pragma unroll
  for (int j = 0; j < 4; ++j) {
    const float m = __uint_as_float(st2[0]), rr = __uint_as_float(st2[1]);
    const unsigned d = pk2(bflo(xq[j]) - m, bfhi(xq[j]) - m);
    const unsigned n = pk2(bflo(d) * rr, bfhi(d) * rr);
    xn[j] = (f2){bflo(n), bfhi(n)};
    OPAQUE(xn[j]);
  }
  BAR();
  if (w < 4) STAMP(10);
  {
    int ln3 = __builtin_amdgcn_mbcnt_hi(-1, __builtin_amdgcn_mbcnt_lo(-1, 0));
    OPAQUE(ln3);
    const int tk = w * 8 + (ln3 >> 3), c8 = ln3 & 7;
    const unsigned pb = (w >> 2) * SLOT + (tk & 31) * 256;
    f4 o0 = lds128f(pb + (((2 * c8) ^ (tk & 7)) << 4)), o1 = lds128f(pb + (((2 * c8 + 1) ^ (tk & 7)) << 4));
#pragma unroll
    for (int k4 = 1; k4 < 4; ++k4) {
      o0 += lds128f(pb + k4 * 12288 + (((2 * c8) ^ (tk & 7)) << 4));
      o1 += lds128f(pb + k4 * 12288 + (((2 * c8 + 1) ^ (tk & 7)) << 4));
    }
    const unsigned ya = L_YI + c8 * 1024 + ((w ^ c8) << 4) + (tk & 7) * 2;
    unsigned yv[8];
#pragma unroll
    for (int j = 0; j < 8; ++j) yv[j] = *(const LAS unsigned short*)(size_t)(ya + j * 128);
    f2 o[4] = {{o0[0], o0[1]}, {o0[2], o0[3]}, {o1[0], o1[1]}, {o1[2], o1[3]}}, e[4], sg[4];
#pragma unroll
    for (int j = 0; j < 4; ++j) {
      const f2 z = o[j] * -1.4426950408889634f;
      e[j] = (f2){__builtin_amdgcn_exp2f(z[0]), __builtin_amdgcn_exp2f(z[1])} + 1.0f;
      sg[j] = xn[j] * (f2){bflo(yv[2 * j]), bflo(yv[2 * j + 1])} * o[j];
    }
    u4 res;
#pragma unroll
    for (int j = 0; j < 4; ++j) {
      const f2 t = __builtin_elementwise_fma(sg[j], (f2){__builtin_amdgcn_rcpf(e[j][0]), __builtin_amdgcn_rcpf(e[j][1])}, (f2){bflo(xq[j]), bfhi(xq[j])});
      res[j] = pk2(t[0], t[1]);
    }
    *(GAS u4*)(out + (rt * 64 + tk) * 1024 + ct2 * 128 + c8 * 16) = res;
  }
  if (w < 4) STAMP(11);
}
}

extern "C" void kernel_launch(void* const* d_in, const int* in_sizes, int n_in, void* d_out, int out_size, void* d_ws, size_t ws_size,
                              hipStream_t stream) {
  (void)in_sizes; (void)n_in; (void)out_size; (void)ws_size;
  (void)hipFuncSetAttribute((const void*)h3_geglu_fused, hipFuncAttributeMaxDynamicSharedMemorySize, LDS_BYTES);
  h3_geglu_fused<<<256, 512, LDS_BYTES, stream>>>((const char*)d_in[0], (const char*)d_in[1], (const char*)d_in[2], (const char*)d_in[3],
                                                  (const char*)d_in[4], (const char*)d_in[5], (const char*)d_in[7], (const char*)d_in[8],
                                                  (const char*)d_in[9], (char*)d_out, (char*)d_ws);
}
```

```cpp
#define PROF
#define PROBE_B 9
#define PROBE_W 0
#define PROBE_I 22
#include <hip/hip_runtime.h>
#include <stdint.h>

#define LAS __attribute__((address_space(3)))
#define GAS __attribute__((address_space(1)))
#define DI __device__ __forceinline__
typedef __bf16 bf8 __attribute__((ext_vector_type(8)));
typedef __bf16 bf2 __attribute__((ext_vector_type(2)));
typedef short s4 __attribute__((ext_vector_type(4)));
typedef float f4 __attribute__((ext_vector_type(4)));
typedef float f2 __attribute__((ext_vector_type(2)));
typedef unsigned u4 __attribute__((ext_vector_type(4)));
typedef unsigned u2 __attribute__((ext_vector_type(2)));
typedef unsigned u2a __attribute__((ext_vector_type(2), aligned(4)));
typedef unsigned long long u64;

namespace {
constexpr unsigned WS_EPOCH = 0;
constexpr unsigned WS_FK = 4096;
constexpr unsigned WS_FS = WS_FK + 256 * 128;
constexpr unsigned WS_FH = WS_FS + 256 * 128;
constexpr unsigned WS_FY = WS_FH + 256 * 128;
constexpr unsigned WS_ROW = WS_FY + 256 * 128;
constexpr unsigned WS_COL = WS_ROW + 2048 * 8;
constexpr unsigned WS_KVT = 1u << 20;
constexpr unsigned WS_YT = 3u << 20;
constexpr unsigned WS_H = 5u << 20;
constexpr unsigned WS_PROF = 32u << 20;
constexpr unsigned SLOT = 49152;
constexpr unsigned L_MISC = 3 * SLOT;
constexpr unsigned L_RS = 159744;
constexpr unsigned L_CS = L_RS + 1024;
constexpr unsigned L_CTL = L_CS + 1024;
constexpr unsigned C_LAND = L_CTL, C_CONS = L_CTL + 40, C_BSYNC = L_CTL + 72, C_PRE = L_CTL + 76, C_WSYNC = L_CTL + 80, C_EPI = L_CTL + 84,
                   C_HPUB = L_CTL + 88, C_GO = L_CTL + 92, C_WD = L_CTL + 96, C_CVIN = L_CTL + 104, C_TAG = L_CTL + 108, C_PAIR = L_CTL + 112,
                   C_RD = L_CTL + 128, C_LD = L_CTL + 144, C_HOK = L_CTL + 160, L_TAG = L_CTL + 192;
constexpr unsigned CV_COPY = 4160, CV_UR = 2 * CV_COPY, CV_CH = CV_UR + 4608;
constexpr unsigned L_CV = L_RS - 2 * CV_CH;
static_assert(L_CV >= 131072, "W_down ring");
constexpr unsigned L_YI = L_CV;
constexpr unsigned LDS_BYTES = 163840;
constexpr int SPIN_CAP = 1 << 18;

DI float bflo(unsigned u) { return __uint_as_float(u << 16); }
DI float bfhi(unsigned u) { return __uint_as_float(u & 0xffff0000u); }
DI unsigned pk2(float a, float b) { f2 v = {a, b}; return __builtin_bit_cast(unsigned, __builtin_convertvector(v, bf2)); }
DI float dot2(unsigned a, bf2 b, float c) { return __builtin_amdgcn_fdot2_f32_bf16(__builtin_bit_cast(bf2, a), b, c, false); }
DI float rbf(float a) { return bflo(pk2(a, a)); }
DI f4 mfma(bf8 a, bf8 b, f4 c) { return __builtin_amdgcn_mfma_f32_16x16x32_bf16(a, b, c, 0, 0, 0); }
DI bf8 lds128(unsigned a) { return *(const LAS bf8*)(size_t)a; }
DI u4 lds128u(unsigned a) { return *(const LAS u4*)(size_t)a; }
DI f4 lds128f(unsigned a) { return *(const LAS f4*)(size_t)a; }
DI void sts128(unsigned a, u4 v) { *(LAS u4*)(size_t)a = v; }
DI void sts128f(unsigned a, f4 v) { *(LAS f4*)(size_t)a = v; }
DI bf8 ldtr(unsigned a0, unsigned a1) {
  s4 lo = __builtin_amdgcn_ds_read_tr16_b64_v4i16((LAS s4*)(size_t)a0);
  s4 hi = __builtin_amdgcn_ds_read_tr16_b64_v4i16((LAS s4*)(size_t)a1);
  return __builtin_bit_cast(bf8, __builtin_shufflevector(lo, hi, 0, 1, 2, 3, 4, 5, 6, 7));
}
#define WAITV(n) asm volatile("s_waitcnt vmcnt(%0)" ::"i"(n) : "memory")
#define WAITL(n) asm volatile("s_waitcnt lgkmcnt(%0)" ::"i"(n) : "memory")
#define SB() __builtin_amdgcn_sched_barrier(0)
#define OPAQUE(x) asm volatile("" : "+v"(x))
#define BAR() do { asm volatile("s_waitcnt lgkmcnt(0)" ::: "memory"); __builtin_amdgcn_s_barrier(); asm volatile("" ::: "memory"); } while (0)

DI void glds2(unsigned v0, unsigned v1, const char* sb, unsigned l) {
  asm volatile("s_mov_b32 m0, %3\n\ts_nop 0\n\tglobal_load_lds_dwordx4 %0, %2\n\tglobal_load_lds_dwordx4 %1, %2 offset:1024"
               ::"v"(v0), "v"(v1), "s"(sb), "s"(l) : "memory");
}
DI void glds4(unsigned v0, unsigned v1, unsigned v2, unsigned v3, const char* sb, unsigned l) {
  asm volatile("s_mov_b32 m0, %5\n\ts_nop 0\n\tglobal_load_lds_dwordx4 %0, %4\n\tglobal_load_lds_dwordx4 %1, %4 offset:1024\n\t"
               "global_load_lds_dwordx4 %2, %4 offset:2048\n\tglobal_load_lds_dwordx4 %3, %4 offset:3072"
               ::"v"(v0), "v"(v1), "v"(v2), "v"(v3), "s"(sb), "s"(l) : "memory");
}
DI void flag_set(char* ws, unsigned off, u64 tag) {
  __hip_atomic_store((GAS u64*)(ws + off), tag, __ATOMIC_RELAXED, __HIP_MEMORY_SCOPE_AGENT);
}
DI void poll(const char* ws, unsigned off, u64 tag) {
#pragma unroll 1
  for (int it = 0; it < SPIN_CAP; ++it) {
    u64 x = __hip_atomic_load((GAS u64*)(ws + off), __ATOMIC_RELAXED, __HIP_MEMORY_SCOPE_AGENT);
    if (__all(x == tag)) break;
    __builtin_amdgcn_s_sleep(4);
  }
  asm volatile("" ::: "memory");
}
DI float fadd(float a, float b) { float r; asm("v_add_f32 %0, %1, %2" : "=v"(r) : "v"(a), "v"(b)); return r; }
DI float fsub(float a, float b) { float r; asm("v_sub_f32 %0, %1, %2" : "=v"(r) : "v"(a), "v"(b)); return r; }
DI float fmul(float a, float b) { float r; asm("v_mul_f32 %0, %1, %2" : "=v"(r) : "v"(a), "v"(b)); return r; }
DI float ffma(float a, float b, float c) { float r; asm("v_fma_f32 %0, %1, %2, %3" : "=v"(r) : "v"(a), "v"(b), "v"(c)); return r; }
DI void poll2(const char* ws, unsigned off0, unsigned off1, u64 tag) {
#pragma unroll 1
  for (int it = 0; it < SPIN_CAP; ++it) {
    const u64 x = __hip_atomic_load((GAS u64*)(ws + off0), __ATOMIC_RELAXED, __HIP_MEMORY_SCOPE_AGENT);
    const u64 y = __hip_atomic_load((GAS u64*)(ws + off1), __ATOMIC_RELAXED, __HIP_MEMORY_SCOPE_AGENT);
    if (__all(x == tag && y == tag)) break;
    __builtin_amdgcn_s_sleep(8);
  }
  asm volatile("" ::: "memory");
}
template <int CTRL> DI float dpp_add(float v) {
  return v + __int_as_float(__builtin_amdgcn_update_dpp(0, __float_as_int(v), CTRL, 0xf, 0xf, true));
}
DI float wave_sum(float v) {
  v = dpp_add<0xB1>(v); v = dpp_add<0x4E>(v); v = dpp_add<0x141>(v); v = dpp_add<0x140>(v);
  const int i = __float_as_int(v);
  return (__int_as_float(__builtin_amdgcn_readlane(i, 0)) + __int_as_float(__builtin_amdgcn_readlane(i, 16))) +
         (__int_as_float(__builtin_amdgcn_readlane(i, 32)) + __int_as_float(__builtin_amdgcn_readlane(i, 48)));
}
DI void ctl_add(unsigned addr) {
  asm volatile("s_waitcnt lgkmcnt(0)\n\ts_mov_b64 exec, 1\n\tds_add_u32 %0, %1\n\ts_mov_b64 exec, -1" ::"v"(addr), "v"(1u) : "memory");
}
DI unsigned ctl_get(unsigned addr) {
  unsigned s, t;
  asm volatile("ds_read_b32 %1, %2\n\ts_waitcnt lgkmcnt(0)\n\tv_readfirstlane_b32 %0, %1\n\ts_nop 1" : "=s"(s), "=&v"(t) : "v"(addr) : "memory");
  return s;
}
DI void ctl_spin(unsigned addr, unsigned target) {
#pragma unroll 1
  for (int it = 0; it < SPIN_CAP; ++it)
    if (ctl_get(addr) >= target) break;
}
DI void ctl_wait(unsigned addr, unsigned target) {
#pragma unroll 1
  for (int it = 0; it < SPIN_CAP; ++it) {
    if (ctl_get(addr) >= target) break;
    __builtin_amdgcn_s_sleep(1);
  }
}
#define CSTAMP(i) do { u64 t_; asm volatile("s_memtime %0\n\ts_waitcnt lgkmcnt(0)" : "=s"(t_)::"memory"); tt[i] = (unsigned)t_; } while (0)
#define CNOW(v) do { u64 t_; asm volatile("s_memtime %0\n\ts_waitcnt lgkmcnt(0)" : "=s"(t_)::"memory"); v = (unsigned)t_; } while (0)
#define STAMP(i) do { if ((i) != 0 && (i) != 12 && (i) != PROBE_I) break; u64 t_; asm volatile("s_memrealtime %0\n\ts_waitcnt lgkmcnt(0)" : "=s"(t_)::"memory"); tt[i] = (unsigned)t_; } while (0)

#define HWIN 4
#define WREG 6
#define SFIRST 0
#define PEEK_AT 5
#define SIDE_SLEEP 0
#define CODE_LINES 240
#define PF_W 1
#define PF_CODE 1
#define PF_L2 14
#define S1_EARLY 1
#define BAR_AT 32
#define KBAR() do { if (g1) BAR(); } while (0)
struct Frag { bf8 x[4]; bf8 w[2][4]; };

__global__ __launch_bounds__(512, 2) void h3_geglu_fused(const char* q_, const char* kk_, const char* vv_, const char* shk_, const char* ssk_,
                                                         const char* sD_, const char* wg_, const char* wu_, const char* wd_, char* out_, char* ws_) {
  extern __shared__ __attribute__((aligned(16))) char smem[];
  unsigned tt[24] = {0};
  STAMP(0);
  typedef const __attribute__((address_space(4))) u64* kptr;
  kptr ka = (kptr)__builtin_amdgcn_kernarg_segment_ptr();
  asm volatile("" : "+s"(ka)::"memory");
  const char *q = (const char*)ka[0], *kk = (const char*)ka[1], *vv = (const char*)ka[2], *shk = (const char*)ka[3], *ssk = (const char*)ka[4], *sD = (const char*)ka[5],
             *wg = (const char*)ka[6], *wu = (const char*)ka[7], *wd = (const char*)ka[8];
  char *out = (char*)ka[9], *ws = (char*)ka[10];
  u64 kbase;
  asm volatile("s_getpc_b64 %0" : "=s"(kbase));
  const int lane = threadIdx.x & 63, w0 = __builtin_amdgcn_readfirstlane(threadIdx.x >> 6) ^ (SFIRST ? 4 : 0);
  const int b = blockIdx.x, xcd = b & 7, r = b >> 3;
  const bool g1 = r < 28;
  const int l15 = lane & 15, g = lane >> 4;
  const __amdgpu_buffer_rsrc_t wsr = __builtin_amdgcn_make_buffer_rsrc(ws, 0, 16 << 20, 0x00020000);
  const int pnl = r >> 2;
  const int pm = (xcd >> 1) * 4 + (r & 3), pn = (xcd & 1) * 7 + pnl;
  const int rt = xcd * 4 + (r & 3), ct2 = r >> 2;
  asm volatile("" ::"s"(q), "s"(ws));
  STAMP(22);
  auto stage_offsets = [&](unsigned (&xv)[4], unsigned (&wv)[4]) {
#pragma unroll
    for (int p = 0; p < 4; ++p) {
      xv[p] = (p * 8 + (lane >> 3)) * 1024 + (((lane & 7) ^ (((p & 1) << 2) | g)) << 4) - p * 1024;
      wv[p] = (p * 4 + g) * 3584 + ((l15 ^ ((g | ((p >> 1) << 2)) << 1)) << 4) - p * 1024;
    }
  };
  auto quarter = [&](const unsigned (&xv)[4], const unsigned (&wv)[4], int st, int qt) {
    const unsigned l = (st % 3) * SLOT + qt * 4096;
    glds4(xv[0], xv[1], xv[2], xv[3], q + pm * 131072 + qt * 32768 + st * 128, l);
    glds4(wv[0], wv[1], wv[2], wv[3], wg + pn * 256 + (st * 64 + qt * 16) * 3584, l + 16384);
    glds4(wv[0], wv[1], wv[2], wv[3], wu + pn * 256 + (st * 64 + qt * 16) * 3584, l + 32768);
  };
  if (w0 < 4) {
    const unsigned kvo = (lane >> 3) * 1024 + (((lane & 7) ^ ((g & 1) << 2)) << 4);
    const char* kb = kk + (r * 64 + w0 * 16) * 1024 + xcd * 128;
    const char* vb = vv + (r * 64 + w0 * 16) * 1024 + xcd * 128;
    asm volatile("s_mov_b32 m0, %6\n\ts_nop 0\n\tglobal_load_lds_dwordx4 %0, %3\n\tglobal_load_lds_dwordx4 %0, %4 offset:1024\n\t"
                 "global_load_lds_dwordx4 %1, %4 offset:2048\n\tglobal_load_lds_dwordx4 %0, %5 offset:3072\n\ts_add_u32 m0, m0, 4096\n\ts_nop 0\n\t"
                 "global_load_lds_dwordx4 %1, %8\n\tglobal_load_lds_dwordx4 %2, %7 offset:1024"
                 ::"v"(kvo), "v"(kvo + 7168), "v"((lane & 31) * 16), "s"(r == 0 && w0 == 0 ? kb : kb - 8192), "s"(kb - 1024), "s"(vb - 3072),
                   "s"(2 * SLOT + w0 * 6144), "s"(shk + xcd * 512 - 1024), "s"(vb + 1024)
                 : "memory", "scc");
  } else if (w0 < 6) {
    if (g1) {
      unsigned xv[4], wv[4];
      stage_offsets(xv, wv);
      quarter(xv, wv, 0, 2 * (w0 & 1)); quarter(xv, wv, 0, 2 * (w0 & 1) + 1); quarter(xv, wv, 1, 2 * (w0 & 1));
    }
  } else if (PF_CODE && w0 == 7 && lane < 8 && r * 8 + lane < CODE_LINES) {
    asm volatile("s_mov_b32 m0, %2\n\ts_nop 0\n\tglobal_load_lds_dword %0, %1" ::"v"((r * 8 + lane) * 128), "s"(kbase), "s"(L_RS) : "memory");
  }
  int w_ = w0;
  asm volatile("" : "+s"(w_));
  const int w = w_, gw = w & 3, tid = w * 64 + lane, t8 = tid & 255;
  auto wslot = [](int i) -> unsigned { const int s = i & 7; return s < 2 ? 2 * SLOT + s * 4096 : s < 5 ? (s - 2) * 4096 : SLOT + (s - 5) * 4096; };
  auto wdstage = [&](unsigned dv0, int kq, int i) {
    glds4(dv0, dv0 + 7168, dv0 + 2 * 7168, dv0 + 3 * 7168, wd + ct2 * 128 + (kq * 14 + i) * 32768, wslot(i) + kq * ((i & 7) < 2 ? 8192 : 12288));
  };
  u64 tag;
  auto tag_get = [&]() {
    const u2 t = *(const volatile LAS u2*)(size_t)L_TAG;
    tag = ((u64)(unsigned)__builtin_amdgcn_readfirstlane(t[1]) << 32) | (unsigned)__builtin_amdgcn_readfirstlane(t[0]);
  };

  u4 xq; u2 st2;
  auto tail_load = [&]() {
    int ln = __builtin_amdgcn_mbcnt_hi(-1, __builtin_amdgcn_mbcnt_lo(-1, 0));
    OPAQUE(ln);
    const int tk = w * 8 + (ln >> 3), c8 = ln & 7;
    xq = *(const GAS u4*)(q + (rt * 64 + tk) * 1024 + ct2 * 128 + c8 * 16);
    st2 = __builtin_amdgcn_raw_buffer_load_b64(wsr, WS_ROW + (rt * 64 + tk) * 8, 0, 16);
  };

  if (w < 4) {
    {
      const unsigned lk = 2 * SLOT + gw * 6144;
      const bool top = r == 0 && gw == 0;
      const int qq = l15 >> 2, pp = lane & 3;
      unsigned ra = lk + qq * 128 + (((2 * g + (pp >> 1)) ^ ((qq >> 1) << 2)) << 4) + (pp & 1) * 8;
      OPAQUE(ra);
      WAITV(0);
      STAMP(18);
      const u2 sw = *(const LAS u2*)(size_t)(lk + 5120 + lane * 8);
      u2 kr[5], vr[4];
#pragma unroll
      for (int i = 0; i < 5; ++i) kr[i] = __builtin_bit_cast(u2, __builtin_amdgcn_ds_read_tr16_b64_v4i16((LAS s4*)(size_t)(ra + 512 + i * 512)));
#pragma unroll
      for (int i = 0; i < 4; ++i) vr[i] = __builtin_bit_cast(u2, __builtin_amdgcn_ds_read_tr16_b64_v4i16((LAS s4*)(size_t)(ra + 3072 + i * 512)));
      if (top) kr[0] = (u2){0, 0};
      const float w0 = bflo(sw[0]), w1 = bfhi(sw[0]), w2 = bflo(sw[1]), w3 = bfhi(sw[1]);
      float kf[20], u[16];
#pragma unroll
      for (int i = 0; i < 5; ++i) { kf[4 * i] = bflo(kr[i][0]); kf[4 * i + 1] = bfhi(kr[i][0]); kf[4 * i + 2] = bflo(kr[i][1]); kf[4 * i + 3] = bfhi(kr[i][1]); }
      SB();
#pragma unroll
      for (int i = 0; i < 16; ++i) u[i] = fmul(kf[i + 1], w3);
      SB();
#pragma unroll
      for (int i = 0; i < 16; ++i) u[i] = ffma(kf[i + 2], w2, u[i]);
      SB();
#pragma unroll
      for (int i = 0; i < 16; ++i) u[i] = ffma(kf[i + 3], w1, u[i]);
      SB();
#pragma unroll
      for (int i = 0; i < 16; ++i) u[i] = ffma(kf[i + 4], w0, u[i]);
      SB();
#pragma unroll
      for (int i = 0; i < 16; ++i) { const unsigned vw = vr[i >> 2][(i >> 1) & 1]; u[i] = fmul(u[i], (i & 1) ? bfhi(vw) : bflo(vw)); }
      SB();
      const unsigned ad = L_MISC + lane * 128;
      sts128(ad + (((7 - 2 * gw) ^ (lane & 7)) << 4), (u4){pk2(u[7], u[6]), pk2(u[5], u[4]), pk2(u[3], u[2]), pk2(u[1], u[0])});
      sts128(ad + (((6 - 2 * gw) ^ (lane & 7)) << 4), (u4){pk2(u[15], u[14]), pk2(u[13], u[12]), pk2(u[11], u[10]), pk2(u[9], u[8])});
      STAMP(1);
      BAR();
    }
    if (g1) {
      const int qq = l15 >> 2, pp = lane & 3;
      const int wm = w >> 1, wn = w & 1;
      f4 acc[2][4][4];
#pragma unroll
      for (int s = 0; s < 2; ++s)
#pragma unroll
        for (int e = 0; e < 4; ++e)
#pragma unroll
          for (int i = 0; i < 4; ++i) acc[s][e][i] = (f4){0.f, 0.f, 0.f, 0.f};
      unsigned xa[2], wa[4];
      xa[0] = (wm * 64 + l15) * 128 + ((g ^ (l15 >> 1)) << 4); xa[1] = xa[0] ^ 64;
#pragma unroll
      for (int e = 0; e < 4; ++e) wa[e] = (8 * g + qq) * 256 + (((4 * wn + e) ^ (qq | ((g & 1) << 2))) << 5) + pp * 8;
      Frag f0, f1;
      s4 lo_;
      auto ldpart = [&](Frag& f, int ks, int j) {
        const unsigned o = 16384 + ks * 8192;
        if (j == 0) f.x[0] = lds128(xa[ks]);
        if (j >= 1 && j <= 16) {
          const int s = (j - 1) >> 3, e = ((j - 1) >> 1) & 3;
          if (j & 1) lo_ = __builtin_amdgcn_ds_read_tr16_b64_v4i16((LAS s4*)(size_t)(wa[e] + o + s * 16384));
          else {
            const s4 hi = __builtin_amdgcn_ds_read_tr16_b64_v4i16((LAS s4*)(size_t)(wa[e] + o + s * 16384 + 1024));
            f.w[s][e] = __builtin_bit_cast(bf8, __builtin_shufflevector(lo_, hi, 0, 1, 2, 3, 4, 5, 6, 7));
          }
        }
        if (j >= 17 && j < 20) f.x[j - 16] = lds128(xa[ks] + (j - 16) * 2048);
      };
      auto mm1 = [&](const Frag& f, int j) { acc[(j >> 2) & 1][j & 3][j >> 3] = mfma(f.w[(j >> 2) & 1][j & 3], f.x[j >> 3], acc[(j >> 2) & 1][j & 3][j >> 3]); };
      auto second = [&](int st) {
        const int d = (st == 2 || st == 5) ? -2 * (int)SLOT : (int)SLOT;
#pragma unroll
        for (int j = 0; j < 32; ++j) {
          if (j == BAR_AT) { BAR(); SB(); }
          mm1(f0, j);
          if (j < 20) ldpart(f1, 1, j);
          if (j == 20) { xa[0] += d; OPAQUE(xa[0]); xa[1] += d; OPAQUE(xa[1]); }
          if (j == 21) { wa[0] += d; OPAQUE(wa[0]); wa[1] += d; OPAQUE(wa[1]); }
          if (j == 22) { wa[2] += d; OPAQUE(wa[2]); wa[3] += d; OPAQUE(wa[3]); }
          SB();
        }
        if (BAR_AT == 32) { BAR(); SB(); }
      };
      OPAQUE(xa[0]); OPAQUE(xa[1]); OPAQUE(wa[0]); OPAQUE(wa[1]); OPAQUE(wa[2]); OPAQUE(wa[3]);
      STAMP(2);
#pragma unroll
      for (int j = 0; j < 20; ++j) ldpart(f0, 0, j);
      SB();
      second(0);
#pragma unroll 1
      for (int st = 1; st < 8; ++st) {
#pragma unroll
        for (int j = 0; j < 32; ++j) { mm1(f1, j); if (j < 20) ldpart(f0, 0, j); SB(); }
        second(st);
      }
      STAMP(16);
      constexpr float C0 = -2.0f * 0.7978845608028654f * 1.4426950408889634f, C1 = C0 * 0.044715f;
      f4 cs[2][4];
      f2 rs[4];
      int ln1 = __builtin_amdgcn_mbcnt_hi(-1, __builtin_amdgcn_mbcnt_lo(-1, 0));
      OPAQUE(ln1);
      const int lane = ln1, l15 = lane & 15, g = lane >> 4;
#pragma unroll
      for (int j = 0; j < 32; ++j) {
        mm1(f1, j);
        if (j < 8) cs[j >> 2][j & 3] = lds128f(L_CS + ((j >> 2) * 128 + wn * 64 + (j & 3) * 16 + 4 * g) * 4);
        if (j >= 8 && j < 12) rs[j - 8] = *(const LAS f2*)(size_t)(L_RS + (wm * 64 + (j - 8) * 16 + l15) * 8);
        SB();
      }
      STAMP(3);
      const unsigned hso = WS_H + ((pm * 8 + wm * 4) * 56 + pn * 4 + wn * 2) * 1024 + lane * 16;
#pragma unroll
      for (int i = 0; i < 4; ++i) {
        const float m = -rs[i][0], rr = rs[i][1];
        const float k0 = C0 * rr, k1 = C1 * rr * rr * rr, i2 = __builtin_amdgcn_rcpf(rr * rr);
        const f2 m2 = {m, m}, k02 = {k0, k0}, k12 = {k1, k1}, i22 = {i2, i2};
        f2 a[8], c[8], z[8];
        SB();
#pragma unroll
        for (int j = 0; j < 8; ++j) {
          const int e = j >> 1, h = (j & 1) * 2;
          a[j] = __builtin_elementwise_fma(m2, (f2){cs[0][e][h], cs[0][e][h + 1]}, (f2){acc[0][e][i][h], acc[0][e][i][h + 1]});
        }
        SB();
#pragma unroll
        for (int j = 0; j < 8; ++j) z[j] = a[j] * a[j];
        SB();
#pragma unroll
        for (int j = 0; j < 8; ++j) z[j] = __builtin_elementwise_fma(z[j], k12, k02);
        SB();
#pragma unroll
        for (int j = 0; j < 8; ++j) z[j] = z[j] * a[j];
        SB();
#pragma unroll
        for (int j = 0; j < 8; ++j) {
          const int e = j >> 1, h = (j & 1) * 2;
          z[j] = (f2){__builtin_amdgcn_exp2f(z[j][0]), __builtin_amdgcn_exp2f(z[j][1])};
          c[j] = __builtin_elementwise_fma(m2, (f2){cs[1][e][h], cs[1][e][h + 1]}, (f2){acc[1][e][i][h], acc[1][e][i][h + 1]});
          SB();
        }
#pragma unroll
        for (int j = 0; j < 8; ++j) z[j] = __builtin_elementwise_fma(z[j], i22, i22);
        SB();
#pragma unroll
        for (int j = 0; j < 8; ++j) {
          z[j] = (f2){__builtin_amdgcn_rcpf(z[j][0]), __builtin_amdgcn_rcpf(z[j][1])};
          c[j] = c[j] * a[j];
          SB();
        }
#pragma unroll
        for (int j = 0; j < 8; ++j) c[j] = c[j] * z[j];
        SB();
        u4 o0, o1;
#pragma unroll
        for (int j = 0; j < 4; ++j) { o0[j] = pk2(c[j][0], c[j][1]); o1[j] = pk2(c[4 + j][0], c[4 + j][1]); }
        __builtin_amdgcn_raw_buffer_store_b128(o0, wsr, hso + i * 56 * 1024, 0, 16);
        __builtin_amdgcn_raw_buffer_store_b128(o1, wsr, hso + i * 56 * 1024 + 1024, 0, 16);
      }
      SB();
      ctl_add(C_EPI);
      tag_get();
      STAMP(4);
    } else {
      const unsigned o = ((xcd * 4 + r - 28) * 448 + gw * 112 + lane) * 128;
      if (PF_W) asm volatile("s_mov_b32 m0, %3\n\ts_nop 0\n\tglobal_load_lds_dword %0, %2\n\tglobal_load_lds_dword %1, %2"
                             ::"v"(o), "v"(o + (lane < 48 ? 8192 : 0)), "s"(wd), "s"(131072 + gw * 256) : "memory");
      if (PF_L2) {
#pragma unroll 1
        for (int j = (r - 28) * 4 + gw; j < PF_L2 * 16; j += 16) {
          const unsigned o2 = ((((j >> 2) & 3) * 14 + (j >> 4)) * 32 + (j & 3) * 8 + (lane >> 3)) * 1024 + (lane & 7) * 128;
          asm volatile("s_waitcnt vmcnt(0)\n\ts_mov_b32 m0, %2\n\ts_nop 0\n\tglobal_load_lds_dword %0, %1" ::"v"(o2), "s"(wd), "s"(131072 + gw * 256) : "memory");
        }
      }
      WAITV(0);
    }
    int ln2 = __builtin_amdgcn_mbcnt_hi(-1, __builtin_amdgcn_mbcnt_lo(-1, 0));
    OPAQUE(ln2);
    {
      const int lane = ln2, l15 = lane & 15, g = lane >> 4, qq = l15 >> 2, pp = lane & 3;
      const unsigned dv0 = (lane >> 3) * 1024 + (((lane & 7) ^ (g << 1)) * 16);
      f4 acc[4][4];
#pragma unroll
      for (int t = 0; t < 4; ++t)
#pragma unroll
        for (int i = 0; i < 4; ++i) acc[t][i] = (f4){0.f, 0.f, 0.f, 0.f};
      const unsigned da = (4 * g + qq) * 128 + (((2 * g + (qq >> 1)) & 3) << 5) + pp * 8;
      unsigned dlo[4];
#pragma unroll
      for (int t = 0; t < 4; ++t) { dlo[t] = (da ^ (t * 32)) + gw * 12288; OPAQUE(dlo[t]); }
      const unsigned dh = 2 * SLOT - gw * 4096;
      constexpr int WR = 6, HW = HWIN;
      bf8 wf[WR][4];
      auto waddr = [&](int i, int t) -> unsigned {
        if ((i & 7) >= 2) return dlo[t] + wslot(i);
        unsigned d_ = dh;
        asm volatile("" : "+s"(d_));
        unsigned a_ = dlo[t] + d_;
        OPAQUE(a_);
        return a_ + (i & 1) * 4096;
      };
      __builtin_amdgcn_s_setprio(2);
      if (g1) {
        WAITV(0);
        if (lane == 0 && __hip_atomic_fetch_add((LAS unsigned*)(size_t)C_HPUB, 1u, __ATOMIC_RELAXED, __HIP_MEMORY_SCOPE_WORKGROUP) == 3)
          flag_set(ws, WS_FH + (pm * 16 + pn) * 128, tag);
      }
      STAMP(5);
      ctl_wait(C_WD + (gw >> 1) * 4, 2);
      if (!g1) tag_get();
#pragma unroll
      for (int t = 0; t < WR * 4; ++t) { const unsigned a_ = waddr(t >> 2, t & 3); wf[t >> 2][t & 3] = ldtr(a_, a_ + 2048); }
      WAITL(0);
      STAMP(17);
#pragma unroll
      for (int i = 8; i < 14; ++i) wdstage(dv0, gw, i);
      __builtin_amdgcn_s_setprio(0);
      STAMP(9);
      poll(ws, WS_FH + ((rt >> 1) * 16 + ((gw * 7) >> 1) + (lane & 3)) * 128, tag);
      STAMP(6);
      u4 hf[HW][4];
      unsigned ho[4];
#pragma unroll
      for (int t = 0; t < 4; ++t) ho[t] = WS_H + ((rt * 4 + t) * 56 + gw * 14) * 1024 + lane * 16;
      auto ldh1 = [&](int i, int t) { hf[i % HW][t] = __builtin_amdgcn_raw_buffer_load_b128(wsr, ho[t] + (i & 3) * 1024, (i >> 2) * 4096, 16); };
#pragma unroll
      for (int i = 0; i < HW * 4; ++i) { ldh1(i >> 2, i & 3); SB(); }
      s4 lo_;
      unsigned wa_;
#pragma unroll
      for (int i = 0; i < 14; ++i) {
        const int nw = i + WR - 1;
        if (i == 11) { tail_load(); SB(); }
#pragma unroll
        for (int j = 0; j < 16; ++j) {
          const int ti = j >> 2, t = j & 3;
          acc[t][ti] = mfma(wf[i % WR][t], __builtin_bit_cast(bf8, hf[i % HW][ti]), acc[t][ti]);
          if (i >= 1 && nw < 14 && t != 3 && (j - ti) < 8) {
            const int e = j - ti;
            if (!(e & 1)) wa_ = waddr(nw, e >> 1);
            const s4 v_ = __builtin_amdgcn_ds_read_tr16_b64_v4i16((LAS s4*)(size_t)(wa_ + (e & 1) * 2048));
            if (e & 1) wf[nw % WR][e >> 1] = __builtin_bit_cast(bf8, __builtin_shufflevector(lo_, v_, 0, 1, 2, 3, 4, 5, 6, 7)); else lo_ = v_;
          }
          if (t == 3 && i + HW < 14) ldh1(i + HW, ti);
          SB();
        }
        if (i == 0) STAMP(19);
      }
      STAMP(7);
#pragma unroll
      for (int ti = 0; ti < 4; ++ti)
#pragma unroll
        for (int t = 0; t < 4; ++t)
          sts128f((ti >> 1) * SLOT + gw * 12288 + ((ti & 1) * 16 + l15) * 256 + (((4 * t + g) ^ (l15 & 7)) << 4), acc[t][ti]);
    }
    STAMP(8);
  } else {
    const int t7 = tid & 127, bw = w & 1;
    unsigned dcr;
    if (w < 6) {
      unsigned xv[4], wv[4];
      stage_offsets(xv, wv);
      auto stage = [&](int st) { quarter(xv, wv, st, 2 * bw); quarter(xv, wv, st, 2 * bw + 1); };
      const unsigned dv0 = (lane >> 3) * 1024 + (((lane & 7) ^ (g << 1)) * 16);
      WAITV(12);
      STAMP(1);
      BAR();
      if (g1) { quarter(xv, wv, 1, 2 * bw + 1); stage(2); WAITV(24); }
      KBAR();
#pragma unroll 1
      for (int st = 3; st < 8; ++st) {
        if (g1) { stage(st); WAITV(24); }
        KBAR();
      }
      STAMP(13);
      if (!g1) ctl_wait(C_TAG, 2);
      asm volatile("global_load_dword %0, %1, %2" : "=&v"(dcr) : "v"(0u), "s"(sD + b * 4) : "memory");
#pragma unroll
      for (int i = 0; i < 4; ++i) wdstage(dv0, bw * 2 + (i & 1), i >> 1);
      WAITV(17);
      KBAR();
      STAMP(3);
#pragma unroll
      for (int i = 0; i < 6; ++i) wdstage(dv0, bw * 2 + (i & 1), 2 + (i >> 1));
      KBAR();
      STAMP(19);
      tag_get();
      asm volatile("s_waitcnt vmcnt(32)" : "+v"(dcr)::"memory");
#pragma unroll
      for (int i = 0; i < 6; ++i) wdstage(dv0, bw * 2 + (i & 1), 5 + (i >> 1));
      WAITV(24);
      ctl_add(C_WD + bw * 4);
      STAMP(2);
      WAITV(0);
      ctl_add(C_WD + bw * 4);
      STAMP(4);
    } else {
      if (t7 < 48) *(LAS unsigned*)(size_t)(L_CTL + t7 * 4) = 0;
      const int ptt = b >> 3, pct = b & 7;
      const int rgrp = (xcd >> 1) * 64 + (xcd & 1) * 32 + r;
      const int cst = 2 * ((r + pnl) & 3) + ((xcd >> 1) & 1);
      const unsigned hoff = (2 * b + bw) * 4096 + lane * 16;
      u4 xr[4];
      auto rows_load = [&]() {
        asm volatile("global_load_dwordx4 %0, %4, %5\n\tglobal_load_dwordx4 %1, %4, %5 offset:1024\n\tglobal_load_dwordx4 %2, %4, %5 offset:2048\n\t"
                     "global_load_dwordx4 %3, %4, %5 offset:3072"
                     : "=&v"(xr[0]), "=&v"(xr[1]), "=&v"(xr[2]), "=&v"(xr[3]) : "v"(lane * 16), "s"(q + (rgrp * 8 + (w & 1) * 4) * 1024) : "memory");
      };
      auto rows_stat = [&]() {
        asm volatile("" : "+v"(xr[0]), "+v"(xr[1]), "+v"(xr[2]), "+v"(xr[3])::"memory");
        float e[4][8], mu[4], va[4];
#pragma unroll
        for (int i = 0; i < 4; ++i) {
#pragma unroll
          for (int j = 0; j < 4; ++j) { e[i][2 * j] = bflo(xr[i][j]); e[i][2 * j + 1] = bfhi(xr[i][j]); }
          mu[i] = fadd(fadd(fadd(e[i][0], e[i][1]), fadd(e[i][2], e[i][3])), fadd(fadd(e[i][4], e[i][5]), fadd(e[i][6], e[i][7])));
        }
        auto fold4 = [&](const float (&v)[4]) {
          const auto s0 = __builtin_amdgcn_permlane16_swap(__float_as_uint(v[0]), __float_as_uint(v[1]), false, false);
          const auto s1 = __builtin_amdgcn_permlane16_swap(__float_as_uint(v[2]), __float_as_uint(v[3]), false, false);
          const float p0 = fadd(__uint_as_float(s0[0]), __uint_as_float(s0[1])), p1 = fadd(__uint_as_float(s1[0]), __uint_as_float(s1[1]));
          const auto s2 = __builtin_amdgcn_permlane32_swap(__float_as_uint(p0), __float_as_uint(p1), false, false);
          float t = fadd(__uint_as_float(s2[0]), __uint_as_float(s2[1]));
          t = dpp_add<0xB1>(t); t = dpp_add<0x4E>(t); t = dpp_add<0x141>(t); t = dpp_add<0x140>(t);
          return t * (1.0f / 512.0f);
        };
        const float mg = fold4(mu);
#pragma unroll
        for (int i = 0; i < 4; ++i) {
          const float m = __int_as_float(__builtin_amdgcn_readlane(__float_as_int(mg), 16 * i));
          float s = 0.f;
#pragma unroll
          for (int j = 0; j < 8; ++j) { const float d = e[i][j] - m; s = ffma(d, d, s); }
          va[i] = s;
        }
        const float vg = fold4(va);
        const u2 o = {__float_as_uint(rbf(mg)), __float_as_uint(__builtin_amdgcn_rcpf(rbf(__builtin_amdgcn_sqrtf(rbf(rbf(vg) + 1e-5f)))))};
        if (l15 == 0) __builtin_amdgcn_raw_buffer_store_b64(o, wsr, WS_ROW + (rgrp * 8 + (w & 1) * 4 + g) * 8, 0, 16);
        asm volatile("" ::: "memory");
      };

      u4 wsh[8], hc[4], uc[4], c1[4]; unsigned hp[4];
      {
        const char* wb = ((xcd >> 2) ? wu : wg) + (g1 ? pn * 256 + (cst * 64 + bw * 32) * 3584 : 0);
        const unsigned o = g * 3584 + l15 * 16;
        if (SIDE_SLEEP) __builtin_amdgcn_s_sleep(SIDE_SLEEP);
        asm volatile("global_load_dwordx4 %0, %8, %12\n\tglobal_load_dwordx4 %1, %9, %12\n\tglobal_load_dwordx4 %2, %10, %12\n\tglobal_load_dwordx4 %3, %11, %12\n\t"
                     "global_load_dwordx4 %4, %8, %13\n\tglobal_load_dwordx4 %5, %9, %13\n\tglobal_load_dwordx4 %6, %10, %13\n\tglobal_load_dwordx4 %7, %11, %13"
                     : "=&v"(wsh[0]), "=&v"(wsh[1]), "=&v"(wsh[2]), "=&v"(wsh[3]), "=&v"(wsh[4]), "=&v"(wsh[5]), "=&v"(wsh[6]), "=&v"(wsh[7])
                     : "v"(o), "v"(o + 4 * 3584), "v"(o + 8 * 3584), "v"(o + 12 * 3584), "s"(wb), "s"(wb + 16 * 3584) : "memory");
        rows_load();
      }
      asm volatile("s_load_dwordx2 %0, %1, 0x0\n\ts_waitcnt lgkmcnt(0)" : "=s"(tag) : "s"(ws) : "memory");
      tag += 1;
      *(LAS u64*)(size_t)L_TAG = tag;
      BAR();
#pragma unroll
      for (int z = 0; z < 4; ++z) {
        const int id = t7 + 128 * z, c2 = id >> 3, ch = id & 7;
        const u4 t = lds128u(L_MISC + c2 * 128 + ((ch ^ (c2 & 7)) << 4));
        __builtin_amdgcn_raw_buffer_store_b128(t, wsr, WS_KVT + (pct * 64 + c2) * 4096 + 3968 - ptt * 128 + ch * 16, 0, 16);
      }
      asm volatile("s_waitcnt vmcnt(4)" : "+v"(wsh[0]), "+v"(wsh[1]), "+v"(wsh[2]), "+v"(wsh[3]), "+v"(wsh[4]), "+v"(wsh[5]), "+v"(wsh[6]), "+v"(wsh[7])::"memory");
      STAMP(17);
      if (g1) {
        float cacc[8];
#pragma unroll
        for (int j = 0; j < 4; ++j) {
          cacc[2 * j] = fadd(fadd(fadd(bflo(wsh[0][j]), bflo(wsh[1][j])), fadd(bflo(wsh[2][j]), bflo(wsh[3][j]))),
                             fadd(fadd(bflo(wsh[4][j]), bflo(wsh[5][j])), fadd(bflo(wsh[6][j]), bflo(wsh[7][j]))));
          cacc[2 * j + 1] = fadd(fadd(fadd(bfhi(wsh[0][j]), bfhi(wsh[1][j])), fadd(bfhi(wsh[2][j]), bfhi(wsh[3][j]))),
                                 fadd(fadd(bfhi(wsh[4][j]), bfhi(wsh[5][j])), fadd(bfhi(wsh[6][j]), bfhi(wsh[7][j]))));
        }
        float c4[4], c2[2];
#pragma unroll
        for (int j = 0; j < 4; ++j) {
          const auto s2 = __builtin_amdgcn_permlane16_swap(__float_as_uint(cacc[j]), __float_as_uint(cacc[4 + j]), false, false);
          c4[j] = fadd(__uint_as_float(s2[0]), __uint_as_float(s2[1]));
        }
#pragma unroll
        for (int j = 0; j < 2; ++j) {
          const auto s2 = __builtin_amdgcn_permlane32_swap(__float_as_uint(c4[j]), __float_as_uint(c4[2 + j]), false, false);
          c2[j] = fadd(__uint_as_float(s2[0]), __uint_as_float(s2[1]));
        }
        __builtin_amdgcn_raw_buffer_store_b64((u2){__float_as_uint(c2[0]), __float_as_uint(c2[1])}, wsr,
                                              WS_COL + ((pn * 16 + cst * 2 + bw) * 256 + (xcd >> 2) * 128 + l15 * 8 + (g & 1) * 4 + (g >> 1) * 2) * 4, 0, 16);
      }
      STAMP(10);
      KBAR();
      rows_stat();
      STAMP(18);
      asm volatile("global_load_dwordx4 %0, %9, %11\n\tglobal_load_dwordx4 %1, %9, %11 offset:1024\n\tglobal_load_dwordx4 %2, %9, %11 offset:2048\n\t"
                   "global_load_dwordx4 %3, %9, %11 offset:3072\n\tglobal_load_dword %4, %10, %11\n\tglobal_load_dword %5, %9, %11 offset:1020\n\t"
                   "global_load_dword %6, %9, %11 offset:2044\n\tglobal_load_dword %7, %9, %11 offset:3068\n\tglobal_load_dword %8, %12, %13"
                   : "=&v"(hc[0]), "=&v"(hc[1]), "=&v"(hc[2]), "=&v"(hc[3]), "=&v"(hp[0]), "=&v"(hp[1]), "=&v"(hp[2]), "=&v"(hp[3]), "=&v"(dcr)
                   : "v"(hoff), "v"(lane ? hoff - 4 : hoff), "s"(ssk), "v"(0u), "s"(sD + b * 4) : "memory");
      KBAR();
      WAITV(9);
      KBAR();
      if (!g1) { ctl_add(C_WSYNC); ctl_spin(C_WSYNC, 2); }
      if (tid == 384 || tid == 385) flag_set(ws, tid == 384 ? WS_FS + rgrp * 128 : WS_FK + (pct * 32 + ptt) * 128, tag);
      STAMP(20);
      unsigned fo = lane < 32 || !g1 ? WS_FK + ((b >> 5) * 32 + (lane & 31)) * 128
                                     : WS_FS + (lane < 48 ? pm * 16 + l15 : (l15 >> 2) * 64 + (xcd & 1) * 32 + (l15 & 3) + 4 * pnl) * 128;
      unsigned fo2 = WS_FS + (rt * 8 + (lane & 7)) * 128;
      OPAQUE(fo); OPAQUE(fo2);
      KBAR();
      asm volatile("s_waitcnt vmcnt(0)" : "+v"(hc[0]), "+v"(hc[1]), "+v"(hc[2]), "+v"(hc[3]), "+v"(hp[0]), "+v"(hp[1]), "+v"(hp[2]), "+v"(hp[3]), "+v"(dcr)::"memory");
      if (lane == 0) hp[0] = 0;
#pragma unroll
      for (int z = 0; z < 4; ++z) {
        c1[z] = (u4){__builtin_amdgcn_alignbit(hc[z][0], hp[z], 16), __builtin_amdgcn_alignbit(hc[z][1], hc[z][0], 16),
                     __builtin_amdgcn_alignbit(hc[z][2], hc[z][1], 16), __builtin_amdgcn_alignbit(hc[z][3], hc[z][2], 16)};
        OPAQUE(c1[z]);
      }
      u64 p0, p1;
      if (!g1) __builtin_amdgcn_s_sleep(70);
      if (PEEK_AT == 5) KBAR();
      asm volatile("global_load_dwordx2 %0, %2, %4 sc1\n\tglobal_load_dwordx2 %1, %3, %4 sc1" : "=&v"(p0), "=&v"(p1) : "v"(fo), "v"(fo2), "s"(ws) : "memory");
      KBAR();
      STAMP(23);
      asm volatile("s_waitcnt vmcnt(0)" : "+v"(p0), "+v"(p1)::"memory");
      if (!__all(p0 == tag && p1 == tag)) poll2(ws, fo, fo2, tag);
      if (!g1) ctl_add(C_TAG);
      STAMP(21);
      u2 rsv, csv[16];
      {
        const unsigned co = WS_COL + (g1 ? pn : 0) * 16384 + t7 * 8;
#define LD4(o, vreg) asm volatile("global_load_dwordx2 %0, %4, %5 sc1\n\tglobal_load_dwordx2 %1, %4, %5 offset:1024 sc1\n\t" \
                                  "global_load_dwordx2 %2, %4, %5 offset:2048 sc1\n\tglobal_load_dwordx2 %3, %4, %5 offset:3072 sc1" \
                                  : "=&v"(csv[o]), "=&v"(csv[o + 1]), "=&v"(csv[o + 2]), "=&v"(csv[o + 3]) : "v"(vreg), "s"(ws) : "memory")
        LD4(0, co); LD4(4, co + 4096); LD4(8, co + 8192); LD4(12, co + 12288);
        asm volatile("global_load_dwordx2 %0, %1, %2 sc1" : "=&v"(rsv) : "v"(WS_ROW + (g1 ? pm : 0) * 1024 + t7 * 8), "s"(ws) : "memory");
      }
      asm volatile("global_load_dwordx4 %0, %4, %5 sc1\n\tglobal_load_dwordx4 %1, %4, %5 offset:1024 sc1\n\tglobal_load_dwordx4 %2, %4, %5 offset:2048 sc1\n\t"
                   "global_load_dwordx4 %3, %4, %5 offset:3072 sc1" : "=&v"(uc[0]), "=&v"(uc[1]), "=&v"(uc[2]), "=&v"(uc[3]) : "v"(WS_KVT + hoff), "s"(ws) : "memory");
      if (PEEK_AT == 4) KBAR();
      const unsigned base = L_CV + bw * CV_CH;
#pragma unroll
      for (int z = 0; z < 4; ++z) {
        sts128(base + 32 + lane * 16 + z * 1024, hc[z]);
        sts128(base + 32 + lane * 16 + z * 1024 + CV_COPY, c1[z]);
      }
      if (lane < 4) sts128(base + (lane >> 1) * CV_COPY + (lane & 1) * 16, (u4){0, 0, 0, 0});
      if (lane >= 32) sts128(base + CV_UR + 4096 + (lane - 32) * 16, (u4){0, 0, 0, 0});
      STAMP(14);
      KBAR();
      asm volatile("s_waitcnt vmcnt(4)" : "+v"(rsv), "+v"(csv[0]), "+v"(csv[1]), "+v"(csv[2]), "+v"(csv[3]), "+v"(csv[4]), "+v"(csv[5]), "+v"(csv[6]), "+v"(csv[7]),
                   "+v"(csv[8]), "+v"(csv[9]), "+v"(csv[10]), "+v"(csv[11]), "+v"(csv[12]), "+v"(csv[13]), "+v"(csv[14]), "+v"(csv[15])::"memory");
      {
        *(LAS u2*)(size_t)(L_RS + t7 * 8) = rsv;
        float s0[4], s1[4];
#pragma unroll
        for (int i = 0; i < 4; ++i) {
          s0[i] = fadd(fadd(__uint_as_float(csv[i][0]), __uint_as_float(csv[4 + i][0])), fadd(__uint_as_float(csv[8 + i][0]), __uint_as_float(csv[12 + i][0])));
          s1[i] = fadd(fadd(__uint_as_float(csv[i][1]), __uint_as_float(csv[4 + i][1])), fadd(__uint_as_float(csv[8 + i][1]), __uint_as_float(csv[12 + i][1])));
        }
        *(LAS u2*)(size_t)(L_CS + t7 * 8) = (u2){__float_as_uint(fadd(fadd(s0[0], s0[1]), fadd(s0[2], s0[3]))), __float_as_uint(fadd(fadd(s1[0], s1[1]), fadd(s1[2], s1[3])))};
      }
      STAMP(16);
      KBAR();
      if (b == 255 && bw == 1) {
#pragma unroll 1
        for (int z = 0; z < 4; ++z) poll(ws, WS_FS + (z * 64 + lane) * 128, tag);
        if (lane == 0) flag_set(ws, WS_EPOCH, tag);
      }
      asm volatile("s_waitcnt vmcnt(0)" : "+v"(uc[0]), "+v"(uc[1]), "+v"(uc[2]), "+v"(uc[3])::"memory");
#pragma unroll
      for (int z = 0; z < 4; ++z) sts128(base + CV_UR + lane * 16 + z * 1024, uc[z]);
      ctl_add(C_CVIN);
      STAMP(15);
    }
    ctl_wait(C_CVIN, 2);
    const int ch = gw >> 1, half = gw & 1;
    const unsigned base = L_CV + ch * CV_CH;
    const unsigned o = 8 * g + l15 + 1, kap = o & 1;
    unsigned aa = base + kap * CV_COPY + 2 * (o + kap) + half * 256, ab = aa + 8;
    unsigned ba = base + CV_UR + 2 * (2032 - 16 * l15 + 8 * g) - 3584 + half * 256;
    OPAQUE(aa); OPAQUE(ab); OPAQUE(ba);
    f4 y[8];
#pragma unroll
    for (int n = 0; n < 8; ++n) y[n] = (f4){0.f, 0.f, 0.f, 0.f};
    bf8 A[4], B[8];
    auto lda = [&](int qi) {
      const int off = (qi & 7) * 512 + (qi >> 3) * 64;
      const u2a lo = *(const LAS u2a*)(size_t)(aa + off), hi = *(const LAS u2a*)(size_t)(ab + off);
      A[qi & 3] = __builtin_bit_cast(bf8, (u4){lo[0], lo[1], hi[0], hi[1]});
    };
#pragma unroll
    for (int i = 0; i < 8; ++i) { B[7 - i] = lds128(ba + i * 512); if (i < 3) lda(i); }
    SB();
    if (g1) ctl_spin(C_EPI, 4);
    STAMP(5);
    __builtin_amdgcn_s_setprio(1);
#pragma unroll 1
    for (int rho = 0; rho < 4; ++rho) {
#pragma unroll
      for (int qi = 0; qi < 8; ++qi) {
#pragma unroll
        for (int p = 7 - qi; p >= 0; --p) {
          y[qi + p] = mfma(A[qi & 3], B[p], y[qi + p]);
          if (p == 7 - qi) { lda(qi + 3); B[p] = lds128(ba + (7 - p) * 512 + 64); }
          SB();
        }
      }
      aa += 64; ab += 64; ba += 64;
      OPAQUE(aa); OPAQUE(ab); OPAQUE(ba);
    }
    STAMP(6);
    ctl_add(C_PAIR + ch * 4); ctl_spin(C_PAIR + ch * 4, 2);
    const unsigned ex0 = base + half * 4096, ex1 = base + (half ^ 1) * 4096;
#pragma unroll
    for (int n = 0; n < 4; ++n) {
      if (half) { const f4 t = y[n]; y[n] = y[4 + n]; y[4 + n] = t; }
      sts128f(ex0 + n * 1024 + lane * 16, y[4 + n]);
    }
    u2 uu[4];
#pragma unroll
    for (int n = 0; n < 4; ++n) uu[n] = *(const LAS u2*)(size_t)(base + CV_UR + 2 * (2044 - (256 * (4 * half + n) + 16 * l15 + 4 * g)));
    ctl_add(C_PAIR + ch * 4);
    const float dc = ch ? bfhi(dcr) : bflo(dcr);
#pragma unroll
    for (int n = 0; n < 4; ++n) {
      y[n][0] = ffma(bfhi(uu[n][1]), dc, y[n][0]); y[n][1] = ffma(bflo(uu[n][1]), dc, y[n][1]);
      y[n][2] = ffma(bfhi(uu[n][0]), dc, y[n][2]); y[n][3] = ffma(bflo(uu[n][0]), dc, y[n][3]);
    }
    ctl_spin(C_PAIR + ch * 4, 4);
    STAMP(11);
    u2 pk[4];
#pragma unroll
    for (int n = 0; n < 4; ++n) {
      const f4 t = lds128f(ex1 + n * 1024 + lane * 16);
      pk[n] = (u2){pk2(fadd(t[0], y[n][0]), fadd(t[1], y[n][1])), pk2(fadd(t[2], y[n][2]), fadd(t[3], y[n][3]))};
    }
#pragma unroll
    for (int n = 0; n < 4; n += 2) {
      const auto s0 = __builtin_amdgcn_permlane16_swap(pk[n][0], pk[n + 1][0], false, false);
      const auto s1 = __builtin_amdgcn_permlane16_swap(pk[n][1], pk[n + 1][1], false, false);
      const u4 o4 = {s0[0], s1[0], s0[1], s1[1]};
      __builtin_amdgcn_raw_buffer_store_b128(o4, wsr, WS_YT + (2 * b + ch) * 4096 + 2 * (256 * (4 * half + n + (g & 1)) + 16 * l15 + 8 * (g >> 1)), 0, 16);
    }
    WAITV(0);
    ctl_add(C_BSYNC);
    ctl_spin(C_BSYNC, 4);
    if (tid == 256) flag_set(ws, WS_FY + b * 128, tag);
    STAMP(7);
    tail_load();
    if (gw == 3) {
      poll(ws, WS_FY + (ct2 * 32 + (lane & 31)) * 128, tag);
      ctl_add(C_GO);
    } else ctl_wait(C_GO, 1);
    STAMP(8);
    u4 yq[2];
#pragma unroll
    for (int z = 0; z < 2; ++z) {
      const int id = t8 + 256 * z;
      yq[z] = __builtin_amdgcn_raw_buffer_load_b128(wsr, WS_YT + (ct2 * 64 + (id >> 3)) * 4096 + rt * 128 + (id & 7) * 16, 0, 16);
    }
#pragma unroll
    for (int z = 0; z < 2; ++z) {
      const int id = t8 + 256 * z, c = id >> 3;
      sts128(L_YI + c * 128 + (((id & 7) ^ (c >> 3)) << 4), yq[z]);
    }
    __builtin_amdgcn_s_setprio(0);
    STAMP(9);
  }
  f2 xn[4];
#pragma unroll
  for (int j = 0; j < 4; ++j) {
    const float m = __uint_as_float(st2[0]), rr = __uint_as_float(st2[1]);
    const unsigned d = pk2(bflo(xq[j]) - m, bfhi(xq[j]) - m);
    const unsigned n = pk2(bflo(d) * rr, bfhi(d) * rr);
    xn[j] = (f2){bflo(n), bfhi(n)};
    OPAQUE(xn[j]);
  }
  BAR();
  if (w < 4) STAMP(10);
  {
    int ln3 = __builtin_amdgcn_mbcnt_hi(-1, __builtin_amdgcn_mbcnt_lo(-1, 0));
    OPAQUE(ln3);
    const int tk = w * 8 + (ln3 >> 3), c8 = ln3 & 7;
    const unsigned pb = (w >> 2) * SLOT + (tk & 31) * 256;
    f4 o0 = lds128f(pb + (((2 * c8) ^ (tk & 7)) << 4)), o1 = lds128f(pb + (((2 * c8 + 1) ^ (tk & 7)) << 4));
#pragma unroll
    for (int k4 = 1; k4 < 4; ++k4) {
      o0 += lds128f(pb + k4 * 12288 + (((2 * c8) ^ (tk & 7)) << 4));
      o1 += lds128f(pb + k4 * 12288 + (((2 * c8 + 1) ^ (tk & 7)) << 4));
    }
    const unsigned ya = L_YI + c8 * 1024 + ((w ^ c8) << 4) + (tk & 7) * 2;
    unsigned yv[8];
#pragma unroll
    for (int j = 0; j < 8; ++j) yv[j] = *(const LAS unsigned short*)(size_t)(ya + j * 128);
    f2 o[4] = {{o0[0], o0[1]}, {o0[2], o0[3]}, {o1[0], o1[1]}, {o1[2], o1[3]}}, e[4], sg[4];
#pragma unroll
    for (int j = 0; j < 4; ++j) {
      const f2 z = o[j] * -1.4426950408889634f;
      e[j] = (f2){__builtin_amdgcn_exp2f(z[0]), __builtin_amdgcn_exp2f(z[1])} + 1.0f;
      sg[j] = xn[j] * (f2){bflo(yv[2 * j]), bflo(yv[2 * j + 1])} * o[j];
    }
    u4 res;
#pragma unroll
    for (int j = 0; j < 4; ++j) {
      const f2 t = __builtin_elementwise_fma(sg[j], (f2){__builtin_amdgcn_rcpf(e[j][0]), __builtin_amdgcn_rcpf(e[j][1])}, (f2){bflo(xq[j]), bfhi(xq[j])});
      res[j] = pk2(t[0], t[1]);
    }
    *(GAS u4*)(out + (rt * 64 + tk) * 1024 + ct2 * 128 + c8 * 16) = res;
  }
  if (w < 4) STAMP(11);
  asm volatile("s_waitcnt vmcnt(0)" ::: "memory");
  STAMP(12);
  if (b == PROBE_B && w == PROBE_W) {
    const unsigned until = tt[12] + 8u * (tt[PROBE_I] - tt[0]);
    for (int it = 0; it < (1 << 20); ++it) { u64 t_; asm volatile("s_memrealtime %0\n\ts_waitcnt lgkmcnt(0)" : "=s"(t_)::"memory"); if ((int)((unsigned)t_ - until) >= 0) break; }
  }
}
}

extern "C" void kernel_launch(void* const* d_in, const int* in_sizes, int n_in, void* d_out, int out_size, void* d_ws, size_t ws_size,
                              hipStream_t stream) {
  (void)in_sizes; (void)n_in; (void)out_size; (void)ws_size;
  (void)hipFuncSetAttribute((const void*)h3_geglu_fused, hipFuncAttributeMaxDynamicSharedMemorySize, LDS_BYTES);
  h3_geglu_fused<<<256, 512, LDS_BYTES, stream>>>((const char*)d_in[0], (const char*)d_in[1], (const char*)d_in[2], (const char*)d_in[3],
                                                  (const char*)d_in[4], (const char*)d_in[5], (const char*)d_in[7], (const char*)d_in[8],
                                                  (const char*)d_in[9], (char*)d_out, (char*)d_ws);
}
```

```cpp
#include <hip/hip_runtime.h>
#include <stdint.h>

#define LAS __attribute__((address_space(3)))
#define GAS __attribute__((address_space(1)))
#define DI __device__ __forceinline__
typedef __bf16 bf8 __attribute__((ext_vector_type(8)));
typedef __bf16 bf2 __attribute__((ext_vector_type(2)));
typedef short s4 __attribute__((ext_vector_type(4)));
typedef float f4 __attribute__((ext_vector_type(4)));
typedef float f2 __attribute__((ext_vector_type(2)));
typedef unsigned u4 __attribute__((ext_vector_type(4)));
typedef unsigned u2 __attribute__((ext_vector_type(2)));
typedef unsigned u2a __attribute__((ext_vector_type(2), aligned(4)));
typedef unsigned long long u64;

namespace {
constexpr unsigned WS_EPOCH = 0;
constexpr unsigned WS_FK = 4096;
constexpr unsigned WS_FS = WS_FK + 256 * 128;
constexpr unsigned WS_FH = WS_FS + 256 * 128;
constexpr unsigned WS_FY = WS_FH + 256 * 128;
constexpr unsigned WS_ROW = WS_FY + 256 * 128;
constexpr unsigned WS_COL = WS_ROW + 2048 * 8;
constexpr unsigned WS_KVT = 1u << 20;
constexpr unsigned WS_YT = 3u << 20;
constexpr unsigned WS_H = 5u << 20;
constexpr unsigned WS_PROF = 32u << 20;
constexpr unsigned SLOT = 49152;
constexpr unsigned L_MISC = 3 * SLOT;
constexpr unsigned L_RS = 159744;
constexpr unsigned L_CS = L_RS + 1024;
constexpr unsigned L_CTL = L_CS + 1024;
constexpr unsigned C_LAND = L_CTL, C_CONS = L_CTL + 40, C_BSYNC = L_CTL + 72, C_PRE = L_CTL + 76, C_WSYNC = L_CTL + 80, C_EPI = L_CTL + 84,
                   C_HPUB = L_CTL + 88, C_GO = L_CTL + 92, C_WD = L_CTL + 96, C_CVIN = L_CTL + 104, C_TAG = L_CTL + 108, C_PAIR = L_CTL + 112,
                   C_RD = L_CTL + 128, C_LD = L_CTL + 144, C_HOK = L_CTL + 160, L_TAG = L_CTL + 192;
constexpr unsigned CV_COPY = 4160, CV_UR = 2 * CV_COPY, CV_CH = CV_UR + 4608;
constexpr unsigned L_CV = L_RS - 2 * CV_CH;
static_assert(L_CV >= 131072, "W_down ring");
constexpr unsigned L_YI = L_CV;
constexpr unsigned LDS_BYTES = 163840;
constexpr int SPIN_CAP = 1 << 18;

DI float bflo(unsigned u) { return __uint_as_float(u << 16); }
DI float bfhi(unsigned u) { return __uint_as_float(u & 0xffff0000u); }
DI unsigned pk2(float a, float b) { f2 v = {a, b}; return __builtin_bit_cast(unsigned, __builtin_convertvector(v, bf2)); }
DI float dot2(unsigned a, bf2 b, float c) { return __builtin_amdgcn_fdot2_f32_bf16(__builtin_bit_cast(bf2, a), b, c, false); }
DI float rbf(float a) { return bflo(pk2(a, a)); }
DI f4 mfma(bf8 a, bf8 b, f4 c) { return __builtin_amdgcn_mfma_f32_16x16x32_bf16(a, b, c, 0, 0, 0); }
DI bf8 lds128(unsigned a) { return *(const LAS bf8*)(size_t)a; }
DI u4 lds128u(unsigned a) { return *(const LAS u4*)(size_t)a; }
DI f4 lds128f(unsigned a) { return *(const LAS f4*)(size_t)a; }
DI void sts128(unsigned a, u4 v) { *(LAS u4*)(size_t)a = v; }
DI void sts128f(unsigned a, f4 v) { *(LAS f4*)(size_t)a = v; }
DI bf8 ldtr(unsigned a0, unsigned a1) {
  s4 lo = __builtin_amdgcn_ds_read_tr16_b64_v4i16((LAS s4*)(size_t)a0);
  s4 hi = __builtin_amdgcn_ds_read_tr16_b64_v4i16((LAS s4*)(size_t)a1);
  return __builtin_bit_cast(bf8, __builtin_shufflevector(lo, hi, 0, 1, 2, 3, 4, 5, 6, 7));
}
#define WAITV(n) asm volatile("s_waitcnt vmcnt(%0)" ::"i"(n) : "memory")
#define WAITL(n) asm volatile("s_waitcnt lgkmcnt(%0)" ::"i"(n) : "memory")
#define SB() __builtin_amdgcn_sched_barrier(0)
#define OPAQUE(x) asm volatile("" : "+v"(x))
#define BAR() do { asm volatile("s_waitcnt lgkmcnt(0)" ::: "memory"); __builtin_amdgcn_s_barrier(); asm volatile("" ::: "memory"); } while (0)

DI void glds2(unsigned v0, unsigned v1, const char* sb, unsigned l) {
  asm volatile("s_mov_b32 m0, %3\n\ts_nop 0\n\tglobal_load_lds_dwordx4 %0, %2\n\tglobal_load_lds_dwordx4 %1, %2 offset:1024"
               ::"v"(v0), "v"(v1), "s"(sb), "s"(l) : "memory");
}
DI void glds4(unsigned v0, unsigned v1, unsigned v2, unsigned v3, const char* sb, unsigned l) {
  asm volatile("s_mov_b32 m0, %5\n\ts_nop 0\n\tglobal_load_lds_dwordx4 %0, %4\n\tglobal_load_lds_dwordx4 %1, %4 offset:1024\n\t"
               "global_load_lds_dwordx4 %2, %4 offset:2048\n\tglobal_load_lds_dwordx4 %3, %4 offset:3072"
               ::"v"(v0), "v"(v1), "v"(v2), "v"(v3), "s"(sb), "s"(l) : "memory");
}
DI void flag_set(char* ws, unsigned off, u64 tag) {
  __hip_atomic_store((GAS u64*)(ws + off), tag, __ATOMIC_RELAXED, __HIP_MEMORY_SCOPE_AGENT);
}
DI void poll(const char* ws, unsigned off, u64 tag) {
#pragma unroll 1
  for (int it = 0; it < SPIN_CAP; ++it) {
    u64 x = __hip_atomic_load((GAS u64*)(ws + off), __ATOMIC_RELAXED, __HIP_MEMORY_SCOPE_AGENT);
    if (__all(x == tag)) break;
    __builtin_amdgcn_s_sleep(4);
  }
  asm volatile("" ::: "memory");
}
DI float fadd(float a, float b) { float r; asm("v_add_f32 %0, %1, %2" : "=v"(r) : "v"(a), "v"(b)); return r; }
DI float fsub(float a, float b) { float r; asm("v_sub_f32 %0, %1, %2" : "=v"(r) : "v"(a), "v"(b)); return r; }
DI float fmul(float a, float b) { float r; asm("v_mul_f32 %0, %1, %2" : "=v"(r) : "v"(a), "v"(b)); return r; }
DI float ffma(float a, float b, float c) { float r; asm("v_fma_f32 %0, %1, %2, %3" : "=v"(r) : "v"(a), "v"(b), "v"(c)); return r; }
DI void poll2(const char* ws, unsigned off0, unsigned off1, u64 tag) {
#pragma unroll 1
  for (int it = 0; it < SPIN_CAP; ++it) {
    const u64 x = __hip_atomic_load((GAS u64*)(ws + off0), __ATOMIC_RELAXED, __HIP_MEMORY_SCOPE_AGENT);
    const u64 y = __hip_atomic_load((GAS u64*)(ws + off1), __ATOMIC_RELAXED, __HIP_MEMORY_SCOPE_AGENT);
    if (__all(x == tag && y == tag)) break;
    __builtin_amdgcn_s_sleep(8);
  }
  asm volatile("" ::: "memory");
}
template <int CTRL> DI float dpp_add(float v) {
  return v + __int_as_float(__builtin_amdgcn_update_dpp(0, __float_as_int(v), CTRL, 0xf, 0xf, true));
}
DI float wave_sum(float v) {
  v = dpp_add<0xB1>(v); v = dpp_add<0x4E>(v); v = dpp_add<0x141>(v); v = dpp_add<0x140>(v);
  const int i = __float_as_int(v);
  return (__int_as_float(__builtin_amdgcn_readlane(i, 0)) + __int_as_float(__builtin_amdgcn_readlane(i, 16))) +
         (__int_as_float(__builtin_amdgcn_readlane(i, 32)) + __int_as_float(__builtin_amdgcn_readlane(i, 48)));
}
DI void ctl_add(unsigned addr) {
  asm volatile("s_waitcnt lgkmcnt(0)\n\ts_mov_b64 exec, 1\n\tds_add_u32 %0, %1\n\ts_mov_b64 exec, -1" ::"v"(addr), "v"(1u) : "memory");
}
DI unsigned ctl_get(unsigned addr) {
  unsigned s, t;
  asm volatile("ds_read_b32 %1, %2\n\ts_waitcnt lgkmcnt(0)\n\tv_readfirstlane_b32 %0, %1\n\ts_nop 1" : "=s"(s), "=&v"(t) : "v"(addr) : "memory");
  return s;
}
DI void ctl_spin(unsigned addr, unsigned target) {
  unsigned t, s, n;
  asm volatile("s_mov_b32 %2, 0x40000\n.Lspin%=:\n\tds_read_b32 %0, %3\n\ts_waitcnt lgkmcnt(0)\n\tv_readfirstlane_b32 %1, %0\n\ts_nop 1\n\t"
               "s_cmp_ge_u32 %1, %4\n\ts_cbranch_scc1 .Lspun%=\n\ts_sub_u32 %2, %2, 1\n\ts_cmp_lg_u32 %2, 0\n\ts_cbranch_scc1 .Lspin%=\n.Lspun%=:"
               : "=&v"(t), "=&s"(s), "=&s"(n) : "v"(addr), "s"(target) : "memory", "scc");
}
DI void ctl_wait(unsigned addr, unsigned target) {
#pragma unroll 1
  for (int it = 0; it < SPIN_CAP; ++it) {
    if (ctl_get(addr) >= target) break;
    __builtin_amdgcn_s_sleep(1);
  }
}
#define STAMP(i)
#define CNOW(v)
#define CSTAMP(i)

#define HWIN 4
#define WREG 6
#define SFIRST 0
#define PROGR 0
#define PRE_FIRST 1
#define CONV_DB 1
#define CPRIO 1
#define APRIO 2
#define PEEK_AT 5
#define SIDE_SLEEP 30
#define CODE_LINES 256
#define PF_W 1
#define PF_CODE 1
#define PF_L2 14
#define S1_EARLY 1
#define BAR_AT 28
#define KBAR() do { if (g1) BAR(); } while (0)
struct Frag { bf8 x[4]; bf8 w[2][4]; };

__global__ __launch_bounds__(512, 2) void h3_geglu_fused(const char* q, const char* kk, const char* vv, const char* shk, const char* ssk,
                                                         const char* sD, const char* wg, const char* wu, const char* wd, char* out, char* ws) {
  extern __shared__ __attribute__((aligned(16))) char smem[];
  u64 kbase;
  asm volatile("s_getpc_b64 %0" : "=s"(kbase));
  const int lane = threadIdx.x & 63, w0 = __builtin_amdgcn_readfirstlane(threadIdx.x >> 6) ^ (SFIRST ? 4 : 0);
  const int b = blockIdx.x, xcd = b & 7, r = b >> 3;
  const bool g1 = r < 28;
  const int l15 = lane & 15, g = lane >> 4;
  const __amdgpu_buffer_rsrc_t wsr = __builtin_amdgcn_make_buffer_rsrc(ws, 0, 16 << 20, 0x00020000);
  const int pnl = r >> 2;
  const int pm = (xcd >> 1) * 4 + (r & 3), pn = (xcd & 1) * 7 + pnl;
  const int rt = xcd * 4 + (r & 3), ct2 = r >> 2;
  auto stage_offsets = [&](unsigned (&xv)[4], unsigned (&wv)[4]) {
#pragma unroll
    for (int p = 0; p < 4; ++p) {
      xv[p] = (p * 8 + (lane >> 3)) * 1024 + (((lane & 7) ^ (((p & 1) << 2) | g)) << 4) - p * 1024;
      wv[p] = (p * 4 + g) * 3584 + ((l15 ^ ((g | ((p >> 1) << 2)) << 1)) << 4) - p * 1024;
    }
  };
  auto quarter = [&](const unsigned (&xv)[4], const unsigned (&wv)[4], int st, int qt) {
    const unsigned l = (st % 3) * SLOT + qt * 4096;
    glds4(xv[0], xv[1], xv[2], xv[3], q + pm * 131072 + qt * 32768 + st * 128, l);
    glds4(wv[0], wv[1], wv[2], wv[3], wg + pn * 256 + (st * 64 + qt * 16) * 3584, l + 16384);
    glds4(wv[0], wv[1], wv[2], wv[3], wu + pn * 256 + (st * 64 + qt * 16) * 3584, l + 32768);
  };
  if (w0 < 4) {
    const unsigned kvo = (lane >> 3) * 1024 + (((lane & 7) ^ ((g & 1) << 2)) << 4);
    const char* kb = kk + (r * 64 + w0 * 16) * 1024 + xcd * 128;
    const char* vb = vv + (r * 64 + w0 * 16) * 1024 + xcd * 128;
    asm volatile("s_mov_b32 m0, %6\n\ts_nop 0\n\tglobal_load_lds_dwordx4 %0, %3\n\tglobal_load_lds_dwordx4 %0, %4 offset:1024\n\t"
                 "global_load_lds_dwordx4 %1, %4 offset:2048\n\tglobal_load_lds_dwordx4 %0, %5 offset:3072\n\ts_add_u32 m0, m0, 4096\n\ts_nop 0\n\t"
                 "global_load_lds_dwordx4 %1, %8\n\tglobal_load_lds_dwordx4 %2, %7 offset:1024"
                 ::"v"(kvo), "v"(kvo + 7168), "v"((lane & 31) * 16), "s"(r == 0 && w0 == 0 ? kb : kb - 8192), "s"(kb - 1024), "s"(vb - 3072),
                   "s"(2 * SLOT + w0 * 6144), "s"(shk + xcd * 512 - 1024), "s"(vb + 1024)
                 : "memory", "scc");
  } else if (w0 < 6) {
    if (g1) {
      unsigned xv[4], wv[4];
      stage_offsets(xv, wv);
      quarter(xv, wv, 0, 2 * (w0 & 1)); quarter(xv, wv, 0, 2 * (w0 & 1) + 1); quarter(xv, wv, 1, 2 * (w0 & 1));
    }
  } else if (PF_CODE && w0 == 7 && lane < 8 && r * 8 + lane < CODE_LINES) {
    asm volatile("s_mov_b32 m0, %2\n\ts_nop 0\n\tglobal_load_lds_dword %0, %1" ::"v"((r * 8 + lane) * 128), "s"(kbase), "s"(L_RS) : "memory");
  }
  int w_ = w0;
  asm volatile("" : "+s"(w_));
  const int w = w_, gw = w & 3, tid = w * 64 + lane, t8 = tid & 255;
  auto wslot = [](int i) -> unsigned { const int s = i & 7; return s < 2 ? 2 * SLOT + s * 4096 : s < 5 ? (s - 2) * 4096 : SLOT + (s - 5) * 4096; };
  auto wdstage = [&](unsigned dv0, int kq, int i) {
    glds4(dv0, dv0 + 7168, dv0 + 2 * 7168, dv0 + 3 * 7168, wd + ct2 * 128 + (kq * 14 + i) * 32768, wslot(i) + kq * ((i & 7) < 2 ? 8192 : 12288));
  };
  u64 tag;
  auto tag_get = [&]() {
    const u2 t = *(const volatile LAS u2*)(size_t)L_TAG;
    tag = ((u64)(unsigned)__builtin_amdgcn_readfirstlane(t[1]) << 32) | (unsigned)__builtin_amdgcn_readfirstlane(t[0]);
  };

  u4 xq; u2 st2;
  auto tail_load = [&]() {
    int ln = __builtin_amdgcn_mbcnt_hi(-1, __builtin_amdgcn_mbcnt_lo(-1, 0));
    OPAQUE(ln);
    const int tk = w * 8 + (ln >> 3), c8 = ln & 7;
    xq = *(const GAS u4*)(q + (rt * 64 + tk) * 1024 + ct2 * 128 + c8 * 16);
    st2 = __builtin_amdgcn_raw_buffer_load_b64(wsr, WS_ROW + (rt * 64 + tk) * 8, 0, 16);
  };

  if (w < 4) {
    {
      const unsigned lk = 2 * SLOT + gw * 6144;
      const bool top = r == 0 && gw == 0;
      const int qq = l15 >> 2, pp = lane & 3;
      unsigned ra = lk + qq * 128 + (((2 * g + (pp >> 1)) ^ ((qq >> 1) << 2)) << 4) + (pp & 1) * 8;
      OPAQUE(ra);
      WAITV(0);
      STAMP(18);
      const u2 sw = *(const LAS u2*)(size_t)(lk + 5120 + lane * 8);
      u2 kr[5], vr[4];
#pragma unroll
      for (int i = 0; i < 5; ++i) kr[i] = __builtin_bit_cast(u2, __builtin_amdgcn_ds_read_tr16_b64_v4i16((LAS s4*)(size_t)(ra + 512 + i * 512)));
#pragma unroll
      for (int i = 0; i < 4; ++i) vr[i] = __builtin_bit_cast(u2, __builtin_amdgcn_ds_read_tr16_b64_v4i16((LAS s4*)(size_t)(ra + 3072 + i * 512)));
      if (top) kr[0] = (u2){0, 0};
      const float w0 = bflo(sw[0]), w1 = bfhi(sw[0]), w2 = bflo(sw[1]), w3 = bfhi(sw[1]);
      float kf[20], u[16];
#pragma unroll
      for (int i = 0; i < 5; ++i) { kf[4 * i] = bflo(kr[i][0]); kf[4 * i + 1] = bfhi(kr[i][0]); kf[4 * i + 2] = bflo(kr[i][1]); kf[4 * i + 3] = bfhi(kr[i][1]); }
      SB();
#pragma unroll
      for (int i = 0; i < 16; ++i) u[i] = fmul(kf[i + 1], w3);
      SB();
#pragma unroll
      for (int i = 0; i < 16; ++i) u[i] = ffma(kf[i + 2], w2, u[i]);
      SB();
#pragma unroll
      for (int i = 0; i < 16; ++i) u[i] = ffma(kf[i + 3], w1, u[i]);
      SB();
#pragma unroll
      for (int i = 0; i < 16; ++i) u[i] = ffma(kf[i + 4], w0, u[i]);
      SB();
#pragma unroll
      for (int i = 0; i < 16; ++i) { const unsigned vw = vr[i >> 2][(i >> 1) & 1]; u[i] = fmul(u[i], (i & 1) ? bfhi(vw) : bflo(vw)); }
      SB();
      const unsigned ad = L_MISC + lane * 128;
      sts128(ad + (((7 - 2 * gw) ^ (lane & 7)) << 4), (u4){pk2(u[7], u[6]), pk2(u[5], u[4]), pk2(u[3], u[2]), pk2(u[1], u[0])});
      sts128(ad + (((6 - 2 * gw) ^ (lane & 7)) << 4), (u4){pk2(u[15], u[14]), pk2(u[13], u[12]), pk2(u[11], u[10]), pk2(u[9], u[8])});
      STAMP(1);
      BAR();
    }
    if (g1) {
      const int qq = l15 >> 2, pp = lane & 3;
      const int wm = w >> 1, wn = w & 1;
      f4 acc[2][4][4];
#pragma unroll
      for (int s = 0; s < 2; ++s)
#pragma unroll
        for (int e = 0; e < 4; ++e)
#pragma unroll
          for (int i = 0; i < 4; ++i) acc[s][e][i] = (f4){0.f, 0.f, 0.f, 0.f};
      unsigned xa[2], wa[4];
      xa[0] = (wm * 64 + l15) * 128 + ((g ^ (l15 >> 1)) << 4); xa[1] = xa[0] ^ 64;
#pragma unroll
      for (int e = 0; e < 4; ++e) wa[e] = (8 * g + qq) * 256 + (((4 * wn + e) ^ (qq | ((g & 1) << 2))) << 5) + pp * 8;
      Frag f0, f1;
      s4 lo_;
      auto ldpart = [&](Frag& f, int ks, int j) {
        const unsigned o = 16384 + ks * 8192;
        if (j == 0) f.x[0] = lds128(xa[ks]);
        if (j >= 1 && j <= 16) {
          const int s = (j - 1) >> 3, e = ((j - 1) >> 1) & 3;
          if (j & 1) lo_ = __builtin_amdgcn_ds_read_tr16_b64_v4i16((LAS s4*)(size_t)(wa[e] + o + s * 16384));
          else {
            const s4 hi = __builtin_amdgcn_ds_read_tr16_b64_v4i16((LAS s4*)(size_t)(wa[e] + o + s * 16384 + 1024));
            f.w[s][e] = __builtin_bit_cast(bf8, __builtin_shufflevector(lo_, hi, 0, 1, 2, 3, 4, 5, 6, 7));
          }
        }
        if (j >= 17 && j < 20) f.x[j - 16] = lds128(xa[ks] + (j - 16) * 2048);
      };
      auto mm1 = [&](const Frag& f, int j) { acc[(j >> 2) & 1][j & 3][j >> 3] = mfma(f.w[(j >> 2) & 1][j & 3], f.x[j >> 3], acc[(j >> 2) & 1][j & 3][j >> 3]); };
      auto second = [&](int st) {
        const int d = (st == 2 || st == 5) ? -2 * (int)SLOT : (int)SLOT;
#pragma unroll
        for (int j = 0; j < 32; ++j) {
          if (j == BAR_AT) { BAR(); SB(); }
          mm1(f0, j);
          if (j < 20) ldpart(f1, 1, j);
          if (j == 20) { xa[0] += d; OPAQUE(xa[0]); xa[1] += d; OPAQUE(xa[1]); }
          if (j == 21) { wa[0] += d; OPAQUE(wa[0]); wa[1] += d; OPAQUE(wa[1]); }
          if (j == 22) { wa[2] += d; OPAQUE(wa[2]); wa[3] += d; OPAQUE(wa[3]); }
          SB();
        }
        if (BAR_AT == 32) { BAR(); SB(); }
      };
      OPAQUE(xa[0]); OPAQUE(xa[1]); OPAQUE(wa[0]); OPAQUE(wa[1]); OPAQUE(wa[2]); OPAQUE(wa[3]);
      STAMP(2); CSTAMP(20);
#pragma unroll
      for (int j = 0; j < 20; ++j) ldpart(f0, 0, j);
      SB();
      second(0);
#pragma unroll 1
      for (int st = 1; st < 8; ++st) {
#pragma unroll
        for (int j = 0; j < 32; ++j) { mm1(f1, j); if (j < 20) ldpart(f0, 0, j); SB(); }
        second(st);
      }
      STAMP(16); CSTAMP(21);
      constexpr float C0 = -2.0f * 0.7978845608028654f * 1.4426950408889634f, C1 = C0 * 0.044715f;
      f4 cs[2][4];
      f2 rs[4];
      int ln1 = __builtin_amdgcn_mbcnt_hi(-1, __builtin_amdgcn_mbcnt_lo(-1, 0));
      OPAQUE(ln1);
      const int lane = ln1, l15 = lane & 15, g = lane >> 4;
#pragma unroll
      for (int j = 0; j < 32; ++j) {
        mm1(f1, j);
        if (j < 8) cs[j >> 2][j & 3] = lds128f(L_CS + ((j >> 2) * 128 + wn * 64 + (j & 3) * 16 + 4 * g) * 4);
        if (j >= 8 && j < 12) rs[j - 8] = *(const LAS f2*)(size_t)(L_RS + (wm * 64 + (j - 8) * 16 + l15) * 8);
        SB();
      }
      STAMP(3);
      const unsigned hso = WS_H + ((pm * 8 + wm * 4) * 56 + pn * 4 + wn * 2) * 1024 + lane * 16;
#pragma unroll
      for (int i = 0; i < 4; ++i) {
        const float m = -rs[i][0], rr = rs[i][1];
        const float k0 = C0 * rr, k1 = C1 * rr * rr * rr, i2 = __builtin_amdgcn_rcpf(rr * rr);
        const f2 m2 = {m, m}, k02 = {k0, k0}, k12 = {k1, k1}, i22 = {i2, i2};
        f2 a[8], c[8], z[8];
        SB();
#pragma unroll
        for (int j = 0; j < 8; ++j) {
          const int e = j >> 1, h = (j & 1) * 2;
          a[j] = __builtin_elementwise_fma(m2, (f2){cs[0][e][h], cs[0][e][h + 1]}, (f2){acc[0][e][i][h], acc[0][e][i][h + 1]});
        }
        SB();
#pragma unroll
        for (int j = 0; j < 8; ++j) z[j] = a[j] * a[j];
        SB();
#pragma unroll
        for (int j = 0; j < 8; ++j) z[j] = __builtin_elementwise_fma(z[j], k12, k02);
        SB();
#pragma unroll
        for (int j = 0; j < 8; ++j) z[j] = z[j] * a[j];
        SB();
#pragma unroll
        for (int j = 0; j < 8; ++j) {
          const int e = j >> 1, h = (j & 1) * 2;
          z[j] = (f2){__builtin_amdgcn_exp2f(z[j][0]), __builtin_amdgcn_exp2f(z[j][1])};
          c[j] = __builtin_elementwise_fma(m2, (f2){cs[1][e][h], cs[1][e][h + 1]}, (f2){acc[1][e][i][h], acc[1][e][i][h + 1]});
          SB();
        }
#pragma unroll
        for (int j = 0; j < 8; ++j) z[j] = __builtin_elementwise_fma(z[j], i22, i22);
        SB();
#pragma unroll
        for (int j = 0; j < 8; ++j) {
          z[j] = (f2){__builtin_amdgcn_rcpf(z[j][0]), __builtin_amdgcn_rcpf(z[j][1])};
          c[j] = c[j] * a[j];
          SB();
        }
#pragma unroll
        for (int j = 0; j < 8; ++j) c[j] = c[j] * z[j];
        SB();
        u4 o0, o1;
#pragma unroll
        for (int j = 0; j < 4; ++j) { o0[j] = pk2(c[j][0], c[j][1]); o1[j] = pk2(c[4 + j][0], c[4 + j][1]); }
        __builtin_amdgcn_raw_buffer_store_b128(o0, wsr, hso + i * 56 * 1024, 0, 16);
        __builtin_amdgcn_raw_buffer_store_b128(o1, wsr, hso + i * 56 * 1024 + 1024, 0, 16);
      }
      SB();
      ctl_add(C_EPI);
      tag_get();
      STAMP(4);
    } else {
      const unsigned o = ((xcd * 4 + r - 28) * 448 + gw * 112 + lane) * 128;
      if (PF_W) asm volatile("s_mov_b32 m0, %3\n\ts_nop 0\n\tglobal_load_lds_dword %0, %2\n\tglobal_load_lds_dword %1, %2"
                             ::"v"(o), "v"(o + (lane < 48 ? 8192 : 0)), "s"(wd), "s"(131072 + gw * 256) : "memory");
      if (PF_L2) {
#pragma unroll 1
        for (int j = (r - 28) * 4 + gw; j < PF_L2 * 16; j += 16) {
          const unsigned o2 = ((((j >> 2) & 3) * 14 + (j >> 4)) * 32 + (j & 3) * 8 + (lane >> 3)) * 1024 + (lane & 7) * 128;
          asm volatile("s_waitcnt vmcnt(0)\n\ts_mov_b32 m0, %2\n\ts_nop 0\n\tglobal_load_lds_dword %0, %1" ::"v"(o2), "s"(wd), "s"(131072 + gw * 256) : "memory");
        }
      }
      WAITV(0);
    }
    int ln2 = __builtin_amdgcn_mbcnt_hi(-1, __builtin_amdgcn_mbcnt_lo(-1, 0));
    OPAQUE(ln2);
    {
      const int lane = ln2, l15 = lane & 15, g = lane >> 4, qq = l15 >> 2, pp = lane & 3;
      const unsigned dv0 = (lane >> 3) * 1024 + (((lane & 7) ^ (g << 1)) * 16);
      f4 acc[4][4];
#pragma unroll
      for (int t = 0; t < 4; ++t)
#pragma unroll
        for (int i = 0; i < 4; ++i) acc[t][i] = (f4){0.f, 0.f, 0.f, 0.f};
      const unsigned da = (4 * g + qq) * 128 + (((2 * g + (qq >> 1)) & 3) << 5) + pp * 8;
      unsigned dlo[4];
#pragma unroll
      for (int t = 0; t < 4; ++t) { dlo[t] = (da ^ (t * 32)) + gw * 12288; OPAQUE(dlo[t]); }
      const unsigned dh = 2 * SLOT - gw * 4096;
      constexpr int WR = 6, HW = HWIN;
      bf8 wf[WR][4];
      auto waddr = [&](int i, int t) -> unsigned {
        if ((i & 7) >= 2) return dlo[t] + wslot(i);
        unsigned d_ = dh;
        asm volatile("" : "+s"(d_));
        unsigned a_ = dlo[t] + d_;
        OPAQUE(a_);
        return a_ + (i & 1) * 4096;
      };
      __builtin_amdgcn_s_setprio(APRIO);
      auto publish = [&]() {
        if (g1) {
          WAITV(0);
          if (lane == 0 && __hip_atomic_fetch_add((LAS unsigned*)(size_t)C_HPUB, 1u, __ATOMIC_RELAXED, __HIP_MEMORY_SCOPE_WORKGROUP) == 3)
            flag_set(ws, WS_FH + (pm * 16 + pn) * 128, tag);
        }
        STAMP(5);
      };
      if (!PRE_FIRST) publish();
      ctl_wait(C_WD + (gw >> 1) * 4, 2);
      if (!g1) tag_get();
#pragma unroll
      for (int t = 0; t < WR * 4; ++t) { const unsigned a_ = waddr(t >> 2, t & 3); wf[t >> 2][t & 3] = ldtr(a_, a_ + 2048); }
      SB();
      if (PRE_FIRST) publish();
      WAITL(0);
      STAMP(17);
#pragma unroll
      for (int i = 8; i < 14; ++i) wdstage(dv0, gw, i);
      __builtin_amdgcn_s_setprio(0);
      STAMP(9);
      const unsigned fh = WS_FH + ((rt >> 1) * 16 + ((gw * 7) >> 1) + (lane & 3)) * 128;
      unsigned have = 0;
      auto need = [&](int i) {
        const unsigned m = PROGR ? 1u << (((gw & 1) * 2 + i) >> 2) : 15u;
        u64 x_;
        unsigned t_, n_;
        asm volatile("s_and_b32 %2, %0, %4\n\ts_cmp_eq_u32 %2, %4\n\ts_cbranch_scc1 .Ldone%=\n\ts_mov_b32 %3, 0x40000\n"
                     ".Lpoll%=:\n\tglobal_load_dwordx2 %1, %5, %6 sc1\n\ts_waitcnt vmcnt(0)\n\tv_cmp_eq_u64 vcc, %7, %1\n\ts_nop 4\n\t"
                     "s_and_b32 %0, vcc_lo, 15\n\ts_and_b32 %2, %0, %4\n\ts_cmp_eq_u32 %2, %4\n\ts_cbranch_scc1 .Ldone%=\n\ts_sleep 2\n\t"
                     "s_sub_u32 %3, %3, 1\n\ts_cmp_lg_u32 %3, 0\n\ts_cbranch_scc1 .Lpoll%=\n.Ldone%=:"
                     : "+s"(have), "=&v"(x_), "=&s"(t_), "=&s"(n_) : "s"(m), "v"(fh), "s"(ws), "s"(tag) : "memory", "vcc", "scc");
        SB();
      };
      need(0);
      STAMP(6);
      u4 hf[HW][4];
      unsigned ho[4];
#pragma unroll
      for (int t = 0; t < 4; ++t) ho[t] = WS_H + ((rt * 4 + t) * 56 + gw * 14) * 1024 + lane * 16;
      auto ldh1 = [&](int i, int t) { hf[i % HW][t] = __builtin_amdgcn_raw_buffer_load_b128(wsr, ho[t] + (i & 3) * 1024, (i >> 2) * 4096, 16); };
#pragma unroll
      for (int i = 0; i < HW * 4; ++i) { if (i == 8) need(2); ldh1(i >> 2, i & 3); SB(); }
      s4 lo_;
      unsigned wa_;
#pragma unroll
      for (int i = 0; i < 14; ++i) {
        const int nw = i + WR - 1;
        if (i == 11) { tail_load(); SB(); }
        if (i + HW < 14 && ((i + HW) & 1) == 0) need(i + HW);
#pragma unroll
        for (int j = 0; j < 16; ++j) {
          const int ti = j >> 2, t = j & 3;
          acc[t][ti] = mfma(wf[i % WR][t], __builtin_bit_cast(bf8, hf[i % HW][ti]), acc[t][ti]);
          if (i >= 1 && nw < 14 && t != 3 && (j - ti) < 8) {
            const int e = j - ti;
            if (!(e & 1)) wa_ = waddr(nw, e >> 1);
            const s4 v_ = __builtin_amdgcn_ds_read_tr16_b64_v4i16((LAS s4*)(size_t)(wa_ + (e & 1) * 2048));
            if (e & 1) wf[nw % WR][e >> 1] = __builtin_bit_cast(bf8, __builtin_shufflevector(lo_, v_, 0, 1, 2, 3, 4, 5, 6, 7)); else lo_ = v_;
          }
          if (t == 3 && i + HW < 14) ldh1(i + HW, ti);
          SB();
        }
        if (i == 0) STAMP(19);
      }
      STAMP(7);
#pragma unroll
      for (int ti = 0; ti < 4; ++ti)
#pragma unroll
        for (int t = 0; t < 4; ++t)
          sts128f((ti >> 1) * SLOT + gw * 12288 + ((ti & 1) * 16 + l15) * 256 + (((4 * t + g) ^ (l15 & 7)) << 4), acc[t][ti]);
    }
    STAMP(8);
  } else {
    const int t7 = tid & 127, bw = w & 1;
    unsigned dcr;
    if (w < 6) {
      unsigned xv[4], wv[4];
      stage_offsets(xv, wv);
      auto stage = [&](int st) { quarter(xv, wv, st, 2 * bw); quarter(xv, wv, st, 2 * bw + 1); };
      const unsigned dv0 = (lane >> 3) * 1024 + (((lane & 7) ^ (g << 1)) * 16);
      WAITV(12);
      STAMP(1);
      BAR();
      if (g1) { quarter(xv, wv, 1, 2 * bw + 1); stage(2); WAITV(24); }
      KBAR();
#pragma unroll 1
      for (int st = 3; st < 8; ++st) {
        if (g1) { stage(st); WAITV(24); }
        KBAR();
      }
      STAMP(13);
      if (!g1) ctl_wait(C_TAG, 2);
      asm volatile("global_load_dword %0, %1, %2" : "=&v"(dcr) : "v"(0u), "s"(sD + b * 4) : "memory");
#pragma unroll
      for (int i = 0; i < 4; ++i) wdstage(dv0, bw * 2 + (i & 1), i >> 1);
      WAITV(17);
      KBAR();
      STAMP(3);
#pragma unroll
      for (int i = 0; i < 6; ++i) wdstage(dv0, bw * 2 + (i & 1), 2 + (i >> 1));
      KBAR();
      STAMP(19);
      tag_get();
      asm volatile("s_waitcnt vmcnt(32)" : "+v"(dcr)::"memory");
#pragma unroll
      for (int i = 0; i < 6; ++i) wdstage(dv0, bw * 2 + (i & 1), 5 + (i >> 1));
      WAITV(24);
      ctl_add(C_WD + bw * 4);
      STAMP(2);
      WAITV(0);
      ctl_add(C_WD + bw * 4);
      STAMP(4);
    } else {
      if (t7 < 48) *(LAS unsigned*)(size_t)(L_CTL + t7 * 4) = 0;
      const int ptt = b >> 3, pct = b & 7;
      const int rgrp = (xcd >> 1) * 64 + (xcd & 1) * 32 + r;
      const int cst = 2 * ((r + pnl) & 3) + ((xcd >> 1) & 1);
      const unsigned hoff = (2 * b + bw) * 4096 + lane * 16;
      u4 xr[4];
      auto rows_load = [&]() {
        asm volatile("global_load_dwordx4 %0, %4, %5\n\tglobal_load_dwordx4 %1, %4, %5 offset:1024\n\tglobal_load_dwordx4 %2, %4, %5 offset:2048\n\t"
                     "global_load_dwordx4 %3, %4, %5 offset:3072"
                     : "=&v"(xr[0]), "=&v"(xr[1]), "=&v"(xr[2]), "=&v"(xr[3]) : "v"(lane * 16), "s"(q + (rgrp * 8 + (w & 1) * 4) * 1024) : "memory");
      };
      auto rows_stat = [&]() {
        asm volatile("" : "+v"(xr[0]), "+v"(xr[1]), "+v"(xr[2]), "+v"(xr[3])::"memory");
        float e[4][8], mu[4], va[4];
#pragma unroll
        for (int i = 0; i < 4; ++i) {
#pragma unroll
          for (int j = 0; j < 4; ++j) { e[i][2 * j] = bflo(xr[i][j]); e[i][2 * j + 1] = bfhi(xr[i][j]); }
          mu[i] = fadd(fadd(fadd(e[i][0], e[i][1]), fadd(e[i][2], e[i][3])), fadd(fadd(e[i][4], e[i][5]), fadd(e[i][6], e[i][7])));
        }
        auto fold4 = [&](const float (&v)[4]) {
          const auto s0 = __builtin_amdgcn_permlane16_swap(__float_as_uint(v[0]), __float_as_uint(v[1]), false, false);
          const auto s1 = __builtin_amdgcn_permlane16_swap(__float_as_uint(v[2]), __float_as_uint(v[3]), false, false);
          const float p0 = fadd(__uint_as_float(s0[0]), __uint_as_float(s0[1])), p1 = fadd(__uint_as_float(s1[0]), __uint_as_float(s1[1]));
          const auto s2 = __builtin_amdgcn_permlane32_swap(__float_as_uint(p0), __float_as_uint(p1), false, false);
          float t = fadd(__uint_as_float(s2[0]), __uint_as_float(s2[1]));
          t = dpp_add<0xB1>(t); t = dpp_add<0x4E>(t); t = dpp_add<0x141>(t); t = dpp_add<0x140>(t);
          return t * (1.0f / 512.0f);
        };
        const float mg = fold4(mu);
#pragma unroll
        for (int i = 0; i < 4; ++i) {
          const float m = __int_as_float(__builtin_amdgcn_readlane(__float_as_int(mg), 16 * i));
          float s = 0.f;
#pragma unroll
          for (int j = 0; j < 8; ++j) { const float d = e[i][j] - m; s = ffma(d, d, s); }
          va[i] = s;
        }
        const float vg = fold4(va);
        const u2 o = {__float_as_uint(rbf(mg)), __float_as_uint(__builtin_amdgcn_rcpf(rbf(__builtin_amdgcn_sqrtf(rbf(rbf(vg) + 1e-5f)))))};
        if (l15 == 0) __builtin_amdgcn_raw_buffer_store_b64(o, wsr, WS_ROW + (rgrp * 8 + (w & 1) * 4 + g) * 8, 0, 16);
        asm volatile("" ::: "memory");
      };

      u4 wsh[8], hc[4], uc[4], c1[4]; unsigned hp[4];
      {
        const char* wb = ((xcd >> 2) ? wu : wg) + (g1 ? pn * 256 + (cst * 64 + bw * 32) * 3584 : 0);
        const unsigned o = g * 3584 + l15 * 16;
        if (SIDE_SLEEP) __builtin_amdgcn_s_sleep(SIDE_SLEEP);
        asm volatile("global_load_dwordx4 %0, %8, %12\n\tglobal_load_dwordx4 %1, %9, %12\n\tglobal_load_dwordx4 %2, %10, %12\n\tglobal_load_dwordx4 %3, %11, %12\n\t"
                     "global_load_dwordx4 %4, %8, %13\n\tglobal_load_dwordx4 %5, %9, %13\n\tglobal_load_dwordx4 %6, %10, %13\n\tglobal_load_dwordx4 %7, %11, %13"
                     : "=&v"(wsh[0]), "=&v"(wsh[1]), "=&v"(wsh[2]), "=&v"(wsh[3]), "=&v"(wsh[4]), "=&v"(wsh[5]), "=&v"(wsh[6]), "=&v"(wsh[7])
                     : "v"(o), "v"(o + 4 * 3584), "v"(o + 8 * 3584), "v"(o + 12 * 3584), "s"(wb), "s"(wb + 16 * 3584) : "memory");
        rows_load();
      }
      asm volatile("s_load_dwordx2 %0, %1, 0x0\n\ts_waitcnt lgkmcnt(0)" : "=s"(tag) : "s"(ws) : "memory");
      tag += 1;
      *(LAS u64*)(size_t)L_TAG = tag;
      BAR();
#pragma unroll
      for (int z = 0; z < 4; ++z) {
        const int id = t7 + 128 * z, c2 = id >> 3, ch = id & 7;
        const u4 t = lds128u(L_MISC + c2 * 128 + ((ch ^ (c2 & 7)) << 4));
        __builtin_amdgcn_raw_buffer_store_b128(t, wsr, WS_KVT + (pct * 64 + c2) * 4096 + 3968 - ptt * 128 + ch * 16, 0, 16);
      }
      asm volatile("s_waitcnt vmcnt(4)" : "+v"(wsh[0]), "+v"(wsh[1]), "+v"(wsh[2]), "+v"(wsh[3]), "+v"(wsh[4]), "+v"(wsh[5]), "+v"(wsh[6]), "+v"(wsh[7])::"memory");
      STAMP(17);
      if (g1) {
        float cacc[8];
#pragma unroll
        for (int j = 0; j < 4; ++j) {
          cacc[2 * j] = fadd(fadd(fadd(bflo(wsh[0][j]), bflo(wsh[1][j])), fadd(bflo(wsh[2][j]), bflo(wsh[3][j]))),
                             fadd(fadd(bflo(wsh[4][j]), bflo(wsh[5][j])), fadd(bflo(wsh[6][j]), bflo(wsh[7][j]))));
          cacc[2 * j + 1] = fadd(fadd(fadd(bfhi(wsh[0][j]), bfhi(wsh[1][j])), fadd(bfhi(wsh[2][j]), bfhi(wsh[3][j]))),
                                 fadd(fadd(bfhi(wsh[4][j]), bfhi(wsh[5][j])), fadd(bfhi(wsh[6][j]), bfhi(wsh[7][j]))));
        }
        float c4[4], c2[2];
#pragma unroll
        for (int j = 0; j < 4; ++j) {
          const auto s2 = __builtin_amdgcn_permlane16_swap(__float_as_uint(cacc[j]), __float_as_uint(cacc[4 + j]), false, false);
          c4[j] = fadd(__uint_as_float(s2[0]), __uint_as_float(s2[1]));
        }
#pragma unroll
        for (int j = 0; j < 2; ++j) {
          const auto s2 = __builtin_amdgcn_permlane32_swap(__float_as_uint(c4[j]), __float_as_uint(c4[2 + j]), false, false);
          c2[j] = fadd(__uint_as_float(s2[0]), __uint_as_float(s2[1]));
        }
        __builtin_amdgcn_raw_buffer_store_b64((u2){__float_as_uint(c2[0]), __float_as_uint(c2[1])}, wsr,
                                              WS_COL + ((pn * 16 + cst * 2 + bw) * 256 + (xcd >> 2) * 128 + l15 * 8 + (g & 1) * 4 + (g >> 1) * 2) * 4, 0, 16);
      }
      STAMP(10);
      KBAR();
      rows_stat();
      STAMP(18);
      asm volatile("global_load_dwordx4 %0, %9, %11\n\tglobal_load_dwordx4 %1, %9, %11 offset:1024\n\tglobal_load_dwordx4 %2, %9, %11 offset:2048\n\t"
                   "global_load_dwordx4 %3, %9, %11 offset:3072\n\tglobal_load_dword %4, %10, %11\n\tglobal_load_dword %5, %9, %11 offset:1020\n\t"
                   "global_load_dword %6, %9, %11 offset:2044\n\tglobal_load_dword %7, %9, %11 offset:3068\n\tglobal_load_dword %8, %12, %13"
                   : "=&v"(hc[0]), "=&v"(hc[1]), "=&v"(hc[2]), "=&v"(hc[3]), "=&v"(hp[0]), "=&v"(hp[1]), "=&v"(hp[2]), "=&v"(hp[3]), "=&v"(dcr)
                   : "v"(hoff), "v"(lane ? hoff - 4 : hoff), "s"(ssk), "v"(0u), "s"(sD + b * 4) : "memory");
      KBAR();
      WAITV(9);
      KBAR();
      if (!g1) { ctl_add(C_WSYNC); ctl_spin(C_WSYNC, 2); }
      if (tid == 384 || tid == 385) flag_set(ws, tid == 384 ? WS_FS + rgrp * 128 : WS_FK + (pct * 32 + ptt) * 128, tag);
      STAMP(20);
      unsigned fo = lane < 32 || !g1 ? WS_FK + ((b >> 5) * 32 + (lane & 31)) * 128
                                     : WS_FS + (lane < 48 ? pm * 16 + l15 : (l15 >> 2) * 64 + (xcd & 1) * 32 + (l15 & 3) + 4 * pnl) * 128;
      unsigned fo2 = WS_FS + (rt * 8 + (lane & 7)) * 128;
      OPAQUE(fo); OPAQUE(fo2);
      KBAR();
      asm volatile("s_waitcnt vmcnt(0)" : "+v"(hc[0]), "+v"(hc[1]), "+v"(hc[2]), "+v"(hc[3]), "+v"(hp[0]), "+v"(hp[1]), "+v"(hp[2]), "+v"(hp[3]), "+v"(dcr)::"memory");
      if (lane == 0) hp[0] = 0;
#pragma unroll
      for (int z = 0; z < 4; ++z) {
        c1[z] = (u4){__builtin_amdgcn_alignbit(hc[z][0], hp[z], 16), __builtin_amdgcn_alignbit(hc[z][1], hc[z][0], 16),
                     __builtin_amdgcn_alignbit(hc[z][2], hc[z][1], 16), __builtin_amdgcn_alignbit(hc[z][3], hc[z][2], 16)};
        OPAQUE(c1[z]);
      }
      u64 p0, p1;
      if (!g1) __builtin_amdgcn_s_sleep(70);
      if (PEEK_AT == 5) KBAR();
      asm volatile("global_load_dwordx2 %0, %2, %4 sc1\n\tglobal_load_dwordx2 %1, %3, %4 sc1" : "=&v"(p0), "=&v"(p1) : "v"(fo), "v"(fo2), "s"(ws) : "memory");
      KBAR();
      STAMP(23);
      asm volatile("s_waitcnt vmcnt(0)" : "+v"(p0), "+v"(p1)::"memory");
      if (!__all(p0 == tag && p1 == tag)) poll2(ws, fo, fo2, tag);
      if (!g1) ctl_add(C_TAG);
      STAMP(21);
      u2 rsv, csv[16];
      {
        const unsigned co = WS_COL + (g1 ? pn : 0) * 16384 + t7 * 8;
#define LD4(o, vreg) asm volatile("global_load_dwordx2 %0, %4, %5 sc1\n\tglobal_load_dwordx2 %1, %4, %5 offset:1024 sc1\n\t" \
                                  "global_load_dwordx2 %2, %4, %5 offset:2048 sc1\n\tglobal_load_dwordx2 %3, %4, %5 offset:3072 sc1" \
                                  : "=&v"(csv[o]), "=&v"(csv[o + 1]), "=&v"(csv[o + 2]), "=&v"(csv[o + 3]) : "v"(vreg), "s"(ws) : "memory")
        LD4(0, co); LD4(4, co + 4096); LD4(8, co + 8192); LD4(12, co + 12288);
        asm volatile("global_load_dwordx2 %0, %1, %2 sc1" : "=&v"(rsv) : "v"(WS_ROW + (g1 ? pm : 0) * 1024 + t7 * 8), "s"(ws) : "memory");
      }
      asm volatile("global_load_dwordx4 %0, %4, %5 sc1\n\tglobal_load_dwordx4 %1, %4, %5 offset:1024 sc1\n\tglobal_load_dwordx4 %2, %4, %5 offset:2048 sc1\n\t"
                   "global_load_dwordx4 %3, %4, %5 offset:3072 sc1" : "=&v"(uc[0]), "=&v"(uc[1]), "=&v"(uc[2]), "=&v"(uc[3]) : "v"(WS_KVT + hoff), "s"(ws) : "memory");
      if (PEEK_AT == 4) KBAR();
      const unsigned base = L_CV + bw * CV_CH;
#pragma unroll
      for (int z = 0; z < 4; ++z) {
        sts128(base + 32 + lane * 16 + z * 1024, hc[z]);
        sts128(base + 32 + lane * 16 + z * 1024 + CV_COPY, c1[z]);
      }
      if (lane < 4) sts128(base + (lane >> 1) * CV_COPY + (lane & 1) * 16, (u4){0, 0, 0, 0});
      if (lane >= 32) sts128(base + CV_UR + 4096 + (lane - 32) * 16, (u4){0, 0, 0, 0});
      STAMP(14);
      KBAR();
      asm volatile("s_waitcnt vmcnt(4)" : "+v"(rsv), "+v"(csv[0]), "+v"(csv[1]), "+v"(csv[2]), "+v"(csv[3]), "+v"(csv[4]), "+v"(csv[5]), "+v"(csv[6]), "+v"(csv[7]),
                   "+v"(csv[8]), "+v"(csv[9]), "+v"(csv[10]), "+v"(csv[11]), "+v"(csv[12]), "+v"(csv[13]), "+v"(csv[14]), "+v"(csv[15])::"memory");
      {
        *(LAS u2*)(size_t)(L_RS + t7 * 8) = rsv;
        float s0[4], s1[4];
#pragma unroll
        for (int i = 0; i < 4; ++i) {
          s0[i] = fadd(fadd(__uint_as_float(csv[i][0]), __uint_as_float(csv[4 + i][0])), fadd(__uint_as_float(csv[8 + i][0]), __uint_as_float(csv[12 + i][0])));
          s1[i] = fadd(fadd(__uint_as_float(csv[i][1]), __uint_as_float(csv[4 + i][1])), fadd(__uint_as_float(csv[8 + i][1]), __uint_as_float(csv[12 + i][1])));
        }
        *(LAS u2*)(size_t)(L_CS + t7 * 8) = (u2){__float_as_uint(fadd(fadd(s0[0], s0[1]), fadd(s0[2], s0[3]))), __float_as_uint(fadd(fadd(s1[0], s1[1]), fadd(s1[2], s1[3])))};
      }
      STAMP(16);
      KBAR();
      if (b == 255 && bw == 1) {
#pragma unroll 1
        for (int z = 0; z < 4; ++z) poll(ws, WS_FS + (z * 64 + lane) * 128, tag);
        if (lane == 0) flag_set(ws, WS_EPOCH, tag);
      }
      asm volatile("s_waitcnt vmcnt(0)" : "+v"(uc[0]), "+v"(uc[1]), "+v"(uc[2]), "+v"(uc[3])::"memory");
#pragma unroll
      for (int z = 0; z < 4; ++z) sts128(base + CV_UR + lane * 16 + z * 1024, uc[z]);
      ctl_add(C_CVIN);
      STAMP(15);
    }
    ctl_wait(C_CVIN, 2);
    const int ch = gw >> 1, half = gw & 1;
    const unsigned base = L_CV + ch * CV_CH;
    const unsigned o = 8 * g + l15 + 1, kap = o & 1;
    unsigned aa = base + kap * CV_COPY + 2 * (o + kap) + half * 256;
    unsigned ba = base + CV_UR + 2 * (2032 - 16 * l15 + 8 * g) - 3584 + half * 256;
    unsigned ab = aa + 8;
    OPAQUE(aa); OPAQUE(ab); OPAQUE(ba);
    f4 y[8];
#pragma unroll
    for (int n = 0; n < 8; ++n) y[n] = (f4){0.f, 0.f, 0.f, 0.f};
    bf8 A[2][8], B[2][8];
    auto ldab = [&](int rho, int i) {
      const u2a lo = *(const LAS u2a*)(size_t)(aa + rho * 64 + i * 512), hi = *(const LAS u2a*)(size_t)(ab + rho * 64 + i * 512);
      A[rho & 1][i] = __builtin_bit_cast(bf8, (u4){lo[0], lo[1], hi[0], hi[1]});
      B[rho & 1][i] = lds128(ba + rho * 64 + (7 - i) * 512);
    };
#pragma unroll
    for (int i = 0; i < 8; ++i) ldab(0, i);
    SB();
    if (g1) ctl_spin(C_EPI, 4);
    STAMP(5);
    __builtin_amdgcn_s_setprio(CPRIO);
#pragma unroll
    for (int rho = 0; rho < 4; ++rho) {
      int cnt = 0;
#pragma unroll
      for (int lv = 0; lv < 8; ++lv)
#pragma unroll
        for (int qi = 0; qi <= lv; ++qi)
#pragma unroll
          for (int p = 0; p <= lv; ++p)
            if ((qi == lv || p == lv) && qi + p < 8) {
              y[qi + p] = mfma(A[rho & 1][qi], B[rho & 1][p], y[qi + p]);
              if (rho < 3 && (cnt & 3) == 0 && (cnt >> 2) < 8) ldab(rho + 1, cnt >> 2);
              ++cnt;
              SB();
            }
    }
    STAMP(6);
    ctl_add(C_PAIR + ch * 4); ctl_spin(C_PAIR + ch * 4, 2);
    const unsigned ex0 = base + half * 4096, ex1 = base + (half ^ 1) * 4096;
#pragma unroll
    for (int n = 0; n < 4; ++n) {
      if (half) { const f4 t = y[n]; y[n] = y[4 + n]; y[4 + n] = t; }
      sts128f(ex0 + n * 1024 + lane * 16, y[4 + n]);
    }
    u2 uu[4];
#pragma unroll
    for (int n = 0; n < 4; ++n) uu[n] = *(const LAS u2*)(size_t)(base + CV_UR + 2 * (2044 - (256 * (4 * half + n) + 16 * l15 + 4 * g)));
    ctl_add(C_PAIR + ch * 4);
    const float dc = ch ? bfhi(dcr) : bflo(dcr);
#pragma unroll
    for (int n = 0; n < 4; ++n) {
      y[n][0] = ffma(bfhi(uu[n][1]), dc, y[n][0]); y[n][1] = ffma(bflo(uu[n][1]), dc, y[n][1]);
      y[n][2] = ffma(bfhi(uu[n][0]), dc, y[n][2]); y[n][3] = ffma(bflo(uu[n][0]), dc, y[n][3]);
    }
    ctl_spin(C_PAIR + ch * 4, 4);
    STAMP(11);
    u2 pk[4];
#pragma unroll
    for (int n = 0; n < 4; ++n) {
      const f4 t = lds128f(ex1 + n * 1024 + lane * 16);
      pk[n] = (u2){pk2(fadd(t[0], y[n][0]), fadd(t[1], y[n][1])), pk2(fadd(t[2], y[n][2]), fadd(t[3], y[n][3]))};
    }
#pragma unroll
    for (int n = 0; n < 4; n += 2) {
      const auto s0 = __builtin_amdgcn_permlane16_swap(pk[n][0], pk[n + 1][0], false, false);
      const auto s1 = __builtin_amdgcn_permlane16_swap(pk[n][1], pk[n + 1][1], false, false);
      const u4 o4 = {s0[0], s1[0], s0[1], s1[1]};
      __builtin_amdgcn_raw_buffer_store_b128(o4, wsr, WS_YT + (2 * b + ch) * 4096 + 2 * (256 * (4 * half + n + (g & 1)) + 16 * l15 + 8 * (g >> 1)), 0, 16);
    }
    STAMP(22);
    WAITV(0);
    ctl_add(C_BSYNC);
    ctl_spin(C_BSYNC, 4);
    if (tid == 256) flag_set(ws, WS_FY + b * 128, tag);
    STAMP(7);
    tail_load();
    if (gw == 3) {
      poll(ws, WS_FY + (ct2 * 32 + (lane & 31)) * 128, tag);
      ctl_add(C_GO);
    } else ctl_wait(C_GO, 1);
    STAMP(8);
    u4 yq[2];
#pragma unroll
    for (int z = 0; z < 2; ++z) {
      const int id = t8 + 256 * z;
      yq[z] = __builtin_amdgcn_raw_buffer_load_b128(wsr, WS_YT + (ct2 * 64 + (id >> 3)) * 4096 + rt * 128 + (id & 7) * 16, 0, 16);
    }
#pragma unroll
    for (int z = 0; z < 2; ++z) {
      const int id = t8 + 256 * z, c = id >> 3;
      sts128(L_YI + c * 128 + (((id & 7) ^ (c >> 3)) << 4), yq[z]);
    }
    __builtin_amdgcn_s_setprio(0);
    STAMP(9);
  }
  f2 xn[4];
#pragma unroll
  for (int j = 0; j < 4; ++j) {
    const float m = __uint_as_float(st2[0]), rr = __uint_as_float(st2[1]);
    const unsigned d = pk2(bflo(xq[j]) - m, bfhi(xq[j]) - m);
    const unsigned n = pk2(bflo(d) * rr, bfhi(d) * rr);
    xn[j] = (f2){bflo(n), bfhi(n)};
    OPAQUE(xn[j]);
  }
  BAR();
  if (w < 4) STAMP(10);
  {
    int ln3 = __builtin_amdgcn_mbcnt_hi(-1, __builtin_amdgcn_mbcnt_lo(-1, 0));
    OPAQUE(ln3);
    const int tk = w * 8 + (ln3 >> 3), c8 = ln3 & 7;
    const unsigned pb = (w >> 2) * SLOT + (tk & 31) * 256;
    f4 o0 = lds128f(pb + (((2 * c8) ^ (tk & 7)) << 4)), o1 = lds128f(pb + (((2 * c8 + 1) ^ (tk & 7)) << 4));
#pragma unroll
    for (int k4 = 1; k4 < 4; ++k4) {
      o0 += lds128f(pb + k4 * 12288 + (((2 * c8) ^ (tk & 7)) << 4));
      o1 += lds128f(pb + k4 * 12288 + (((2 * c8 + 1) ^ (tk & 7)) << 4));
    }
    const unsigned ya = L_YI + c8 * 1024 + ((w ^ c8) << 4) + (tk & 7) * 2;
    unsigned yv[8];
#pragma unroll
    for (int j = 0; j < 8; ++j) yv[j] = *(const LAS unsigned short*)(size_t)(ya + j * 128);
    f2 o[4] = {{o0[0], o0[1]}, {o0[2], o0[3]}, {o1[0], o1[1]}, {o1[2], o1[3]}}, e[4], sg[4];
#pragma unroll
    for (int j = 0; j < 4; ++j) {
      const f2 z = o[j] * -1.4426950408889634f;
      e[j] = (f2){__builtin_amdgcn_exp2f(z[0]), __builtin_amdgcn_exp2f(z[1])} + 1.0f;
      sg[j] = xn[j] * (f2){bflo(yv[2 * j]), bflo(yv[2 * j + 1])} * o[j];
    }
    u4 res;
#pragma unroll
    for (int j = 0; j < 4; ++j) {
      const f2 t = __builtin_elementwise_fma(sg[j], (f2){__builtin_amdgcn_rcpf(e[j][0]), __builtin_amdgcn_rcpf(e[j][1])}, (f2){bflo(xq[j]), bfhi(xq[j])});
      res[j] = pk2(t[0], t[1]);
    }
    *(GAS u4*)(out + (rt * 64 + tk) * 1024 + ct2 * 128 + c8 * 16) = res;
  }
  if (w < 4) STAMP(11);
}
}

extern "C" void kernel_launch(void* const* d_in, const int* in_sizes, int n_in, void* d_out, int out_size, void* d_ws, size_t ws_size,
                              hipStream_t stream) {
  (void)in_sizes; (void)n_in; (void)out_size; (void)ws_size;
  (void)hipFuncSetAttribute((const void*)h3_geglu_fused, hipFuncAttributeMaxDynamicSharedMemorySize, LDS_BYTES);
  h3_geglu_fused<<<256, 512, LDS_BYTES, stream>>>((const char*)d_in[0], (const char*)d_in[1], (const char*)d_in[2], (const char*)d_in[3],
                                                  (const char*)d_in[4], (const char*)d_in[5], (const char*)d_in[7], (const char*)d_in[8],
                                                  (const char*)d_in[9], (char*)d_out, (char*)d_ws);
}
```

```cpp
#include <hip/hip_runtime.h>
#include <stdint.h>

#define LAS __attribute__((address_space(3)))
#define GAS __attribute__((address_space(1)))
#define DI __device__ __forceinline__
typedef __bf16 bf8 __attribute__((ext_vector_type(8)));
typedef __bf16 bf2 __attribute__((ext_vector_type(2)));
typedef short s4 __attribute__((ext_vector_type(4)));
typedef float f4 __attribute__((ext_vector_type(4)));
typedef float f2 __attribute__((ext_vector_type(2)));
typedef unsigned u4 __attribute__((ext_vector_type(4)));
typedef unsigned u2 __attribute__((ext_vector_type(2)));
typedef unsigned u2a __attribute__((ext_vector_type(2), aligned(4)));
typedef unsigned long long u64;

namespace {
constexpr unsigned WS_EPOCH = 0;
constexpr unsigned WS_FK = 4096;
constexpr unsigned WS_FS = WS_FK + 256 * 128;
constexpr unsigned WS_FH = WS_FS + 256 * 128;
constexpr unsigned WS_FY = WS_FH + 256 * 128;
constexpr unsigned WS_ROW = WS_FY + 256 * 128;
constexpr unsigned WS_COL = WS_ROW + 2048 * 8;
constexpr unsigned WS_KVT = 1u << 20;
constexpr unsigned WS_YT = 3u << 20;
constexpr unsigned WS_H = 5u << 20;
constexpr unsigned WS_PROF = 32u << 20;
constexpr unsigned SLOT = 49152;
constexpr unsigned L_MISC = 3 * SLOT;
constexpr unsigned L_RS = 159744;
constexpr unsigned L_CS = L_RS + 1024;
constexpr unsigned L_CTL = L_CS + 1024;
constexpr unsigned C_LAND = L_CTL, C_CONS = L_CTL + 40, C_BSYNC = L_CTL + 72, C_PRE = L_CTL + 76, C_WSYNC = L_CTL + 80, C_EPI = L_CTL + 84,
                   C_HPUB = L_CTL + 88, C_GO = L_CTL + 92, C_WD = L_CTL + 96, C_CVIN = L_CTL + 104, C_TAG = L_CTL + 108, C_PAIR = L_CTL + 112,
                   C_RD = L_CTL + 128, C_LD = L_CTL + 144, C_HOK = L_CTL + 160, L_TAG = L_CTL + 192;
constexpr unsigned CV_COPY = 4160, CV_UR = 2 * CV_COPY, CV_CH = CV_UR + 4608;
constexpr unsigned L_CV = L_RS - 2 * CV_CH;
static_assert(L_CV >= 131072, "W_down ring");
constexpr unsigned L_YI = L_CV;
constexpr unsigned LDS_BYTES = 163840;
constexpr int SPIN_CAP = 1 << 18;

DI float bflo(unsigned u) { return __uint_as_float(u << 16); }
DI float bfhi(unsigned u) { return __uint_as_float(u & 0xffff0000u); }
DI unsigned pk2(float a, float b) { f2 v = {a, b}; return __builtin_bit_cast(unsigned, __builtin_convertvector(v, bf2)); }
DI float dot2(unsigned a, bf2 b, float c) { return __builtin_amdgcn_fdot2_f32_bf16(__builtin_bit_cast(bf2, a), b, c, false); }
DI float rbf(float a) { return bflo(pk2(a, a)); }
DI f4 mfma(bf8 a, bf8 b, f4 c) { return __builtin_amdgcn_mfma_f32_16x16x32_bf16(a, b, c, 0, 0, 0); }
DI bf8 lds128(unsigned a) { return *(const LAS bf8*)(size_t)a; }
DI u4 lds128u(unsigned a) { return *(const LAS u4*)(size_t)a; }
DI f4 lds128f(unsigned a) { return *(const LAS f4*)(size_t)a; }
DI void sts128(unsigned a, u4 v) { *(LAS u4*)(size_t)a = v; }
DI void sts128f(unsigned a, f4 v) { *(LAS f4*)(size_t)a = v; }
DI bf8 ldtr(unsigned a0, unsigned a1) {
  s4 lo = __builtin_amdgcn_ds_read_tr16_b64_v4i16((LAS s4*)(size_t)a0);
  s4 hi = __builtin_amdgcn_ds_read_tr16_b64_v4i16((LAS s4*)(size_t)a1);
  return __builtin_bit_cast(bf8, __builtin_shufflevector(lo, hi, 0, 1, 2, 3, 4, 5, 6, 7));
}
#define WAITV(n) asm volatile("s_waitcnt vmcnt(%0)" ::"i"(n) : "memory")
#define WAITL(n) asm volatile("s_waitcnt lgkmcnt(%0)" ::"i"(n) : "memory")
#define SB() __builtin_amdgcn_sched_barrier(0)
#define OPAQUE(x) asm volatile("" : "+v"(x))
#define BAR() do { asm volatile("s_waitcnt lgkmcnt(0)" ::: "memory"); __builtin_amdgcn_s_barrier(); asm volatile("" ::: "memory"); } while (0)

DI void glds2(unsigned v0, unsigned v1, const char* sb, unsigned l) {
  asm volatile("s_mov_b32 m0, %3\n\ts_nop 0\n\tglobal_load_lds_dwordx4 %0, %2\n\tglobal_load_lds_dwordx4 %1, %2 offset:1024"
               ::"v"(v0), "v"(v1), "s"(sb), "s"(l) : "memory");
}
DI void glds4(unsigned v0, unsigned v1, unsigned v2, unsigned v3, const char* sb, unsigned l) {
  asm volatile("s_mov_b32 m0, %5\n\ts_nop 0\n\tglobal_load_lds_dwordx4 %0, %4\n\tglobal_load_lds_dwordx4 %1, %4 offset:1024\n\t"
               "global_load_lds_dwordx4 %2, %4 offset:2048\n\tglobal_load_lds_dwordx4 %3, %4 offset:3072"
               ::"v"(v0), "v"(v1), "v"(v2), "v"(v3), "s"(sb), "s"(l) : "memory");
}
DI void flag_set(char* ws, unsigned off, u64 tag) {
  __hip_atomic_store((GAS u64*)(ws + off), tag, __ATOMIC_RELAXED, __HIP_MEMORY_SCOPE_AGENT);
}
DI void poll(const char* ws, unsigned off, u64 tag) {
#pragma unroll 1
  for (int it = 0; it < SPIN_CAP; ++it) {
    u64 x = __hip_atomic_load((GAS u64*)(ws + off), __ATOMIC_RELAXED, __HIP_MEMORY_SCOPE_AGENT);
    if (__all(x == tag)) break;
    __builtin_amdgcn_s_sleep(4);
  }
  asm volatile("" ::: "memory");
}
DI float fadd(float a, float b) { float r; asm("v_add_f32 %0, %1, %2" : "=v"(r) : "v"(a), "v"(b)); return r; }
DI float fsub(float a, float b) { float r; asm("v_sub_f32 %0, %1, %2" : "=v"(r) : "v"(a), "v"(b)); return r; }
DI float fmul(float a, float b) { float r; asm("v_mul_f32 %0, %1, %2" : "=v"(r) : "v"(a), "v"(b)); return r; }
DI float ffma(float a, float b, float c) { float r; asm("v_fma_f32 %0, %1, %2, %3" : "=v"(r) : "v"(a), "v"(b), "v"(c)); return r; }
DI void poll2(const char* ws, unsigned off0, unsigned off1, u64 tag) {
#pragma unroll 1
  for (int it = 0; it < SPIN_CAP; ++it) {
    const u64 x = __hip_atomic_load((GAS u64*)(ws + off0), __ATOMIC_RELAXED, __HIP_MEMORY_SCOPE_AGENT);
    const u64 y = __hip_atomic_load((GAS u64*)(ws + off1), __ATOMIC_RELAXED, __HIP_MEMORY_SCOPE_AGENT);
    if (__all(x == tag && y == tag)) break;
    __builtin_amdgcn_s_sleep(8);
  }
  asm volatile("" ::: "memory");
}
template <int CTRL> DI float dpp_add(float v) {
  return v + __int_as_float(__builtin_amdgcn_update_dpp(0, __float_as_int(v), CTRL, 0xf, 0xf, true));
}
DI float wave_sum(float v) {
  v = dpp_add<0xB1>(v); v = dpp_add<0x4E>(v); v = dpp_add<0x141>(v); v = dpp_add<0x140>(v);
  const int i = __float_as_int(v);
  return (__int_as_float(__builtin_amdgcn_readlane(i, 0)) + __int_as_float(__builtin_amdgcn_readlane(i, 16))) +
         (__int_as_float(__builtin_amdgcn_readlane(i, 32)) + __int_as_float(__builtin_amdgcn_readlane(i, 48)));
}
DI void ctl_add(unsigned addr) {
  asm volatile("s_waitcnt lgkmcnt(0)\n\ts_mov_b64 exec, 1\n\tds_add_u32 %0, %1\n\ts_mov_b64 exec, -1" ::"v"(addr), "v"(1u) : "memory");
}
DI unsigned ctl_get(unsigned addr) {
  unsigned s, t;
  asm volatile("ds_read_b32 %1, %2\n\ts_waitcnt lgkmcnt(0)\n\tv_readfirstlane_b32 %0, %1\n\ts_nop 1" : "=s"(s), "=&v"(t) : "v"(addr) : "memory");
  return s;
}
DI void ctl_spin(unsigned addr, unsigned target) {
  unsigned t, s, n;
  asm volatile("s_mov_b32 %2, 0x40000\n.Lspin%=:\n\tds_read_b32 %0, %3\n\ts_waitcnt lgkmcnt(0)\n\tv_readfirstlane_b32 %1, %0\n\ts_nop 1\n\t"
               "s_cmp_ge_u32 %1, %4\n\ts_cbranch_scc1 .Lspun%=\n\ts_sub_u32 %2, %2, 1\n\ts_cmp_lg_u32 %2, 0\n\ts_cbranch_scc1 .Lspin%=\n.Lspun%=:"
               : "=&v"(t), "=&s"(s), "=&s"(n) : "v"(addr), "s"(target) : "memory", "scc");
}
DI void ctl_wait(unsigned addr, unsigned target) {
#pragma unroll 1
  for (int it = 0; it < SPIN_CAP; ++it) {
    if (ctl_get(addr) >= target) break;
    __builtin_amdgcn_s_sleep(1);
  }
}
#define STAMP(i)
#define CNOW(v)
#define CSTAMP(i)

#define HWIN 4
#define WREG 6
#define SFIRST 0
#define PROGR 0
#define PRE_FIRST 1
#define CONV_DB 1
#define CPRIO 1
#define APRIO 2
#define LIGHT67 1
#define ROWS_FIRST 0
#define PEEK_AT 5
#define SIDE_SLEEP 0
#define CODE_LINES 256
#define PF_W 1
#define PF_CODE 1
#define PF_L2 14
#define S1_EARLY 1
#define BAR_AT 28
#define KBAR() do { if (g1) BAR(); } while (0)
struct Frag { bf8 x[4]; bf8 w[2][4]; };

__global__ __launch_bounds__(512, 2) void h3_geglu_fused(const char* q, const char* kk, const char* vv, const char* shk, const char* ssk,
                                                         const char* sD, const char* wg, const char* wu, const char* wd, char* out, char* ws) {
  extern __shared__ __attribute__((aligned(16))) char smem[];
  u64 kbase;
  asm volatile("s_getpc_b64 %0" : "=s"(kbase));
  const int lane = threadIdx.x & 63, w0 = __builtin_amdgcn_readfirstlane(threadIdx.x >> 6) ^ (SFIRST ? 4 : 0);
  const int b = blockIdx.x, xcd = b & 7, r = b >> 3;
  const bool g1 = r < 28;
  const int l15 = lane & 15, g = lane >> 4;
  const __amdgpu_buffer_rsrc_t wsr = __builtin_amdgcn_make_buffer_rsrc(ws, 0, 16 << 20, 0x00020000);
  const int pnl = r >> 2;
  const int pm = (xcd >> 1) * 4 + (r & 3), pn = (xcd & 1) * 7 + pnl;
  const int rt = xcd * 4 + (r & 3), ct2 = r >> 2;
  auto stage_offsets = [&](unsigned (&xv)[4], unsigned (&wv)[4]) {
#pragma unroll
    for (int p = 0; p < 4; ++p) {
      xv[p] = (p * 8 + (lane >> 3)) * 1024 + (((lane & 7) ^ (((p & 1) << 2) | g)) << 4) - p * 1024;
      wv[p] = (p * 4 + g) * 3584 + ((l15 ^ ((g | ((p >> 1) << 2)) << 1)) << 4) - p * 1024;
    }
  };
  auto quarter = [&](const unsigned (&xv)[4], const unsigned (&wv)[4], int st, int qt) {
    const unsigned l = (st % 3) * SLOT + qt * 4096;
    glds4(xv[0], xv[1], xv[2], xv[3], q + pm * 131072 + qt * 32768 + st * 128, l);
    glds4(wv[0], wv[1], wv[2], wv[3], wg + pn * 256 + (st * 64 + qt * 16) * 3584, l + 16384);
    glds4(wv[0], wv[1], wv[2], wv[3], wu + pn * 256 + (st * 64 + qt * 16) * 3584, l + 32768);
  };
  if (w0 < 4) {
    const unsigned kvo = (lane >> 3) * 1024 + (((lane & 7) ^ ((g & 1) << 2)) << 4);
    const char* kb = kk + (r * 64 + w0 * 16) * 1024 + xcd * 128;
    const char* vb = vv + (r * 64 + w0 * 16) * 1024 + xcd * 128;
    asm volatile("s_mov_b32 m0, %6\n\ts_nop 0\n\tglobal_load_lds_dwordx4 %0, %3\n\tglobal_load_lds_dwordx4 %0, %4 offset:1024\n\t"
                 "global_load_lds_dwordx4 %1, %4 offset:2048\n\tglobal_load_lds_dwordx4 %0, %5 offset:3072\n\ts_add_u32 m0, m0, 4096\n\ts_nop 0\n\t"
                 "global_load_lds_dwordx4 %1, %8\n\tglobal_load_lds_dwordx4 %2, %7 offset:1024"
                 ::"v"(kvo), "v"(kvo + 7168), "v"((lane & 31) * 16), "s"(r == 0 && w0 == 0 ? kb : kb - 8192), "s"(kb - 1024), "s"(vb - 3072),
                   "s"(2 * SLOT + w0 * 6144), "s"(shk + xcd * 512 - 1024), "s"(vb + 1024)
                 : "memory", "scc");
  } else if (w0 < 6) {
    if (g1) {
      unsigned xv[4], wv[4];
      stage_offsets(xv, wv);
      quarter(xv, wv, 0, 2 * (w0 & 1)); quarter(xv, wv, 0, 2 * (w0 & 1) + 1); quarter(xv, wv, 1, 2 * (w0 & 1));
    }
  } else if (PF_CODE && w0 == 7 && lane < 8 && r * 8 + lane < CODE_LINES) {
    asm volatile("s_mov_b32 m0, %2\n\ts_nop 0\n\tglobal_load_lds_dword %0, %1" ::"v"((r * 8 + lane) * 128), "s"(kbase), "s"(L_RS) : "memory");
  }
  int w_ = w0;
  asm volatile("" : "+s"(w_));
  const int w = w_, gw = w & 3, tid = w * 64 + lane, t8 = tid & 255;
  auto wslot = [](int i) -> unsigned { const int s = i & 7; return s < 2 ? 2 * SLOT + s * 4096 : s < 5 ? (s - 2) * 4096 : SLOT + (s - 5) * 4096; };
  auto wdstage = [&](unsigned dv0, int kq, int i) {
    glds4(dv0, dv0 + 7168, dv0 + 2 * 7168, dv0 + 3 * 7168, wd + ct2 * 128 + (kq * 14 + i) * 32768, wslot(i) + kq * ((i & 7) < 2 ? 8192 : 12288));
  };
  u64 tag;
  auto tag_get = [&]() {
    const u2 t = *(const volatile LAS u2*)(size_t)L_TAG;
    tag = ((u64)(unsigned)__builtin_amdgcn_readfirstlane(t[1]) << 32) | (unsigned)__builtin_amdgcn_readfirstlane(t[0]);
  };

  u4 xq; u2 st2;
  auto tail_load = [&]() {
    int ln = __builtin_amdgcn_mbcnt_hi(-1, __builtin_amdgcn_mbcnt_lo(-1, 0));
    OPAQUE(ln);
    const int tk = w * 8 + (ln >> 3), c8 = ln & 7;
    xq = *(const GAS u4*)(q + (rt * 64 + tk) * 1024 + ct2 * 128 + c8 * 16);
    st2 = __builtin_amdgcn_raw_buffer_load_b64(wsr, WS_ROW + (rt * 64 + tk) * 8, 0, 16);
  };

  if (w < 4) {
    {
      const unsigned lk = 2 * SLOT + gw * 6144;
      const bool top = r == 0 && gw == 0;
      const int qq = l15 >> 2, pp = lane & 3;
      unsigned ra = lk + qq * 128 + (((2 * g + (pp >> 1)) ^ ((qq >> 1) << 2)) << 4) + (pp & 1) * 8;
      OPAQUE(ra);
      WAITV(0);
      STAMP(18);
      const u2 sw = *(const LAS u2*)(size_t)(lk + 5120 + lane * 8);
      u2 kr[5], vr[4];
#pragma unroll
      for (int i = 0; i < 5; ++i) kr[i] = __builtin_bit_cast(u2, __builtin_amdgcn_ds_read_tr16_b64_v4i16((LAS s4*)(size_t)(ra + 512 + i * 512)));
#pragma unroll
      for (int i = 0; i < 4; ++i) vr[i] = __builtin_bit_cast(u2, __builtin_amdgcn_ds_read_tr16_b64_v4i16((LAS s4*)(size_t)(ra + 3072 + i * 512)));
      if (top) kr[0] = (u2){0, 0};
      const float w0 = bflo(sw[0]), w1 = bfhi(sw[0]), w2 = bflo(sw[1]), w3 = bfhi(sw[1]);
      float kf[20], u[16];
#pragma unroll
      for (int i = 0; i < 5; ++i) { kf[4 * i] = bflo(kr[i][0]); kf[4 * i + 1] = bfhi(kr[i][0]); kf[4 * i + 2] = bflo(kr[i][1]); kf[4 * i + 3] = bfhi(kr[i][1]); }
      SB();
#pragma unroll
      for (int i = 0; i < 16; ++i) u[i] = fmul(kf[i + 1], w3);
      SB();
#pragma unroll
      for (int i = 0; i < 16; ++i) u[i] = ffma(kf[i + 2], w2, u[i]);
      SB();
#pragma unroll
      for (int i = 0; i < 16; ++i) u[i] = ffma(kf[i + 3], w1, u[i]);
      SB();
#pragma unroll
      for (int i = 0; i < 16; ++i) u[i] = ffma(kf[i + 4], w0, u[i]);
      SB();
#pragma unroll
      for (int i = 0; i < 16; ++i) { const unsigned vw = vr[i >> 2][(i >> 1) & 1]; u[i] = fmul(u[i], (i & 1) ? bfhi(vw) : bflo(vw)); }
      SB();
      const unsigned ad = L_MISC + lane * 128;
      sts128(ad + (((7 - 2 * gw) ^ (lane & 7)) << 4), (u4){pk2(u[7], u[6]), pk2(u[5], u[4]), pk2(u[3], u[2]), pk2(u[1], u[0])});
      sts128(ad + (((6 - 2 * gw) ^ (lane & 7)) << 4), (u4){pk2(u[15], u[14]), pk2(u[13], u[12]), pk2(u[11], u[10]), pk2(u[9], u[8])});
      STAMP(1);
      BAR();
    }
    if (g1) {
      const int qq = l15 >> 2, pp = lane & 3;
      const int wm = w >> 1, wn = w & 1;
      f4 acc[2][4][4];
#pragma unroll
      for (int s = 0; s < 2; ++s)
#pragma unroll
        for (int e = 0; e < 4; ++e)
#pragma unroll
          for (int i = 0; i < 4; ++i) acc[s][e][i] = (f4){0.f, 0.f, 0.f, 0.f};
      unsigned xa[2], wa[4];
      xa[0] = (wm * 64 + l15) * 128 + ((g ^ (l15 >> 1)) << 4); xa[1] = xa[0] ^ 64;
#pragma unroll
      for (int e = 0; e < 4; ++e) wa[e] = (8 * g + qq) * 256 + (((4 * wn + e) ^ (qq | ((g & 1) << 2))) << 5) + pp * 8;
      Frag f0, f1;
      s4 lo_;
      auto ldpart = [&](Frag& f, int ks, int j) {
        const unsigned o = 16384 + ks * 8192;
        if (j == 0) f.x[0] = lds128(xa[ks]);
        if (j >= 1 && j <= 16) {
          const int s = (j - 1) >> 3, e = ((j - 1) >> 1) & 3;
          if (j & 1) lo_ = __builtin_amdgcn_ds_read_tr16_b64_v4i16((LAS s4*)(size_t)(wa[e] + o + s * 16384));
          else {
            const s4 hi = __builtin_amdgcn_ds_read_tr16_b64_v4i16((LAS s4*)(size_t)(wa[e] + o + s * 16384 + 1024));
            f.w[s][e] = __builtin_bit_cast(bf8, __builtin_shufflevector(lo_, hi, 0, 1, 2, 3, 4, 5, 6, 7));
          }
        }
        if (j >= 17 && j < 20) f.x[j - 16] = lds128(xa[ks] + (j - 16) * 2048);
      };
      auto mm1 = [&](const Frag& f, int j) { acc[(j >> 2) & 1][j & 3][j >> 3] = mfma(f.w[(j >> 2) & 1][j & 3], f.x[j >> 3], acc[(j >> 2) & 1][j & 3][j >> 3]); };
      auto second = [&](int st) {
        const int d = (st == 2 || st == 5) ? -2 * (int)SLOT : (int)SLOT;
#pragma unroll
        for (int j = 0; j < 32; ++j) {
          if (j == BAR_AT) { BAR(); SB(); }
          mm1(f0, j);
          if (j < 20) ldpart(f1, 1, j);
          if (j == 20) { xa[0] += d; OPAQUE(xa[0]); xa[1] += d; OPAQUE(xa[1]); }
          if (j == 21) { wa[0] += d; OPAQUE(wa[0]); wa[1] += d; OPAQUE(wa[1]); }
          if (j == 22) { wa[2] += d; OPAQUE(wa[2]); wa[3] += d; OPAQUE(wa[3]); }
          SB();
        }
        if (BAR_AT == 32) { BAR(); SB(); }
      };
      OPAQUE(xa[0]); OPAQUE(xa[1]); OPAQUE(wa[0]); OPAQUE(wa[1]); OPAQUE(wa[2]); OPAQUE(wa[3]);
      STAMP(2); CSTAMP(20);
#pragma unroll
      for (int j = 0; j < 20; ++j) ldpart(f0, 0, j);
      SB();
      second(0);
#pragma unroll 1
      for (int st = 1; st < 8; ++st) {
#pragma unroll
        for (int j = 0; j < 32; ++j) { mm1(f1, j); if (j < 20) ldpart(f0, 0, j); SB(); }
        second(st);
      }
      STAMP(16); CSTAMP(21);
      constexpr float C0 = -2.0f * 0.7978845608028654f * 1.4426950408889634f, C1 = C0 * 0.044715f;
      f4 cs[2][4];
      f2 rs[4];
      int ln1 = __builtin_amdgcn_mbcnt_hi(-1, __builtin_amdgcn_mbcnt_lo(-1, 0));
      OPAQUE(ln1);
      const int lane = ln1, l15 = lane & 15, g = lane >> 4;
#pragma unroll
      for (int j = 0; j < 32; ++j) {
        mm1(f1, j);
        if (j < 8) cs[j >> 2][j & 3] = lds128f(L_CS + ((j >> 2) * 128 + wn * 64 + (j & 3) * 16 + 4 * g) * 4);
        if (j >= 8 && j < 12) rs[j - 8] = *(const LAS f2*)(size_t)(L_RS + (wm * 64 + (j - 8) * 16 + l15) * 8);
        SB();
      }
      STAMP(3);
      const unsigned hso = WS_H + ((pm * 8 + wm * 4) * 56 + pn * 4 + wn * 2) * 1024 + lane * 16;
#pragma unroll
      for (int i = 0; i < 4; ++i) {
        const float m = -rs[i][0], rr = rs[i][1];
        const float k0 = C0 * rr, k1 = C1 * rr * rr * rr, i2 = __builtin_amdgcn_rcpf(rr * rr);
        const f2 m2 = {m, m}, k02 = {k0, k0}, k12 = {k1, k1}, i22 = {i2, i2};
        f2 a[8], c[8], z[8];
        SB();
#pragma unroll
        for (int j = 0; j < 8; ++j) {
          const int e = j >> 1, h = (j & 1) * 2;
          a[j] = __builtin_elementwise_fma(m2, (f2){cs[0][e][h], cs[0][e][h + 1]}, (f2){acc[0][e][i][h], acc[0][e][i][h + 1]});
        }
        SB();
#pragma unroll
        for (int j = 0; j < 8; ++j) z[j] = a[j] * a[j];
        SB();
#pragma unroll
        for (int j = 0; j < 8; ++j) z[j] = __builtin_elementwise_fma(z[j], k12, k02);
        SB();
#pragma unroll
        for (int j = 0; j < 8; ++j) z[j] = z[j] * a[j];
        SB();
#pragma unroll
        for (int j = 0; j < 8; ++j) {
          const int e = j >> 1, h = (j & 1) * 2;
          z[j] = (f2){__builtin_amdgcn_exp2f(z[j][0]), __builtin_amdgcn_exp2f(z[j][1])};
          c[j] = __builtin_elementwise_fma(m2, (f2){cs[1][e][h], cs[1][e][h + 1]}, (f2){acc[1][e][i][h], acc[1][e][i][h + 1]});
          SB();
        }
#pragma unroll
        for (int j = 0; j < 8; ++j) z[j] = __builtin_elementwise_fma(z[j], i22, i22);
        SB();
#pragma unroll
        for (int j = 0; j < 8; ++j) {
          z[j] = (f2){__builtin_amdgcn_rcpf(z[j][0]), __builtin_amdgcn_rcpf(z[j][1])};
          c[j] = c[j] * a[j];
          SB();
        }
#pragma unroll
        for (int j = 0; j < 8; ++j) c[j] = c[j] * z[j];
        SB();
        u4 o0, o1;
#pragma unroll
        for (int j = 0; j < 4; ++j) { o0[j] = pk2(c[j][0], c[j][1]); o1[j] = pk2(c[4 + j][0], c[4 + j][1]); }
        __builtin_amdgcn_raw_buffer_store_b128(o0, wsr, hso + i * 56 * 1024, 0, 16);
        __builtin_amdgcn_raw_buffer_store_b128(o1, wsr, hso + i * 56 * 1024 + 1024, 0, 16);
      }
      SB();
      ctl_add(C_EPI);
      tag_get();
      STAMP(4);
    } else {
      const unsigned o = ((xcd * 4 + r - 28) * 448 + gw * 112 + lane) * 128;
      if (PF_W) asm volatile("s_mov_b32 m0, %3\n\ts_nop 0\n\tglobal_load_lds_dword %0, %2\n\tglobal_load_lds_dword %1, %2"
                             ::"v"(o), "v"(o + (lane < 48 ? 8192 : 0)), "s"(wd), "s"(131072 + gw * 256) : "memory");
      if (PF_L2) {
#pragma unroll 1
        for (int j = (r - 28) * 4 + gw; j < PF_L2 * 16; j += 16) {
          const unsigned o2 = ((((j >> 2) & 3) * 14 + (j >> 4)) * 32 + (j & 3) * 8 + (lane >> 3)) * 1024 + (lane & 7) * 128;
          asm volatile("s_waitcnt vmcnt(0)\n\ts_mov_b32 m0, %2\n\ts_nop 0\n\tglobal_load_lds_dword %0, %1" ::"v"(o2), "s"(wd), "s"(131072 + gw * 256) : "memory");
        }
      }
      WAITV(0);
    }
    int ln2 = __builtin_amdgcn_mbcnt_hi(-1, __builtin_amdgcn_mbcnt_lo(-1, 0));
    OPAQUE(ln2);
    {
      const int lane = ln2, l15 = lane & 15, g = lane >> 4, qq = l15 >> 2, pp = lane & 3;
      const unsigned dv0 = (lane >> 3) * 1024 + (((lane & 7) ^ (g << 1)) * 16);
      f4 acc[4][4];
#pragma unroll
      for (int t = 0; t < 4; ++t)
#pragma unroll
        for (int i = 0; i < 4; ++i) acc[t][i] = (f4){0.f, 0.f, 0.f, 0.f};
      const unsigned da = (4 * g + qq) * 128 + (((2 * g + (qq >> 1)) & 3) << 5) + pp * 8;
      unsigned dlo[4];
#pragma unroll
      for (int t = 0; t < 4; ++t) { dlo[t] = (da ^ (t * 32)) + gw * 12288; OPAQUE(dlo[t]); }
      const unsigned dh = 2 * SLOT - gw * 4096;
      constexpr int WR = 6, HW = HWIN;
      bf8 wf[WR][4];
      auto waddr = [&](int i, int t) -> unsigned {
        if ((i & 7) >= 2) return dlo[t] + wslot(i);
        unsigned d_ = dh;
        asm volatile("" : "+s"(d_));
        unsigned a_ = dlo[t] + d_;
        OPAQUE(a_);
        return a_ + (i & 1) * 4096;
      };
      __builtin_amdgcn_s_setprio(APRIO);
      auto publish = [&]() {
        if (g1) {
          WAITV(0);
          if (lane == 0 && __hip_atomic_fetch_add((LAS unsigned*)(size_t)C_HPUB, 1u, __ATOMIC_RELAXED, __HIP_MEMORY_SCOPE_WORKGROUP) == 3)
            flag_set(ws, WS_FH + (pm * 16 + pn) * 128, tag);
        }
        STAMP(5);
      };
      if (!PRE_FIRST) publish();
      ctl_wait(C_WD + (gw >> 1) * 4, 2);
      if (!g1) tag_get();
#pragma unroll
      for (int t = 0; t < WR * 4; ++t) { const unsigned a_ = waddr(t >> 2, t & 3); wf[t >> 2][t & 3] = ldtr(a_, a_ + 2048); }
      SB();
      if (PRE_FIRST) publish();
      WAITL(0);
      STAMP(17);
#pragma unroll
      for (int i = 8; i < 14; ++i) wdstage(dv0, gw, i);
      __builtin_amdgcn_s_setprio(0);
      STAMP(9);
      const unsigned fh = WS_FH + ((rt >> 1) * 16 + ((gw * 7) >> 1) + (lane & 3)) * 128;
      unsigned have = 0;
      auto need = [&](int i) {
        const unsigned m = PROGR ? 1u << (((gw & 1) * 2 + i) >> 2) : 15u;
        u64 x_;
        unsigned t_, n_;
        asm volatile("s_and_b32 %2, %0, %4\n\ts_cmp_eq_u32 %2, %4\n\ts_cbranch_scc1 .Ldone%=\n\ts_mov_b32 %3, 0x40000\n"
                     ".Lpoll%=:\n\tglobal_load_dwordx2 %1, %5, %6 sc1\n\ts_waitcnt vmcnt(0)\n\tv_cmp_eq_u64 vcc, %7, %1\n\ts_nop 4\n\t"
                     "s_and_b32 %0, vcc_lo, 15\n\ts_and_b32 %2, %0, %4\n\ts_cmp_eq_u32 %2, %4\n\ts_cbranch_scc1 .Ldone%=\n\ts_sleep 2\n\t"
                     "s_sub_u32 %3, %3, 1\n\ts_cmp_lg_u32 %3, 0\n\ts_cbranch_scc1 .Lpoll%=\n.Ldone%=:"
                     : "+s"(have), "=&v"(x_), "=&s"(t_), "=&s"(n_) : "s"(m), "v"(fh), "s"(ws), "s"(tag) : "memory", "vcc", "scc");
        SB();
      };
      need(0);
      STAMP(6);
      u4 hf[HW][4];
      unsigned ho[4];
#pragma unroll
      for (int t = 0; t < 4; ++t) ho[t] = WS_H + ((rt * 4 + t) * 56 + gw * 14) * 1024 + lane * 16;
      auto ldh1 = [&](int i, int t) { hf[i % HW][t] = __builtin_amdgcn_raw_buffer_load_b128(wsr, ho[t] + (i & 3) * 1024, (i >> 2) * 4096, 16); };
#pragma unroll
      for (int i = 0; i < HW * 4; ++i) { if (i == 8) need(2); ldh1(i >> 2, i & 3); SB(); }
      s4 lo_;
      unsigned wa_;
#pragma unroll
      for (int i = 0; i < 14; ++i) {
        const int nw = i + WR - 1;
        if (i == 11) { tail_load(); SB(); }
        if (i + HW < 14 && ((i + HW) & 1) == 0) need(i + HW);
#pragma unroll
        for (int j = 0; j < 16; ++j) {
          const int ti = j >> 2, t = j & 3;
          acc[t][ti] = mfma(wf[i % WR][t], __builtin_bit_cast(bf8, hf[i % HW][ti]), acc[t][ti]);
          if (i >= 1 && nw < 14 && t != 3 && (j - ti) < 8) {
            const int e = j - ti;
            if (!(e & 1)) wa_ = waddr(nw, e >> 1);
            const s4 v_ = __builtin_amdgcn_ds_read_tr16_b64_v4i16((LAS s4*)(size_t)(wa_ + (e & 1) * 2048));
            if (e & 1) wf[nw % WR][e >> 1] = __builtin_bit_cast(bf8, __builtin_shufflevector(lo_, v_, 0, 1, 2, 3, 4, 5, 6, 7)); else lo_ = v_;
          }
          if (t == 3 && i + HW < 14) ldh1(i + HW, ti);
          SB();
        }
        if (i == 0) STAMP(19);
      }
      STAMP(7);
#pragma unroll
      for (int ti = 0; ti < 4; ++ti)
#pragma unroll
        for (int t = 0; t < 4; ++t)
          sts128f((ti >> 1) * SLOT + gw * 12288 + ((ti & 1) * 16 + l15) * 256 + (((4 * t + g) ^ (l15 & 7)) << 4), acc[t][ti]);
    }
    STAMP(8);
  } else {
    const int t7 = tid & 127, bw = w & 1;
    unsigned dcr;
    if (w < 6) {
      unsigned xv[4], wv[4];
      stage_offsets(xv, wv);
      auto stage = [&](int st) { quarter(xv, wv, st, 2 * bw); quarter(xv, wv, st, 2 * bw + 1); };
      const unsigned dv0 = (lane >> 3) * 1024 + (((lane & 7) ^ (g << 1)) * 16);
      WAITV(12);
      STAMP(1);
      BAR();
      if (g1) { quarter(xv, wv, 1, 2 * bw + 1); stage(2); WAITV(24); }
      KBAR();
#pragma unroll 1
      for (int st = 3; st < 8; ++st) {
        if (g1) { stage(st); WAITV(24); }
        KBAR();
      }
      STAMP(13);
      if (!g1) ctl_wait(C_TAG, 2);
      asm volatile("global_load_dword %0, %1, %2" : "=&v"(dcr) : "v"(0u), "s"(sD + b * 4) : "memory");
#pragma unroll
      for (int i = 0; i < 4; ++i) wdstage(dv0, bw * 2 + (i & 1), i >> 1);
      WAITV(17);
      KBAR();
      STAMP(3);
#pragma unroll
      for (int i = 0; i < 6; ++i) wdstage(dv0, bw * 2 + (i & 1), 2 + (i >> 1));
      KBAR();
      STAMP(19);
      tag_get();
      asm volatile("s_waitcnt vmcnt(32)" : "+v"(dcr)::"memory");
#pragma unroll
      for (int i = 0; i < 6; ++i) wdstage(dv0, bw * 2 + (i & 1), 5 + (i >> 1));
      WAITV(24);
      ctl_add(C_WD + bw * 4);
      STAMP(2);
      WAITV(0);
      ctl_add(C_WD + bw * 4);
      STAMP(4);
    } else {
      if (t7 < 48) *(LAS unsigned*)(size_t)(L_CTL + t7 * 4) = 0;
      const int ptt = b >> 3, pct = b & 7;
      const int rgrp = (xcd >> 1) * 64 + (xcd & 1) * 32 + r;
      const int cst = 2 * ((r + pnl) & 3) + ((xcd >> 1) & 1);
      const unsigned hoff = (2 * b + bw) * 4096 + lane * 16;
      u4 xr[4];
      auto rows_load = [&]() {
        asm volatile("global_load_dwordx4 %0, %4, %5\n\tglobal_load_dwordx4 %1, %4, %5 offset:1024\n\tglobal_load_dwordx4 %2, %4, %5 offset:2048\n\t"
                     "global_load_dwordx4 %3, %4, %5 offset:3072"
                     : "=&v"(xr[0]), "=&v"(xr[1]), "=&v"(xr[2]), "=&v"(xr[3]) : "v"(lane * 16), "s"(q + (rgrp * 8 + (w & 1) * 4) * 1024) : "memory");
      };
      auto rows_stat = [&]() {
        asm volatile("" : "+v"(xr[0]), "+v"(xr[1]), "+v"(xr[2]), "+v"(xr[3])::"memory");
        float e[4][8], mu[4], va[4];
#pragma unroll
        for (int i = 0; i < 4; ++i) {
#pragma unroll
          for (int j = 0; j < 4; ++j) { e[i][2 * j] = bflo(xr[i][j]); e[i][2 * j + 1] = bfhi(xr[i][j]); }
          mu[i] = fadd(fadd(fadd(e[i][0], e[i][1]), fadd(e[i][2], e[i][3])), fadd(fadd(e[i][4], e[i][5]), fadd(e[i][6], e[i][7])));
        }
        auto fold4 = [&](const float (&v)[4]) {
          const auto s0 = __builtin_amdgcn_permlane16_swap(__float_as_uint(v[0]), __float_as_uint(v[1]), false, false);
          const auto s1 = __builtin_amdgcn_permlane16_swap(__float_as_uint(v[2]), __float_as_uint(v[3]), false, false);
          const float p0 = fadd(__uint_as_float(s0[0]), __uint_as_float(s0[1])), p1 = fadd(__uint_as_float(s1[0]), __uint_as_float(s1[1]));
          const auto s2 = __builtin_amdgcn_permlane32_swap(__float_as_uint(p0), __float_as_uint(p1), false, false);
          float t = fadd(__uint_as_float(s2[0]), __uint_as_float(s2[1]));
          t = dpp_add<0xB1>(t); t = dpp_add<0x4E>(t); t = dpp_add<0x141>(t); t = dpp_add<0x140>(t);
          return t * (1.0f / 512.0f);
        };
        const float mg = fold4(mu);
#pragma unroll
        for (int i = 0; i < 4; ++i) {
          const float m = __int_as_float(__builtin_amdgcn_readlane(__float_as_int(mg), 16 * i));
          float s = 0.f;
#pragma unroll
          for (int j = 0; j < 8; ++j) { const float d = e[i][j] - m; s = ffma(d, d, s); }
          va[i] = s;
        }
        const float vg = fold4(va);
        const u2 o = {__float_as_uint(rbf(mg)), __float_as_uint(__builtin_amdgcn_rcpf(rbf(__builtin_amdgcn_sqrtf(rbf(rbf(vg) + 1e-5f)))))};
        if (l15 == 0) __builtin_amdgcn_raw_buffer_store_b64(o, wsr, WS_ROW + (rgrp * 8 + (w & 1) * 4 + g) * 8, 0, 16);
        asm volatile("" ::: "memory");
      };

      u4 wsh[8], hc[4], uc[4], c1[4]; unsigned hp[4];
      {
        const char* wb = ((xcd >> 2) ? wu : wg) + (g1 ? pn * 256 + (cst * 64 + bw * 32) * 3584 : 0);
        const unsigned o = g * 3584 + l15 * 16;
        if (SIDE_SLEEP) __builtin_amdgcn_s_sleep(SIDE_SLEEP);
        asm volatile("global_load_dwordx4 %0, %8, %12\n\tglobal_load_dwordx4 %1, %9, %12\n\tglobal_load_dwordx4 %2, %10, %12\n\tglobal_load_dwordx4 %3, %11, %12\n\t"
                     "global_load_dwordx4 %4, %8, %13\n\tglobal_load_dwordx4 %5, %9, %13\n\tglobal_load_dwordx4 %6, %10, %13\n\tglobal_load_dwordx4 %7, %11, %13"
                     : "=&v"(wsh[0]), "=&v"(wsh[1]), "=&v"(wsh[2]), "=&v"(wsh[3]), "=&v"(wsh[4]), "=&v"(wsh[5]), "=&v"(wsh[6]), "=&v"(wsh[7])
                     : "v"(o), "v"(o + 4 * 3584), "v"(o + 8 * 3584), "v"(o + 12 * 3584), "s"(wb), "s"(wb + 16 * 3584) : "memory");
        rows_load();
      }
      asm volatile("s_load_dwordx2 %0, %1, 0x0\n\ts_waitcnt lgkmcnt(0)" : "=s"(tag) : "s"(ws) : "memory");
      tag += 1;
      *(LAS u64*)(size_t)L_TAG = tag;
      BAR();
#pragma unroll
      for (int z = 0; z < 4; ++z) {
        const int id = t7 + 128 * z, c2 = id >> 3, ch = id & 7;
        const u4 t = lds128u(L_MISC + c2 * 128 + ((ch ^ (c2 & 7)) << 4));
        __builtin_amdgcn_raw_buffer_store_b128(t, wsr, WS_KVT + (pct * 64 + c2) * 4096 + 3968 - ptt * 128 + ch * 16, 0, 16);
      }
      asm volatile("s_waitcnt vmcnt(4)" : "+v"(wsh[0]), "+v"(wsh[1]), "+v"(wsh[2]), "+v"(wsh[3]), "+v"(wsh[4]), "+v"(wsh[5]), "+v"(wsh[6]), "+v"(wsh[7])::"memory");
      STAMP(17);
      if (g1) {
        float cacc[8];
#pragma unroll
        for (int j = 0; j < 4; ++j) {
          cacc[2 * j] = fadd(fadd(fadd(bflo(wsh[0][j]), bflo(wsh[1][j])), fadd(bflo(wsh[2][j]), bflo(wsh[3][j]))),
                             fadd(fadd(bflo(wsh[4][j]), bflo(wsh[5][j])), fadd(bflo(wsh[6][j]), bflo(wsh[7][j]))));
          cacc[2 * j + 1] = fadd(fadd(fadd(bfhi(wsh[0][j]), bfhi(wsh[1][j])), fadd(bfhi(wsh[2][j]), bfhi(wsh[3][j]))),
                                 fadd(fadd(bfhi(wsh[4][j]), bfhi(wsh[5][j])), fadd(bfhi(wsh[6][j]), bfhi(wsh[7][j]))));
        }
        float c4[4], c2[2];
#pragma unroll
        for (int j = 0; j < 4; ++j) {
          const auto s2 = __builtin_amdgcn_permlane16_swap(__float_as_uint(cacc[j]), __float_as_uint(cacc[4 + j]), false, false);
          c4[j] = fadd(__uint_as_float(s2[0]), __uint_as_float(s2[1]));
        }
#pragma unroll
        for (int j = 0; j < 2; ++j) {
          const auto s2 = __builtin_amdgcn_permlane32_swap(__float_as_uint(c4[j]), __float_as_uint(c4[2 + j]), false, false);
          c2[j] = fadd(__uint_as_float(s2[0]), __uint_as_float(s2[1]));
        }
        __builtin_amdgcn_raw_buffer_store_b64((u2){__float_as_uint(c2[0]), __float_as_uint(c2[1])}, wsr,
                                              WS_COL + ((pn * 16 + cst * 2 + bw) * 256 + (xcd >> 2) * 128 + l15 * 8 + (g & 1) * 4 + (g >> 1) * 2) * 4, 0, 16);
      }
      STAMP(10);
      KBAR();
      rows_stat();
      STAMP(18);
      asm volatile("global_load_dwordx4 %0, %9, %11\n\tglobal_load_dwordx4 %1, %9, %11 offset:1024\n\tglobal_load_dwordx4 %2, %9, %11 offset:2048\n\t"
                   "global_load_dwordx4 %3, %9, %11 offset:3072\n\tglobal_load_dword %4, %10, %11\n\tglobal_load_dword %5, %9, %11 offset:1020\n\t"
                   "global_load_dword %6, %9, %11 offset:2044\n\tglobal_load_dword %7, %9, %11 offset:3068\n\tglobal_load_dword %8, %12, %13"
                   : "=&v"(hc[0]), "=&v"(hc[1]), "=&v"(hc[2]), "=&v"(hc[3]), "=&v"(hp[0]), "=&v"(hp[1]), "=&v"(hp[2]), "=&v"(hp[3]), "=&v"(dcr)
                   : "v"(hoff), "v"(lane ? hoff - 4 : hoff), "s"(ssk), "v"(0u), "s"(sD + b * 4) : "memory");
      KBAR();
      WAITV(9);
      KBAR();
      if (!g1) { ctl_add(C_WSYNC); ctl_spin(C_WSYNC, 2); }
      if (tid == 384 || tid == 385) flag_set(ws, tid == 384 ? WS_FS + rgrp * 128 : WS_FK + (pct * 32 + ptt) * 128, tag);
      STAMP(20);
      unsigned fo = lane < 32 || !g1 ? WS_FK + ((b >> 5) * 32 + (lane & 31)) * 128
                                     : WS_FS + (lane < 48 ? pm * 16 + l15 : (l15 >> 2) * 64 + (xcd & 1) * 32 + (l15 & 3) + 4 * pnl) * 128;
      unsigned fo2 = WS_FS + (rt * 8 + (lane & 7)) * 128;
      OPAQUE(fo); OPAQUE(fo2);
      KBAR();
      asm volatile("s_waitcnt vmcnt(0)" : "+v"(hc[0]), "+v"(hc[1]), "+v"(hc[2]), "+v"(hc[3]), "+v"(hp[0]), "+v"(hp[1]), "+v"(hp[2]), "+v"(hp[3]), "+v"(dcr)::"memory");
      if (lane == 0) hp[0] = 0;
#pragma unroll
      for (int z = 0; z < 4; ++z) {
        c1[z] = (u4){__builtin_amdgcn_alignbit(hc[z][0], hp[z], 16), __builtin_amdgcn_alignbit(hc[z][1], hc[z][0], 16),
                     __builtin_amdgcn_alignbit(hc[z][2], hc[z][1], 16), __builtin_amdgcn_alignbit(hc[z][3], hc[z][2], 16)};
        OPAQUE(c1[z]);
      }
      u64 p0, p1;
      if (!g1) __builtin_amdgcn_s_sleep(70);
      if (PEEK_AT == 5) KBAR();
      asm volatile("global_load_dwordx2 %0, %2, %4 sc1\n\tglobal_load_dwordx2 %1, %3, %4 sc1" : "=&v"(p0), "=&v"(p1) : "v"(fo), "v"(fo2), "s"(ws) : "memory");
      KBAR();
      STAMP(23);
      asm volatile("s_waitcnt vmcnt(0)" : "+v"(p0), "+v"(p1)::"memory");
      if (!__all(p0 == tag && p1 == tag)) poll2(ws, fo, fo2, tag);
      if (!g1) ctl_add(C_TAG);
      STAMP(21);
      u2 rsv, csv[16];
      {
        const unsigned co = WS_COL + (g1 ? pn : 0) * 16384 + t7 * 8;
#define LD4(o, vreg) asm volatile("global_load_dwordx2 %0, %4, %5 sc1\n\tglobal_load_dwordx2 %1, %4, %5 offset:1024 sc1\n\t" \
                                  "global_load_dwordx2 %2, %4, %5 offset:2048 sc1\n\tglobal_load_dwordx2 %3, %4, %5 offset:3072 sc1" \
                                  : "=&v"(csv[o]), "=&v"(csv[o + 1]), "=&v"(csv[o + 2]), "=&v"(csv[o + 3]) : "v"(vreg), "s"(ws) : "memory")
        LD4(0, co); LD4(4, co + 4096); LD4(8, co + 8192); LD4(12, co + 12288);
        asm volatile("global_load_dwordx2 %0, %1, %2 sc1" : "=&v"(rsv) : "v"(WS_ROW + (g1 ? pm : 0) * 1024 + t7 * 8), "s"(ws) : "memory");
      }
      if (PEEK_AT == 4) KBAR();
      STAMP(14);
      KBAR();
      asm volatile("global_load_dwordx4 %0, %4, %5 sc1\n\tglobal_load_dwordx4 %1, %4, %5 offset:1024 sc1\n\tglobal_load_dwordx4 %2, %4, %5 offset:2048 sc1\n\t"
                   "global_load_dwordx4 %3, %4, %5 offset:3072 sc1" : "=&v"(uc[0]), "=&v"(uc[1]), "=&v"(uc[2]), "=&v"(uc[3]) : "v"(WS_KVT + hoff), "s"(ws) : "memory");
      const unsigned base = L_CV + bw * CV_CH;
#pragma unroll
      for (int z = 0; z < 4; ++z) {
        sts128(base + 32 + lane * 16 + z * 1024, hc[z]);
        sts128(base + 32 + lane * 16 + z * 1024 + CV_COPY, c1[z]);
      }
      if (lane < 4) sts128(base + (lane >> 1) * CV_COPY + (lane & 1) * 16, (u4){0, 0, 0, 0});
      if (lane >= 32) sts128(base + CV_UR + 4096 + (lane - 32) * 16, (u4){0, 0, 0, 0});
      asm volatile("s_waitcnt vmcnt(4)" : "+v"(rsv), "+v"(csv[0]), "+v"(csv[1]), "+v"(csv[2]), "+v"(csv[3]), "+v"(csv[4]), "+v"(csv[5]), "+v"(csv[6]), "+v"(csv[7]),
                   "+v"(csv[8]), "+v"(csv[9]), "+v"(csv[10]), "+v"(csv[11]), "+v"(csv[12]), "+v"(csv[13]), "+v"(csv[14]), "+v"(csv[15])::"memory");
      {
        *(LAS u2*)(size_t)(L_RS + t7 * 8) = rsv;
        float s0[4], s1[4];
#pragma unroll
        for (int i = 0; i < 4; ++i) {
          s0[i] = fadd(fadd(__uint_as_float(csv[i][0]), __uint_as_float(csv[4 + i][0])), fadd(__uint_as_float(csv[8 + i][0]), __uint_as_float(csv[12 + i][0])));
          s1[i] = fadd(fadd(__uint_as_float(csv[i][1]), __uint_as_float(csv[4 + i][1])), fadd(__uint_as_float(csv[8 + i][1]), __uint_as_float(csv[12 + i][1])));
        }
        *(LAS u2*)(size_t)(L_CS + t7 * 8) = (u2){__float_as_uint(fadd(fadd(s0[0], s0[1]), fadd(s0[2], s0[3]))), __float_as_uint(fadd(fadd(s1[0], s1[1]), fadd(s1[2], s1[3])))};
      }
      STAMP(16);
      KBAR();
      if (b == 255 && bw == 1) {
#pragma unroll 1
        for (int z = 0; z < 4; ++z) poll(ws, WS_FS + (z * 64 + lane) * 128, tag);
        if (lane == 0) flag_set(ws, WS_EPOCH, tag);
      }
      asm volatile("s_waitcnt vmcnt(0)" : "+v"(uc[0]), "+v"(uc[1]), "+v"(uc[2]), "+v"(uc[3])::"memory");
#pragma unroll
      for (int z = 0; z < 4; ++z) sts128(base + CV_UR + lane * 16 + z * 1024, uc[z]);
      ctl_add(C_CVIN);
      STAMP(15);
    }
    ctl_wait(C_CVIN, 2);
    const int ch = gw >> 1, half = gw & 1;
    const unsigned base = L_CV + ch * CV_CH;
    const unsigned o = 8 * g + l15 + 1, kap = o & 1;
    unsigned aa = base + kap * CV_COPY + 2 * (o + kap) + half * 256;
    unsigned ba = base + CV_UR + 2 * (2032 - 16 * l15 + 8 * g) - 3584 + half * 256;
    unsigned ab = aa + 8;
    OPAQUE(aa); OPAQUE(ab); OPAQUE(ba);
    f4 y[8];
#pragma unroll
    for (int n = 0; n < 8; ++n) y[n] = (f4){0.f, 0.f, 0.f, 0.f};
    bf8 A[2][8], B[2][8];
    auto ldab = [&](int rho, int i) {
      const u2a lo = *(const LAS u2a*)(size_t)(aa + rho * 64 + i * 512), hi = *(const LAS u2a*)(size_t)(ab + rho * 64 + i * 512);
      A[rho & 1][i] = __builtin_bit_cast(bf8, (u4){lo[0], lo[1], hi[0], hi[1]});
      B[rho & 1][i] = lds128(ba + rho * 64 + (7 - i) * 512);
    };
#pragma unroll
    for (int i = 0; i < 8; ++i) ldab(0, i);
    SB();
    if (g1) ctl_spin(C_EPI, 4);
    STAMP(5);
    __builtin_amdgcn_s_setprio(CPRIO);
#pragma unroll
    for (int rho = 0; rho < 4; ++rho) {
      int cnt = 0;
#pragma unroll
      for (int lv = 0; lv < 8; ++lv)
#pragma unroll
        for (int qi = 0; qi <= lv; ++qi)
#pragma unroll
          for (int p = 0; p <= lv; ++p)
            if ((qi == lv || p == lv) && qi + p < 8) {
              y[qi + p] = mfma(A[rho & 1][qi], B[rho & 1][p], y[qi + p]);
              if (rho < 3 && (cnt & 3) == 0 && (cnt >> 2) < 8) ldab(rho + 1, cnt >> 2);
              ++cnt;
              SB();
            }
    }
    STAMP(6);
    ctl_add(C_PAIR + ch * 4); ctl_spin(C_PAIR + ch * 4, 2);
    const unsigned ex0 = base + half * 4096, ex1 = base + (half ^ 1) * 4096;
#pragma unroll
    for (int n = 0; n < 4; ++n) {
      if (half) { const f4 t = y[n]; y[n] = y[4 + n]; y[4 + n] = t; }
      sts128f(ex0 + n * 1024 + lane * 16, y[4 + n]);
    }
    u2 uu[4];
#pragma unroll
    for (int n = 0; n < 4; ++n) uu[n] = *(const LAS u2*)(size_t)(base + CV_UR + 2 * (2044 - (256 * (4 * half + n) + 16 * l15 + 4 * g)));
    ctl_add(C_PAIR + ch * 4);
    const float dc = ch ? bfhi(dcr) : bflo(dcr);
#pragma unroll
    for (int n = 0; n < 4; ++n) {
      y[n][0] = ffma(bfhi(uu[n][1]), dc, y[n][0]); y[n][1] = ffma(bflo(uu[n][1]), dc, y[n][1]);
      y[n][2] = ffma(bfhi(uu[n][0]), dc, y[n][2]); y[n][3] = ffma(bflo(uu[n][0]), dc, y[n][3]);
    }
    ctl_spin(C_PAIR + ch * 4, 4);
    STAMP(11);
    u2 pk[4];
#pragma unroll
    for (int n = 0; n < 4; ++n) {
      const f4 t = lds128f(ex1 + n * 1024 + lane * 16);
      pk[n] = (u2){pk2(fadd(t[0], y[n][0]), fadd(t[1], y[n][1])), pk2(fadd(t[2], y[n][2]), fadd(t[3], y[n][3]))};
    }
#pragma unroll
    for (int n = 0; n < 4; n += 2) {
      const auto s0 = __builtin_amdgcn_permlane16_swap(pk[n][0], pk[n + 1][0], false, false);
      const auto s1 = __builtin_amdgcn_permlane16_swap(pk[n][1], pk[n + 1][1], false, false);
      const u4 o4 = {s0[0], s1[0], s0[1], s1[1]};
      __builtin_amdgcn_raw_buffer_store_b128(o4, wsr, WS_YT + (2 * b + ch) * 4096 + 2 * (256 * (4 * half + n + (g & 1)) + 16 * l15 + 8 * (g >> 1)), 0, 16);
    }
    STAMP(22);
    WAITV(0);
    ctl_add(C_BSYNC);
    ctl_spin(C_BSYNC, 4);
    if (tid == 256) flag_set(ws, WS_FY + b * 128, tag);
    STAMP(7);
    tail_load();
    if (gw == 3) {
      poll(ws, WS_FY + (ct2 * 32 + (lane & 31)) * 128, tag);
      ctl_add(C_GO);
    } else ctl_wait(C_GO, 1);
    STAMP(8);
    u4 yq[2];
#pragma unroll
    for (int z = 0; z < 2; ++z) {
      const int id = t8 + 256 * z;
      yq[z] = __builtin_amdgcn_raw_buffer_load_b128(wsr, WS_YT + (ct2 * 64 + (id >> 3)) * 4096 + rt * 128 + (id & 7) * 16, 0, 16);
    }
#pragma unroll
    for (int z = 0; z < 2; ++z) {
      const int id = t8 + 256 * z, c = id >> 3;
      sts128(L_YI + c * 128 + (((id & 7) ^ (c >> 3)) << 4), yq[z]);
    }
    __builtin_amdgcn_s_setprio(0);
    STAMP(9);
  }
  f2 xn[4], xf[4];
#pragma unroll
  for (int j = 0; j < 4; ++j) {
    xf[j] = (f2){bflo(xq[j]), bfhi(xq[j])};
    OPAQUE(xf[j]);
    const float m = __uint_as_float(st2[0]), rr = __uint_as_float(st2[1]);
    const unsigned d = pk2(xf[j][0] - m, xf[j][1] - m);
    const unsigned n = pk2(bflo(d) * rr, bfhi(d) * rr);
    xn[j] = (f2){bflo(n), bfhi(n)};
    OPAQUE(xn[j]);
  }
  int ln3 = __builtin_amdgcn_mbcnt_hi(-1, __builtin_amdgcn_mbcnt_lo(-1, 0));
  OPAQUE(ln3);
  const int tk = w * 8 + (ln3 >> 3), c8 = ln3 & 7;
  unsigned p0a = (w >> 2) * SLOT + (tk & 31) * 256 + (((2 * c8) ^ (tk & 7)) << 4), p1a = p0a ^ 16, ya = L_YI + c8 * 1024 + ((w ^ c8) << 4) + (tk & 7) * 2;
  unsigned oo = (rt * 64 + tk) * 1024 + ct2 * 128 + c8 * 16;
  OPAQUE(p0a); OPAQUE(p1a); OPAQUE(ya); OPAQUE(oo);
  BAR();
  if (w < 4) STAMP(10);
  {
    f4 o0 = lds128f(p0a), o1 = lds128f(p1a);
#pragma unroll
    for (int k4 = 1; k4 < 4; ++k4) {
      o0 += lds128f(p0a + k4 * 12288);
      o1 += lds128f(p1a + k4 * 12288);
    }
    unsigned yv[8];
#pragma unroll
    for (int j = 0; j < 8; ++j) yv[j] = *(const LAS unsigned short*)(size_t)(ya + j * 128);
    f2 o[4] = {{o0[0], o0[1]}, {o0[2], o0[3]}, {o1[0], o1[1]}, {o1[2], o1[3]}}, e[4], sg[4];
#pragma unroll
    for (int j = 0; j < 4; ++j) {
      const f2 z = o[j] * -1.4426950408889634f;
      e[j] = (f2){__builtin_amdgcn_exp2f(z[0]), __builtin_amdgcn_exp2f(z[1])} + 1.0f;
      sg[j] = xn[j] * (f2){bflo(yv[2 * j]), bflo(yv[2 * j + 1])} * o[j];
    }
    u4 res;
#pragma unroll
    for (int j = 0; j < 4; ++j) {
      const f2 t = __builtin_elementwise_fma(sg[j], (f2){__builtin_amdgcn_rcpf(e[j][0]), __builtin_amdgcn_rcpf(e[j][1])}, xf[j]);
      res[j] = pk2(t[0], t[1]);
    }
    *(GAS u4*)(out + oo) = res;
  }
  if (w < 4) STAMP(11);
}
}

extern "C" void kernel_launch(void* const* d_in, const int* in_sizes, int n_in, void* d_out, int out_size, void* d_ws, size_t ws_size,
                              hipStream_t stream) {
  (void)in_sizes; (void)n_in; (void)out_size; (void)ws_size;
  (void)hipFuncSetAttribute((const void*)h3_geglu_fused, hipFuncAttributeMaxDynamicSharedMemorySize, LDS_BYTES);
  h3_geglu_fused<<<256, 512, LDS_BYTES, stream>>>((const char*)d_in[0], (const char*)d_in[1], (const char*)d_in[2], (const char*)d_in[3],
                                                  (const char*)d_in[4], (const char*)d_in[5], (const char*)d_in[7], (const char*)d_in[8],
                                                  (const char*)d_in[9], (char*)d_out, (char*)d_ws);
}
```

```cpp
#include <hip/hip_runtime.h>
#include <stdint.h>

#define LAS __attribute__((address_space(3)))
#define GAS __attribute__((address_space(1)))
#define DI __device__ __forceinline__
typedef __bf16 bf8 __attribute__((ext_vector_type(8)));
typedef __bf16 bf2 __attribute__((ext_vector_type(2)));
typedef short s4 __attribute__((ext_vector_type(4)));
typedef float f4 __attribute__((ext_vector_type(4)));
typedef float f2 __attribute__((ext_vector_type(2)));
typedef unsigned u4 __attribute__((ext_vector_type(4)));
typedef unsigned u2 __attribute__((ext_vector_type(2)));
typedef unsigned u2a __attribute__((ext_vector_type(2), aligned(4)));
typedef unsigned long long u64;

namespace {
constexpr unsigned WS_EPOCH = 0;
constexpr unsigned WS_FK = 4096;
constexpr unsigned WS_FS = WS_FK + 256 * 128;
constexpr unsigned WS_FH = WS_FS + 256 * 128;
constexpr unsigned WS_FY = WS_FH + 256 * 128;
constexpr unsigned WS_ROW = WS_FY + 256 * 128;
constexpr unsigned WS_COL = WS_ROW + 2048 * 8;
constexpr unsigned WS_KVT = 1u << 20;
constexpr unsigned WS_YT = 3u << 20;
constexpr unsigned WS_H = 5u << 20;
constexpr unsigned WS_PROF = 32u << 20;
constexpr unsigned SLOT = 49152;
constexpr unsigned L_MISC = 3 * SLOT;
constexpr unsigned L_RS = 159744;
constexpr unsigned L_CS = L_RS + 1024;
constexpr unsigned L_CTL = L_CS + 1024;
constexpr unsigned C_LAND = L_CTL, C_CONS = L_CTL + 40, C_BSYNC = L_CTL + 72, C_PRE = L_CTL + 76, C_WSYNC = L_CTL + 80, C_EPI = L_CTL + 84,
                   C_HPUB = L_CTL + 88, C_GO = L_CTL + 92, C_WD = L_CTL + 96, C_CVIN = L_CTL + 104, C_TAG = L_CTL + 108, C_PAIR = L_CTL + 112,
                   C_RD = L_CTL + 128, C_LD = L_CTL + 144, C_HOK = L_CTL + 160, L_TAG = L_CTL + 192;
constexpr unsigned CV_COPY = 4160, CV_UR = 2 * CV_COPY, CV_CH = CV_UR + 4608;
constexpr unsigned L_CV = L_RS - 2 * CV_CH;
static_assert(L_CV >= 131072, "W_down ring");
constexpr unsigned L_YI = L_CV;
constexpr unsigned LDS_BYTES = 163840;
constexpr int SPIN_CAP = 1 << 18;

DI float bflo(unsigned u) { return __uint_as_float(u << 16); }
DI float bfhi(unsigned u) { return __uint_as_float(u & 0xffff0000u); }
DI unsigned pk2(float a, float b) { f2 v = {a, b}; return __builtin_bit_cast(unsigned, __builtin_convertvector(v, bf2)); }
DI float dot2(unsigned a, bf2 b, float c) { return __builtin_amdgcn_fdot2_f32_bf16(__builtin_bit_cast(bf2, a), b, c, false); }
DI float rbf(float a) { return bflo(pk2(a, a)); }
DI f4 mfma(bf8 a, bf8 b, f4 c) { return __builtin_amdgcn_mfma_f32_16x16x32_bf16(a, b, c, 0, 0, 0); }
DI bf8 lds128(unsigned a) { return *(const LAS bf8*)(size_t)a; }
DI u4 lds128u(unsigned a) { return *(const LAS u4*)(size_t)a; }
DI f4 lds128f(unsigned a) { return *(const LAS f4*)(size_t)a; }
DI void sts128(unsigned a, u4 v) { *(LAS u4*)(size_t)a = v; }
DI void sts128f(unsigned a, f4 v) { *(LAS f4*)(size_t)a = v; }
DI bf8 ldtr(unsigned a0, unsigned a1) {
  s4 lo = __builtin_amdgcn_ds_read_tr16_b64_v4i16((LAS s4*)(size_t)a0);
  s4 hi = __builtin_amdgcn_ds_read_tr16_b64_v4i16((LAS s4*)(size_t)a1);
  return __builtin_bit_cast(bf8, __builtin_shufflevector(lo, hi, 0, 1, 2, 3, 4, 5, 6, 7));
}
#define WAITV(n) asm volatile("s_waitcnt vmcnt(%0)" ::"i"(n) : "memory")
#define WAITL(n) asm volatile("s_waitcnt lgkmcnt(%0)" ::"i"(n) : "memory")
#define SB() __builtin_amdgcn_sched_barrier(0)
#define OPAQUE(x) asm volatile("" : "+v"(x))
#define BAR() do { asm volatile("s_waitcnt lgkmcnt(0)" ::: "memory"); __builtin_amdgcn_s_barrier(); asm volatile("" ::: "memory"); } while (0)

DI void glds2(unsigned v0, unsigned v1, const char* sb, unsigned l) {
  asm volatile("s_mov_b32 m0, %3\n\ts_nop 0\n\tglobal_load_lds_dwordx4 %0, %2\n\tglobal_load_lds_dwordx4 %1, %2 offset:1024"
               ::"v"(v0), "v"(v1), "s"(sb), "s"(l) : "memory");
}
DI void glds4(unsigned v0, unsigned v1, unsigned v2, unsigned v3, const char* sb, unsigned l) {
  asm volatile("s_mov_b32 m0, %5\n\ts_nop 0\n\tglobal_load_lds_dwordx4 %0, %4\n\tglobal_load_lds_dwordx4 %1, %4 offset:1024\n\t"
               "global_load_lds_dwordx4 %2, %4 offset:2048\n\tglobal_load_lds_dwordx4 %3, %4 offset:3072"
               ::"v"(v0), "v"(v1), "v"(v2), "v"(v3), "s"(sb), "s"(l) : "memory");
}
DI void flag_set(char* ws, unsigned off, u64 tag) {
  __hip_atomic_store((GAS u64*)(ws + off), tag, __ATOMIC_RELAXED, __HIP_MEMORY_SCOPE_AGENT);
}
DI void poll(const char* ws, unsigned off, u64 tag) {
#pragma unroll 1
  for (int it = 0; it < SPIN_CAP; ++it) {
    u64 x = __hip_atomic_load((GAS u64*)(ws + off), __ATOMIC_RELAXED, __HIP_MEMORY_SCOPE_AGENT);
    if (__all(x == tag)) break;
    __builtin_amdgcn_s_sleep(4);
  }
  asm volatile("" ::: "memory");
}
DI float fadd(float a, float b) { float r; asm("v_add_f32 %0, %1, %2" : "=v"(r) : "v"(a), "v"(b)); return r; }
DI float fsub(float a, float b) { float r; asm("v_sub_f32 %0, %1, %2" : "=v"(r) : "v"(a), "v"(b)); return r; }
DI float fmul(float a, float b) { float r; asm("v_mul_f32 %0, %1, %2" : "=v"(r) : "v"(a), "v"(b)); return r; }
DI float ffma(float a, float b, float c) { float r; asm("v_fma_f32 %0, %1, %2, %3" : "=v"(r) : "v"(a), "v"(b), "v"(c)); return r; }
DI void poll2(const char* ws, unsigned off0, unsigned off1, u64 tag) {
#pragma unroll 1
  for (int it = 0; it < SPIN_CAP; ++it) {
    const u64 x = __hip_atomic_load((GAS u64*)(ws + off0), __ATOMIC_RELAXED, __HIP_MEMORY_SCOPE_AGENT);
    const u64 y = __hip_atomic_load((GAS u64*)(ws + off1), __ATOMIC_RELAXED, __HIP_MEMORY_SCOPE_AGENT);
    if (__all(x == tag && y == tag)) break;
    __builtin_amdgcn_s_sleep(8);
  }
  asm volatile("" ::: "memory");
}
template <int CTRL> DI float dpp_add(float v) {
  return v + __int_as_float(__builtin_amdgcn_update_dpp(0, __float_as_int(v), CTRL, 0xf, 0xf, true));
}
DI float wave_sum(float v) {
  v = dpp_add<0xB1>(v); v = dpp_add<0x4E>(v); v = dpp_add<0x141>(v); v = dpp_add<0x140>(v);
  const int i = __float_as_int(v);
  return (__int_as_float(__builtin_amdgcn_readlane(i, 0)) + __int_as_float(__builtin_amdgcn_readlane(i, 16))) +
         (__int_as_float(__builtin_amdgcn_readlane(i, 32)) + __int_as_float(__builtin_amdgcn_readlane(i, 48)));
}
DI void ctl_add(unsigned addr) {
  asm volatile("s_waitcnt lgkmcnt(0)\n\ts_mov_b64 exec, 1\n\tds_add_u32 %0, %1\n\ts_mov_b64 exec, -1" ::"v"(addr), "v"(1u) : "memory");
}
DI unsigned ctl_get(unsigned addr) {
  unsigned s, t;
  asm volatile("ds_read_b32 %1, %2\n\ts_waitcnt lgkmcnt(0)\n\tv_readfirstlane_b32 %0, %1\n\ts_nop 1" : "=s"(s), "=&v"(t) : "v"(addr) : "memory");
  return s;
}
DI void ctl_spin(unsigned addr, unsigned target) {
  unsigned t, s, n;
  asm volatile("s_mov_b32 %2, 0x40000\n.Lspin%=:\n\tds_read_b32 %0, %3\n\ts_waitcnt lgkmcnt(0)\n\tv_readfirstlane_b32 %1, %0\n\ts_nop 1\n\t"
               "s_cmp_ge_u32 %1, %4\n\ts_cbranch_scc1 .Lspun%=\n\ts_sub_u32 %2, %2, 1\n\ts_cmp_lg_u32 %2, 0\n\ts_cbranch_scc1 .Lspin%=\n.Lspun%=:"
               : "=&v"(t), "=&s"(s), "=&s"(n) : "v"(addr), "s"(target) : "memory", "scc");
}
DI void ctl_wait(unsigned addr, unsigned target) {
#pragma unroll 1
  for (int it = 0; it < SPIN_CAP; ++it) {
    if (ctl_get(addr) >= target) break;
    __builtin_amdgcn_s_sleep(1);
  }
}
#define STAMP(i)
#define CNOW(v)
#define CSTAMP(i)

#define HWIN 4
#define WREG 6
#define SFIRST 0
#define PROGR 0
#define PRE_FIRST 1
#define CONV_DB 1
#define CPRIO 1
#define APRIO 2
#define LIGHT67 1
#define ROWS_FIRST 0
#define PEEK_AT 5
#define SIDE_SLEEP 0
#define CODE_LINES 256
#define PF_W 1
#define PF_CODE 1
#define PF_L2 14
#define S1_EARLY 1
#define BAR_AT 28
#define KBAR() do { if (g1) BAR(); } while (0)
struct Frag { bf8 x[4]; bf8 w[2][4]; };

__global__ __launch_bounds__(512, 2) void h3_geglu_fused(const char* q, const char* kk, const char* vv, const char* shk, const char* ssk,
                                                         const char* sD, const char* wg, const char* wu, const char* wd, char* out, char* ws) {
  extern __shared__ __attribute__((aligned(16))) char smem[];
  u64 kbase;
  asm volatile("s_getpc_b64 %0\n.Lh3_begin:" : "=s"(kbase));
  const int lane = threadIdx.x & 63, w0 = __builtin_amdgcn_readfirstlane(threadIdx.x >> 6) ^ (SFIRST ? 4 : 0);
  const int b = blockIdx.x, xcd = b & 7, r = b >> 3;
  const bool g1 = r < 28;
  const int l15 = lane & 15, g = lane >> 4;
  const __amdgpu_buffer_rsrc_t wsr = __builtin_amdgcn_make_buffer_rsrc(ws, 0, 16 << 20, 0x00020000);
  const int pnl = r >> 2;
  const int pm = (xcd >> 1) * 4 + (r & 3), pn = (xcd & 1) * 7 + pnl;
  const int rt = xcd * 4 + (r & 3), ct2 = r >> 2;
  auto stage_offsets = [&](unsigned (&xv)[4], unsigned (&wv)[4]) {
#pragma unroll
    for (int p = 0; p < 4; ++p) {
      xv[p] = (p * 8 + (lane >> 3)) * 1024 + (((lane & 7) ^ (((p & 1) << 2) | g)) << 4) - p * 1024;
      wv[p] = (p * 4 + g) * 3584 + ((l15 ^ ((g | ((p >> 1) << 2)) << 1)) << 4) - p * 1024;
    }
  };
  auto quarter = [&](const unsigned (&xv)[4], const unsigned (&wv)[4], int st, int qt) {
    const unsigned l = (st % 3) * SLOT + qt * 4096;
    glds4(xv[0], xv[1], xv[2], xv[3], q + pm * 131072 + qt * 32768 + st * 128, l);
    glds4(wv[0], wv[1], wv[2], wv[3], wg + pn * 256 + (st * 64 + qt * 16) * 3584, l + 16384);
    glds4(wv[0], wv[1], wv[2], wv[3], wu + pn * 256 + (st * 64 + qt * 16) * 3584, l + 32768);
  };
  if ((w0 >> 1) == 2) {
    if (g1) {
      unsigned xv[4], wv[4];
      stage_offsets(xv, wv);
#pragma unroll 1
      for (int i = 0; i < 3; ++i) quarter(xv, wv, i >> 1, 2 * (w0 & 1) + (i & 1));
    }
  } else if (w0 < 4) {
    const unsigned kvo = (lane >> 3) * 1024 + (((lane & 7) ^ ((g & 1) << 2)) << 4);
    const char* kb = kk + (r * 64 + w0 * 16) * 1024 + xcd * 128;
    const char* vb = vv + (r * 64 + w0 * 16) * 1024 + xcd * 128;
    asm volatile("s_mov_b32 m0, %6\n\ts_nop 0\n\tglobal_load_lds_dwordx4 %0, %3\n\tglobal_load_lds_dwordx4 %0, %4 offset:1024\n\t"
                 "global_load_lds_dwordx4 %1, %4 offset:2048\n\tglobal_load_lds_dwordx4 %0, %5 offset:3072\n\ts_add_u32 m0, m0, 4096\n\ts_nop 0\n\t"
                 "global_load_lds_dwordx4 %1, %8\n\tglobal_load_lds_dwordx4 %2, %7 offset:1024"
                 ::"v"(kvo), "v"(kvo + 7168), "v"((lane & 31) * 16), "s"(r == 0 && w0 == 0 ? kb : kb - 8192), "s"(kb - 1024), "s"(vb - 3072),
                   "s"(2 * SLOT + w0 * 6144), "s"(shk + xcd * 512 - 1024), "s"(vb + 1024)
                 : "memory", "scc");
  } else if (w0 == 7 && lane < 8) {
    unsigned len;
    asm volatile("s_mov_b32 %0, .Lh3_end-.Lh3_begin" : "=s"(len));
    if ((unsigned)(r * 8 + lane) * 128 < len)
      asm volatile("s_mov_b32 m0, %2\n\ts_nop 0\n\tglobal_load_lds_dword %0, %1" ::"v"((r * 8 + lane) * 128), "s"(kbase), "s"(L_RS) : "memory");
  }
  int w_ = w0;
  asm volatile("" : "+s"(w_));
  const int w = w_, gw = w & 3, tid = w * 64 + lane, t8 = tid & 255;
  auto wslot = [](int i) -> unsigned { const int s = i & 7; return s < 2 ? 2 * SLOT + s * 4096 : s < 5 ? (s - 2) * 4096 : SLOT + (s - 5) * 4096; };
  auto wdstage = [&](unsigned dv0, int kq, int i) {
    glds4(dv0, dv0 + 7168, dv0 + 2 * 7168, dv0 + 3 * 7168, wd + ct2 * 128 + (kq * 14 + i) * 32768, wslot(i) + kq * ((i & 7) < 2 ? 8192 : 12288));
  };
  u64 tag;
  auto tag_get = [&]() {
    const u2 t = *(const volatile LAS u2*)(size_t)L_TAG;
    tag = ((u64)(unsigned)__builtin_amdgcn_readfirstlane(t[1]) << 32) | (unsigned)__builtin_amdgcn_readfirstlane(t[0]);
  };

  u4 xq; u2 st2;
  auto tail_load = [&]() {
    int ln = __builtin_amdgcn_mbcnt_hi(-1, __builtin_amdgcn_mbcnt_lo(-1, 0));
    OPAQUE(ln);
    const int tk = w * 8 + (ln >> 3), c8 = ln & 7;
    xq = *(const GAS u4*)(q + (rt * 64 + tk) * 1024 + ct2 * 128 + c8 * 16);
    st2 = __builtin_amdgcn_raw_buffer_load_b64(wsr, WS_ROW + (rt * 64 + tk) * 8, 0, 16);
  };

  if (w < 4) {
    {
      const unsigned lk = 2 * SLOT + gw * 6144;
      const bool top = r == 0 && gw == 0;
      const int qq = l15 >> 2, pp = lane & 3;
      unsigned ra = lk + qq * 128 + (((2 * g + (pp >> 1)) ^ ((qq >> 1) << 2)) << 4) + (pp & 1) * 8;
      OPAQUE(ra);
      WAITV(0);
      STAMP(18);
      const u2 sw = *(const LAS u2*)(size_t)(lk + 5120 + lane * 8);
      u2 kr[5], vr[4];
#pragma unroll
      for (int i = 0; i < 5; ++i) kr[i] = __builtin_bit_cast(u2, __builtin_amdgcn_ds_read_tr16_b64_v4i16((LAS s4*)(size_t)(ra + 512 + i * 512)));
#pragma unroll
      for (int i = 0; i < 4; ++i) vr[i] = __builtin_bit_cast(u2, __builtin_amdgcn_ds_read_tr16_b64_v4i16((LAS s4*)(size_t)(ra + 3072 + i * 512)));
      if (top) kr[0] = (u2){0, 0};
      const float w0 = bflo(sw[0]), w1 = bfhi(sw[0]), w2 = bflo(sw[1]), w3 = bfhi(sw[1]);
      float kf[20], u[16];
#pragma unroll
      for (int i = 0; i < 5; ++i) { kf[4 * i] = bflo(kr[i][0]); kf[4 * i + 1] = bfhi(kr[i][0]); kf[4 * i + 2] = bflo(kr[i][1]); kf[4 * i + 3] = bfhi(kr[i][1]); }
      SB();
#pragma unroll
      for (int i = 0; i < 16; ++i) u[i] = fmul(kf[i + 1], w3);
      SB();
#pragma unroll
      for (int i = 0; i < 16; ++i) u[i] = ffma(kf[i + 2], w2, u[i]);
      SB();
#pragma unroll
      for (int i = 0; i < 16; ++i) u[i] = ffma(kf[i + 3], w1, u[i]);
      SB();
#pragma unroll
      for (int i = 0; i < 16; ++i) u[i] = ffma(kf[i + 4], w0, u[i]);
      SB();
#pragma unroll
      for (int i = 0; i < 16; ++i) { const unsigned vw = vr[i >> 2][(i >> 1) & 1]; u[i] = fmul(u[i], (i & 1) ? bfhi(vw) : bflo(vw)); }
      SB();
      const unsigned ad = L_MISC + lane * 128;
      sts128(ad + (((7 - 2 * gw) ^ (lane & 7)) << 4), (u4){pk2(u[7], u[6]), pk2(u[5], u[4]), pk2(u[3], u[2]), pk2(u[1], u[0])});
      sts128(ad + (((6 - 2 * gw) ^ (lane & 7)) << 4), (u4){pk2(u[15], u[14]), pk2(u[13], u[12]), pk2(u[11], u[10]), pk2(u[9], u[8])});
      STAMP(1);
      BAR();
    }
    if (g1) {
      const int qq = l15 >> 2, pp = lane & 3;
      const int wm = w >> 1, wn = w & 1;
      f4 acc[2][4][4];
#pragma unroll
      for (int s = 0; s < 2; ++s)
#pragma unroll
        for (int e = 0; e < 4; ++e)
#pragma unroll
          for (int i = 0; i < 4; ++i) acc[s][e][i] = (f4){0.f, 0.f, 0.f, 0.f};
      unsigned xa[2], wa[4];
      xa[0] = (wm * 64 + l15) * 128 + ((g ^ (l15 >> 1)) << 4); xa[1] = xa[0] ^ 64;
#pragma unroll
      for (int e = 0; e < 4; ++e) wa[e] = (8 * g + qq) * 256 + (((4 * wn + e) ^ (qq | ((g & 1) << 2))) << 5) + pp * 8;
      Frag f0, f1;
      s4 lo_;
      auto ldpart = [&](Frag& f, int ks, int j) {
        const unsigned o = 16384 + ks * 8192;
        if (j == 0) f.x[0] = lds128(xa[ks]);
        if (j >= 1 && j <= 16) {
          const int s = (j - 1) >> 3, e = ((j - 1) >> 1) & 3;
          if (j & 1) lo_ = __builtin_amdgcn_ds_read_tr16_b64_v4i16((LAS s4*)(size_t)(wa[e] + o + s * 16384));
          else {
            const s4 hi = __builtin_amdgcn_ds_read_tr16_b64_v4i16((LAS s4*)(size_t)(wa[e] + o + s * 16384 + 1024));
            f.w[s][e] = __builtin_bit_cast(bf8, __builtin_shufflevector(lo_, hi, 0, 1, 2, 3, 4, 5, 6, 7));
          }
        }
        if (j >= 17 && j < 20) f.x[j - 16] = lds128(xa[ks] + (j - 16) * 2048);
      };
      auto mm1 = [&](const Frag& f, int j) { acc[(j >> 2) & 1][j & 3][j >> 3] = mfma(f.w[(j >> 2) & 1][j & 3], f.x[j >> 3], acc[(j >> 2) & 1][j & 3][j >> 3]); };
      auto second = [&](int st) {
        const int d = (st == 2 || st == 5) ? -2 * (int)SLOT : (int)SLOT;
#pragma unroll
        for (int j = 0; j < 32; ++j) {
          if (j == BAR_AT) { BAR(); SB(); }
          mm1(f0, j);
          if (j < 20) ldpart(f1, 1, j);
          if (j == 20) { xa[0] += d; OPAQUE(xa[0]); xa[1] += d; OPAQUE(xa[1]); }
          if (j == 21) { wa[0] += d; OPAQUE(wa[0]); wa[1] += d; OPAQUE(wa[1]); }
          if (j == 22) { wa[2] += d; OPAQUE(wa[2]); wa[3] += d; OPAQUE(wa[3]); }
          SB();
        }
        if (BAR_AT == 32) { BAR(); SB(); }
      };
      OPAQUE(xa[0]); OPAQUE(xa[1]); OPAQUE(wa[0]); OPAQUE(wa[1]); OPAQUE(wa[2]); OPAQUE(wa[3]);
      STAMP(2); CSTAMP(20);
#pragma unroll
      for (int j = 0; j < 20; ++j) ldpart(f0, 0, j);
      SB();
      second(0);
#pragma unroll 1
      for (int st = 1; st < 8; ++st) {
#pragma unroll
        for (int j = 0; j < 32; ++j) { mm1(f1, j); if (j < 20) ldpart(f0, 0, j); SB(); }
        second(st);
      }
      STAMP(16); CSTAMP(21);
      constexpr float C0 = -2.0f * 0.7978845608028654f * 1.4426950408889634f, C1 = C0 * 0.044715f;
      f4 cs[2][4];
      f2 rs[4];
      int ln1 = __builtin_amdgcn_mbcnt_hi(-1, __builtin_amdgcn_mbcnt_lo(-1, 0));
      OPAQUE(ln1);
      const int lane = ln1, l15 = lane & 15, g = lane >> 4;
#pragma unroll
      for (int j = 0; j < 32; ++j) {
        mm1(f1, j);
        if (j < 8) cs[j >> 2][j & 3] = lds128f(L_CS + ((j >> 2) * 128 + wn * 64 + (j & 3) * 16 + 4 * g) * 4);
        if (j >= 8 && j < 12) rs[j - 8] = *(const LAS f2*)(size_t)(L_RS + (wm * 64 + (j - 8) * 16 + l15) * 8);
        SB();
      }
      STAMP(3);
      const unsigned hso = WS_H + ((pm * 8 + wm * 4) * 56 + pn * 4 + wn * 2) * 1024 + lane * 16;
#pragma unroll
      for (int i = 0; i < 4; ++i) {
        const float m = -rs[i][0], rr = rs[i][1];
        const float k0 = C0 * rr, k1 = C1 * rr * rr * rr, i2 = __builtin_amdgcn_rcpf(rr * rr);
        const f2 m2 = {m, m}, k02 = {k0, k0}, k12 = {k1, k1}, i22 = {i2, i2};
        f2 a[8], c[8], z[8];
        SB();
#pragma unroll
        for (int j = 0; j < 8; ++j) {
          const int e = j >> 1, h = (j & 1) * 2;
          a[j] = __builtin_elementwise_fma(m2, (f2){cs[0][e][h], cs[0][e][h + 1]}, (f2){acc[0][e][i][h], acc[0][e][i][h + 1]});
        }
        SB();
#pragma unroll
        for (int j = 0; j < 8; ++j) z[j] = a[j] * a[j];
        SB();
#pragma unroll
        for (int j = 0; j < 8; ++j) z[j] = __builtin_elementwise_fma(z[j], k12, k02);
        SB();
#pragma unroll
        for (int j = 0; j < 8; ++j) z[j] = z[j] * a[j];
        SB();
#pragma unroll
        for (int j = 0; j < 8; ++j) {
          const int e = j >> 1, h = (j & 1) * 2;
          z[j] = (f2){__builtin_amdgcn_exp2f(z[j][0]), __builtin_amdgcn_exp2f(z[j][1])};
          c[j] = __builtin_elementwise_fma(m2, (f2){cs[1][e][h], cs[1][e][h + 1]}, (f2){acc[1][e][i][h], acc[1][e][i][h + 1]});
          SB();
        }
#pragma unroll
        for (int j = 0; j < 8; ++j) z[j] = __builtin_elementwise_fma(z[j], i22, i22);
        SB();
#pragma unroll
        for (int j = 0; j < 8; ++j) {
          z[j] = (f2){__builtin_amdgcn_rcpf(z[j][0]), __builtin_amdgcn_rcpf(z[j][1])};
          c[j] = c[j] * a[j];
          SB();
        }
#pragma unroll
        for (int j = 0; j < 8; ++j) c[j] = c[j] * z[j];
        SB();
        u4 o0, o1;
#pragma unroll
        for (int j = 0; j < 4; ++j) { o0[j] = pk2(c[j][0], c[j][1]); o1[j] = pk2(c[4 + j][0], c[4 + j][1]); }
        __builtin_amdgcn_raw_buffer_store_b128(o0, wsr, hso + i * 56 * 1024, 0, 16);
        __builtin_amdgcn_raw_buffer_store_b128(o1, wsr, hso + i * 56 * 1024 + 1024, 0, 16);
      }
      SB();
      ctl_add(C_EPI);
      tag_get();
      STAMP(4);
    } else {
      const unsigned o = ((xcd * 4 + r - 28) * 448 + gw * 112 + lane) * 128;
      if (PF_W) asm volatile("s_mov_b32 m0, %3\n\ts_nop 0\n\tglobal_load_lds_dword %0, %2\n\tglobal_load_lds_dword %1, %2"
                             ::"v"(o), "v"(o + (lane < 48 ? 8192 : 0)), "s"(wd), "s"(131072 + gw * 256) : "memory");
      if (PF_L2) {
#pragma unroll 1
        for (int j = (r - 28) * 4 + gw; j < PF_L2 * 16; j += 16) {
          const unsigned o2 = ((((j >> 2) & 3) * 14 + (j >> 4)) * 32 + (j & 3) * 8 + (lane >> 3)) * 1024 + (lane & 7) * 128;
          asm volatile("s_waitcnt vmcnt(0)\n\ts_mov_b32 m0, %2\n\ts_nop 0\n\tglobal_load_lds_dword %0, %1" ::"v"(o2), "s"(wd), "s"(131072 + gw * 256) : "memory");
        }
      }
      WAITV(0);
    }
    int ln2 = __builtin_amdgcn_mbcnt_hi(-1, __builtin_amdgcn_mbcnt_lo(-1, 0));
    OPAQUE(ln2);
    {
      const int lane = ln2, l15 = lane & 15, g = lane >> 4, qq = l15 >> 2, pp = lane & 3;
      const unsigned dv0 = (lane >> 3) * 1024 + (((lane & 7) ^ (g << 1)) * 16);
      f4 acc[4][4];
#pragma unroll
      for (int t = 0; t < 4; ++t)
#pragma unroll
        for (int i = 0; i < 4; ++i) acc[t][i] = (f4){0.f, 0.f, 0.f, 0.f};
      const unsigned da = (4 * g + qq) * 128 + (((2 * g + (qq >> 1)) & 3) << 5) + pp * 8;
      unsigned dlo[4];
#pragma unroll
      for (int t = 0; t < 4; ++t) { dlo[t] = (da ^ (t * 32)) + gw * 12288; OPAQUE(dlo[t]); }
      const unsigned dh = 2 * SLOT - gw * 4096;
      constexpr int WR = 6, HW = HWIN;
      bf8 wf[WR][4];
      auto waddr = [&](int i, int t) -> unsigned {
        if ((i & 7) >= 2) return dlo[t] + wslot(i);
        unsigned d_ = dh;
        asm volatile("" : "+s"(d_));
        unsigned a_ = dlo[t] + d_;
        OPAQUE(a_);
        return a_ + (i & 1) * 4096;
      };
      __builtin_amdgcn_s_setprio(APRIO);
      auto publish = [&]() {
        if (g1) {
          WAITV(0);
          if (lane == 0 && __hip_atomic_fetch_add((LAS unsigned*)(size_t)C_HPUB, 1u, __ATOMIC_RELAXED, __HIP_MEMORY_SCOPE_WORKGROUP) == 3)
            flag_set(ws, WS_FH + (pm * 16 + pn) * 128, tag);
        }
        STAMP(5);
      };
      if (!PRE_FIRST) publish();
      ctl_wait(C_WD + (gw >> 1) * 4, 2);
      if (!g1) tag_get();
#pragma unroll
      for (int t = 0; t < WR * 4; ++t) { const unsigned a_ = waddr(t >> 2, t & 3); wf[t >> 2][t & 3] = ldtr(a_, a_ + 2048); }
      SB();
      if (PRE_FIRST) publish();
      WAITL(0);
      STAMP(17);
#pragma unroll
      for (int i = 8; i < 14; ++i) wdstage(dv0, gw, i);
      __builtin_amdgcn_s_setprio(0);
      STAMP(9);
      const unsigned fh = WS_FH + ((rt >> 1) * 16 + ((gw * 7) >> 1) + (lane & 3)) * 128;
      unsigned have = 0;
      auto need = [&](int i) {
        const unsigned m = PROGR ? 1u << (((gw & 1) * 2 + i) >> 2) : 15u;
        u64 x_;
        unsigned t_, n_;
        asm volatile("s_and_b32 %2, %0, %4\n\ts_cmp_eq_u32 %2, %4\n\ts_cbranch_scc1 .Ldone%=\n\ts_mov_b32 %3, 0x40000\n"
                     ".Lpoll%=:\n\tglobal_load_dwordx2 %1, %5, %6 sc1\n\ts_waitcnt vmcnt(0)\n\tv_cmp_eq_u64 vcc, %7, %1\n\ts_nop 4\n\t"
                     "s_and_b32 %0, vcc_lo, 15\n\ts_and_b32 %2, %0, %4\n\ts_cmp_eq_u32 %2, %4\n\ts_cbranch_scc1 .Ldone%=\n\ts_sleep 2\n\t"
                     "s_sub_u32 %3, %3, 1\n\ts_cmp_lg_u32 %3, 0\n\ts_cbranch_scc1 .Lpoll%=\n.Ldone%=:"
                     : "+s"(have), "=&v"(x_), "=&s"(t_), "=&s"(n_) : "s"(m), "v"(fh), "s"(ws), "s"(tag) : "memory", "vcc", "scc");
        SB();
      };
      need(0);
      STAMP(6);
      u4 hf[HW][4];
      unsigned ho[4];
#pragma unroll
      for (int t = 0; t < 4; ++t) ho[t] = WS_H + ((rt * 4 + t) * 56 + gw * 14) * 1024 + lane * 16;
      auto ldh1 = [&](int i, int t) { hf[i % HW][t] = __builtin_amdgcn_raw_buffer_load_b128(wsr, ho[t] + (i & 3) * 1024, (i >> 2) * 4096, 16); };
#pragma unroll
      for (int i = 0; i < HW * 4; ++i) { if (i == 8) need(2); ldh1(i >> 2, i & 3); SB(); }
      s4 lo_;
      unsigned wa_;
#pragma unroll
      for (int i = 0; i < 14; ++i) {
        const int nw = i + WR - 1;
        if (i == 11) { tail_load(); SB(); }
        if (i + HW < 14 && ((i + HW) & 1) == 0) need(i + HW);
#pragma unroll
        for (int j = 0; j < 16; ++j) {
          const int ti = j >> 2, t = j & 3;
          acc[t][ti] = mfma(wf[i % WR][t], __builtin_bit_cast(bf8, hf[i % HW][ti]), acc[t][ti]);
          if (i >= 1 && nw < 14 && t != 3 && (j - ti) < 8) {
            const int e = j - ti;
            if (!(e & 1)) wa_ = waddr(nw, e >> 1);
            const s4 v_ = __builtin_amdgcn_ds_read_tr16_b64_v4i16((LAS s4*)(size_t)(wa_ + (e & 1) * 2048));
            if (e & 1) wf[nw % WR][e >> 1] = __builtin_bit_cast(bf8, __builtin_shufflevector(lo_, v_, 0, 1, 2, 3, 4, 5, 6, 7)); else lo_ = v_;
          }
          if (t == 3 && i + HW < 14) ldh1(i + HW, ti);
          SB();
        }
        if (i == 0) STAMP(19);
      }
      STAMP(7);
#pragma unroll
      for (int ti = 0; ti < 4; ++ti)
#pragma unroll
        for (int t = 0; t < 4; ++t)
          sts128f((ti >> 1) * SLOT + gw * 12288 + ((ti & 1) * 16 + l15) * 256 + (((4 * t + g) ^ (l15 & 7)) << 4), acc[t][ti]);
    }
    STAMP(8);
  } else {
    const int t7 = tid & 127, bw = w & 1;
    unsigned dcr;
    if (w < 6) {
      unsigned xv[4], wv[4];
      stage_offsets(xv, wv);
      auto stage = [&](int st) { quarter(xv, wv, st, 2 * bw); quarter(xv, wv, st, 2 * bw + 1); };
      const unsigned dv0 = (lane >> 3) * 1024 + (((lane & 7) ^ (g << 1)) * 16);
      WAITV(12);
      STAMP(1);
      BAR();
      if (g1) { quarter(xv, wv, 1, 2 * bw + 1); stage(2); WAITV(24); }
      KBAR();
#pragma unroll 1
      for (int st = 3; st < 8; ++st) {
        if (g1) { stage(st); WAITV(24); }
        KBAR();
      }
      STAMP(13);
      if (!g1) ctl_wait(C_TAG, 2);
      asm volatile("global_load_dword %0, %1, %2" : "=&v"(dcr) : "v"(0u), "s"(sD + b * 4) : "memory");
#pragma unroll
      for (int i = 0; i < 4; ++i) wdstage(dv0, bw * 2 + (i & 1), i >> 1);
      WAITV(17);
      KBAR();
      STAMP(3);
#pragma unroll
      for (int i = 0; i < 6; ++i) wdstage(dv0, bw * 2 + (i & 1), 2 + (i >> 1));
      KBAR();
      STAMP(19);
      tag_get();
      asm volatile("s_waitcnt vmcnt(32)" : "+v"(dcr)::"memory");
#pragma unroll
      for (int i = 0; i < 6; ++i) wdstage(dv0, bw * 2 + (i & 1), 5 + (i >> 1));
      WAITV(24);
      ctl_add(C_WD + bw * 4);
      STAMP(2);
      WAITV(0);
      ctl_add(C_WD + bw * 4);
      STAMP(4);
    } else {
      if (t7 < 48) *(LAS unsigned*)(size_t)(L_CTL + t7 * 4) = 0;
      const int ptt = b >> 3, pct = b & 7;
      const int rgrp = (xcd >> 1) * 64 + (xcd & 1) * 32 + r;
      const int cst = 2 * ((r + pnl) & 3) + ((xcd >> 1) & 1);
      const unsigned hoff = (2 * b + bw) * 4096 + lane * 16;
      u4 xr[4];
      auto rows_load = [&]() {
        asm volatile("global_load_dwordx4 %0, %4, %5\n\tglobal_load_dwordx4 %1, %4, %5 offset:1024\n\tglobal_load_dwordx4 %2, %4, %5 offset:2048\n\t"
                     "global_load_dwordx4 %3, %4, %5 offset:3072"
                     : "=&v"(xr[0]), "=&v"(xr[1]), "=&v"(xr[2]), "=&v"(xr[3]) : "v"(lane * 16), "s"(q + (rgrp * 8 + (w & 1) * 4) * 1024) : "memory");
      };
      auto rows_stat = [&]() {
        asm volatile("" : "+v"(xr[0]), "+v"(xr[1]), "+v"(xr[2]), "+v"(xr[3])::"memory");
        float e[4][8], mu[4], va[4];
#pragma unroll
        for (int i = 0; i < 4; ++i) {
#pragma unroll
          for (int j = 0; j < 4; ++j) { e[i][2 * j] = bflo(xr[i][j]); e[i][2 * j + 1] = bfhi(xr[i][j]); }
          mu[i] = fadd(fadd(fadd(e[i][0], e[i][1]), fadd(e[i][2], e[i][3])), fadd(fadd(e[i][4], e[i][5]), fadd(e[i][6], e[i][7])));
        }
        auto fold4 = [&](const float (&v)[4]) {
          const auto s0 = __builtin_amdgcn_permlane16_swap(__float_as_uint(v[0]), __float_as_uint(v[1]), false, false);
          const auto s1 = __builtin_amdgcn_permlane16_swap(__float_as_uint(v[2]), __float_as_uint(v[3]), false, false);
          const float p0 = fadd(__uint_as_float(s0[0]), __uint_as_float(s0[1])), p1 = fadd(__uint_as_float(s1[0]), __uint_as_float(s1[1]));
          const auto s2 = __builtin_amdgcn_permlane32_swap(__float_as_uint(p0), __float_as_uint(p1), false, false);
          float t = fadd(__uint_as_float(s2[0]), __uint_as_float(s2[1]));
          t = dpp_add<0xB1>(t); t = dpp_add<0x4E>(t); t = dpp_add<0x141>(t); t = dpp_add<0x140>(t);
          return t * (1.0f / 512.0f);
        };
        const float mg = fold4(mu);
#pragma unroll
        for (int i = 0; i < 4; ++i) {
          const float m = __int_as_float(__builtin_amdgcn_readlane(__float_as_int(mg), 16 * i));
          float s = 0.f;
#pragma unroll
          for (int j = 0; j < 8; ++j) { const float d = e[i][j] - m; s = ffma(d, d, s); }
          va[i] = s;
        }
        const float vg = fold4(va);
        const u2 o = {__float_as_uint(rbf(mg)), __float_as_uint(__builtin_amdgcn_rcpf(rbf(__builtin_amdgcn_sqrtf(rbf(rbf(vg) + 1e-5f)))))};
        if (l15 == 0) __builtin_amdgcn_raw_buffer_store_b64(o, wsr, WS_ROW + (rgrp * 8 + (w & 1) * 4 + g) * 8, 0, 16);
        asm volatile("" ::: "memory");
      };

      u4 wsh[8], hc[4], uc[4], c1[4]; unsigned hp[4]; u2 ep;
      {
        const char* wb = ((xcd >> 2) ? wu : wg) + (g1 ? pn * 256 + (cst * 64 + bw * 32) * 3584 : 0);
        const unsigned o = g * 3584 + l15 * 16;
        if (SIDE_SLEEP) __builtin_amdgcn_s_sleep(SIDE_SLEEP);
        asm volatile("global_load_dwordx4 %0, %8, %12\n\tglobal_load_dwordx4 %1, %9, %12\n\tglobal_load_dwordx4 %2, %10, %12\n\tglobal_load_dwordx4 %3, %11, %12\n\t"
                     "global_load_dwordx4 %4, %8, %13\n\tglobal_load_dwordx4 %5, %9, %13\n\tglobal_load_dwordx4 %6, %10, %13\n\tglobal_load_dwordx4 %7, %11, %13"
                     : "=&v"(wsh[0]), "=&v"(wsh[1]), "=&v"(wsh[2]), "=&v"(wsh[3]), "=&v"(wsh[4]), "=&v"(wsh[5]), "=&v"(wsh[6]), "=&v"(wsh[7])
                     : "v"(o), "v"(o + 4 * 3584), "v"(o + 8 * 3584), "v"(o + 12 * 3584), "s"(wb), "s"(wb + 16 * 3584) : "memory");
        asm volatile("global_load_dwordx2 %0, %1, %2 sc1" : "=&v"(ep) : "v"(WS_EPOCH), "s"(ws) : "memory");
        rows_load();
      }
      BAR();
#pragma unroll
      for (int z = 0; z < 4; ++z) {
        const int id = t7 + 128 * z, c2 = id >> 3, ch = id & 7;
        const u4 t = lds128u(L_MISC + c2 * 128 + ((ch ^ (c2 & 7)) << 4));
        __builtin_amdgcn_raw_buffer_store_b128(t, wsr, WS_KVT + (pct * 64 + c2) * 4096 + 3968 - ptt * 128 + ch * 16, 0, 16);
      }
      asm volatile("s_waitcnt vmcnt(4)" : "+v"(wsh[0]), "+v"(wsh[1]), "+v"(wsh[2]), "+v"(wsh[3]), "+v"(wsh[4]), "+v"(wsh[5]), "+v"(wsh[6]), "+v"(wsh[7]), "+v"(ep)::"memory");
      tag = (((u64)(unsigned)__builtin_amdgcn_readfirstlane(ep[1]) << 32) | (unsigned)__builtin_amdgcn_readfirstlane(ep[0])) + 1;
      *(LAS u64*)(size_t)L_TAG = tag;
      STAMP(17);
      if (g1) {
        float cacc[8];
#pragma unroll
        for (int j = 0; j < 4; ++j) {
          cacc[2 * j] = fadd(fadd(fadd(bflo(wsh[0][j]), bflo(wsh[1][j])), fadd(bflo(wsh[2][j]), bflo(wsh[3][j]))),
                             fadd(fadd(bflo(wsh[4][j]), bflo(wsh[5][j])), fadd(bflo(wsh[6][j]), bflo(wsh[7][j]))));
          cacc[2 * j + 1] = fadd(fadd(fadd(bfhi(wsh[0][j]), bfhi(wsh[1][j])), fadd(bfhi(wsh[2][j]), bfhi(wsh[3][j]))),
                                 fadd(fadd(bfhi(wsh[4][j]), bfhi(wsh[5][j])), fadd(bfhi(wsh[6][j]), bfhi(wsh[7][j]))));
        }
        float c4[4], c2[2];
#pragma unroll
        for (int j = 0; j < 4; ++j) {
          const auto s2 = __builtin_amdgcn_permlane16_swap(__float_as_uint(cacc[j]), __float_as_uint(cacc[4 + j]), false, false);
          c4[j] = fadd(__uint_as_float(s2[0]), __uint_as_float(s2[1]));
        }
#pragma unroll
        for (int j = 0; j < 2; ++j) {
          const auto s2 = __builtin_amdgcn_permlane32_swap(__float_as_uint(c4[j]), __float_as_uint(c4[2 + j]), false, false);
          c2[j] = fadd(__uint_as_float(s2[0]), __uint_as_float(s2[1]));
        }
        __builtin_amdgcn_raw_buffer_store_b64((u2){__float_as_uint(c2[0]), __float_as_uint(c2[1])}, wsr,
                                              WS_COL + ((pn * 16 + cst * 2 + bw) * 256 + (xcd >> 2) * 128 + l15 * 8 + (g & 1) * 4 + (g >> 1) * 2) * 4, 0, 16);
      }
      STAMP(10);
      KBAR();
      rows_stat();
      STAMP(18);
      asm volatile("global_load_dwordx4 %0, %9, %11\n\tglobal_load_dwordx4 %1, %9, %11 offset:1024\n\tglobal_load_dwordx4 %2, %9, %11 offset:2048\n\t"
                   "global_load_dwordx4 %3, %9, %11 offset:3072\n\tglobal_load_dword %4, %10, %11\n\tglobal_load_dword %5, %9, %11 offset:1020\n\t"
                   "global_load_dword %6, %9, %11 offset:2044\n\tglobal_load_dword %7, %9, %11 offset:3068\n\tglobal_load_dword %8, %12, %13"
                   : "=&v"(hc[0]), "=&v"(hc[1]), "=&v"(hc[2]), "=&v"(hc[3]), "=&v"(hp[0]), "=&v"(hp[1]), "=&v"(hp[2]), "=&v"(hp[3]), "=&v"(dcr)
                   : "v"(hoff), "v"(lane ? hoff - 4 : hoff), "s"(ssk), "v"(0u), "s"(sD + b * 4) : "memory");
      KBAR();
      WAITV(9);
      KBAR();
      if (!g1) { ctl_add(C_WSYNC); ctl_spin(C_WSYNC, 2); }
      if (tid == 384 || tid == 385) flag_set(ws, tid == 384 ? WS_FS + rgrp * 128 : WS_FK + (pct * 32 + ptt) * 128, tag);
      STAMP(20);
      unsigned fo = lane < 32 || !g1 ? WS_FK + ((b >> 5) * 32 + (lane & 31)) * 128
                                     : WS_FS + (lane < 48 ? pm * 16 + l15 : (l15 >> 2) * 64 + (xcd & 1) * 32 + (l15 & 3) + 4 * pnl) * 128;
      unsigned fo2 = WS_FS + (rt * 8 + (lane & 7)) * 128;
      OPAQUE(fo); OPAQUE(fo2);
      KBAR();
      asm volatile("s_waitcnt vmcnt(0)" : "+v"(hc[0]), "+v"(hc[1]), "+v"(hc[2]), "+v"(hc[3]), "+v"(hp[0]), "+v"(hp[1]), "+v"(hp[2]), "+v"(hp[3]), "+v"(dcr)::"memory");
      if (lane == 0) hp[0] = 0;
#pragma unroll
      for (int z = 0; z < 4; ++z) {
        c1[z] = (u4){__builtin_amdgcn_alignbit(hc[z][0], hp[z], 16), __builtin_amdgcn_alignbit(hc[z][1], hc[z][0], 16),
                     __builtin_amdgcn_alignbit(hc[z][2], hc[z][1], 16), __builtin_amdgcn_alignbit(hc[z][3], hc[z][2], 16)};
        OPAQUE(c1[z]);
      }
      u64 p0, p1;
      if (!g1) __builtin_amdgcn_s_sleep(70);
      if (PEEK_AT == 5) KBAR();
      asm volatile("global_load_dwordx2 %0, %2, %4 sc1\n\tglobal_load_dwordx2 %1, %3, %4 sc1" : "=&v"(p0), "=&v"(p1) : "v"(fo), "v"(fo2), "s"(ws) : "memory");
      KBAR();
      STAMP(23);
      asm volatile("s_waitcnt vmcnt(0)" : "+v"(p0), "+v"(p1)::"memory");
      if (!__all(p0 == tag && p1 == tag)) poll2(ws, fo, fo2, tag);
      if (!g1) ctl_add(C_TAG);
      STAMP(21);
      u2 rsv, csv[16];
      {
        const unsigned co = WS_COL + (g1 ? pn : 0) * 16384 + t7 * 8;
#define LD4(o, vreg) asm volatile("global_load_dwordx2 %0, %4, %5 sc1\n\tglobal_load_dwordx2 %1, %4, %5 offset:1024 sc1\n\t" \
                                  "global_load_dwordx2 %2, %4, %5 offset:2048 sc1\n\tglobal_load_dwordx2 %3, %4, %5 offset:3072 sc1" \
                                  : "=&v"(csv[o]), "=&v"(csv[o + 1]), "=&v"(csv[o + 2]), "=&v"(csv[o + 3]) : "v"(vreg), "s"(ws) : "memory")
        LD4(0, co); LD4(4, co + 4096); LD4(8, co + 8192); LD4(12, co + 12288);
        asm volatile("global_load_dwordx2 %0, %1, %2 sc1" : "=&v"(rsv) : "v"(WS_ROW + (g1 ? pm : 0) * 1024 + t7 * 8), "s"(ws) : "memory");
      }
      if (PEEK_AT == 4) KBAR();
      STAMP(14);
      KBAR();
      asm volatile("global_load_dwordx4 %0, %4, %5 sc1\n\tglobal_load_dwordx4 %1, %4, %5 offset:1024 sc1\n\tglobal_load_dwordx4 %2, %4, %5 offset:2048 sc1\n\t"
                   "global_load_dwordx4 %3, %4, %5 offset:3072 sc1" : "=&v"(uc[0]), "=&v"(uc[1]), "=&v"(uc[2]), "=&v"(uc[3]) : "v"(WS_KVT + hoff), "s"(ws) : "memory");
      const unsigned base = L_CV + bw * CV_CH;
#pragma unroll
      for (int z = 0; z < 4; ++z) {
        sts128(base + 32 + lane * 16 + z * 1024, hc[z]);
        sts128(base + 32 + lane * 16 + z * 1024 + CV_COPY, c1[z]);
      }
      if (lane < 4) sts128(base + (lane >> 1) * CV_COPY + (lane & 1) * 16, (u4){0, 0, 0, 0});
      if (lane >= 32) sts128(base + CV_UR + 4096 + (lane - 32) * 16, (u4){0, 0, 0, 0});
      asm volatile("s_waitcnt vmcnt(4)" : "+v"(rsv), "+v"(csv[0]), "+v"(csv[1]), "+v"(csv[2]), "+v"(csv[3]), "+v"(csv[4]), "+v"(csv[5]), "+v"(csv[6]), "+v"(csv[7]),
                   "+v"(csv[8]), "+v"(csv[9]), "+v"(csv[10]), "+v"(csv[11]), "+v"(csv[12]), "+v"(csv[13]), "+v"(csv[14]), "+v"(csv[15])::"memory");
      {
        *(LAS u2*)(size_t)(L_RS + t7 * 8) = rsv;
        float s0[4], s1[4];
#pragma unroll
        for (int i = 0; i < 4; ++i) {
          s0[i] = fadd(fadd(__uint_as_float(csv[i][0]), __uint_as_float(csv[4 + i][0])), fadd(__uint_as_float(csv[8 + i][0]), __uint_as_float(csv[12 + i][0])));
          s1[i] = fadd(fadd(__uint_as_float(csv[i][1]), __uint_as_float(csv[4 + i][1])), fadd(__uint_as_float(csv[8 + i][1]), __uint_as_float(csv[12 + i][1])));
        }
        *(LAS u2*)(size_t)(L_CS + t7 * 8) = (u2){__float_as_uint(fadd(fadd(s0[0], s0[1]), fadd(s0[2], s0[3]))), __float_as_uint(fadd(fadd(s1[0], s1[1]), fadd(s1[2], s1[3])))};
      }
      STAMP(16);
      KBAR();
      if (b == 255 && bw == 1) {
#pragma unroll 1
        for (int z = 0; z < 4; ++z) poll(ws, WS_FS + (z * 64 + lane) * 128, tag);
        if (lane == 0) flag_set(ws, WS_EPOCH, tag);
      }
      asm volatile("s_waitcnt vmcnt(0)" : "+v"(uc[0]), "+v"(uc[1]), "+v"(uc[2]), "+v"(uc[3])::"memory");
#pragma unroll
      for (int z = 0; z < 4; ++z) sts128(base + CV_UR + lane * 16 + z * 1024, uc[z]);
      ctl_add(C_CVIN);
      STAMP(15);
    }
    ctl_wait(C_CVIN, 2);
    const int ch = gw >> 1, half = gw & 1;
    const unsigned base = L_CV + ch * CV_CH;
    const unsigned o = 8 * g + l15 + 1, kap = o & 1;
    unsigned aa = base + kap * CV_COPY + 2 * (o + kap) + half * 256;
    unsigned ba = base + CV_UR + 2 * (2032 - 16 * l15 + 8 * g) - 3584 + half * 256;
    unsigned ab = aa + 8;
    OPAQUE(aa); OPAQUE(ab); OPAQUE(ba);
    f4 y[8];
#pragma unroll
    for (int n = 0; n < 8; ++n) y[n] = (f4){0.f, 0.f, 0.f, 0.f};
    bf8 A[2][8], B[2][8];
    auto ldab = [&](int rho, int i) {
      const u2a lo = *(const LAS u2a*)(size_t)(aa + rho * 64 + i * 512), hi = *(const LAS u2a*)(size_t)(ab + rho * 64 + i * 512);
      A[rho & 1][i] = __builtin_bit_cast(bf8, (u4){lo[0], lo[1], hi[0], hi[1]});
      B[rho & 1][i] = lds128(ba + rho * 64 + (7 - i) * 512);
    };
#pragma unroll
    for (int i = 0; i < 8; ++i) ldab(0, i);
    SB();
    if (g1) ctl_spin(C_EPI, 4);
    STAMP(5);
    __builtin_amdgcn_s_setprio(CPRIO);
#pragma unroll
    for (int rho = 0; rho < 4; ++rho) {
      int cnt = 0;
#pragma unroll
      for (int lv = 0; lv < 8; ++lv)
#pragma unroll
        for (int qi = 0; qi <= lv; ++qi)
#pragma unroll
          for (int p = 0; p <= lv; ++p)
            if ((qi == lv || p == lv) && qi + p < 8) {
              y[qi + p] = mfma(A[rho & 1][qi], B[rho & 1][p], y[qi + p]);
              if (rho < 3 && (cnt & 3) == 0 && (cnt >> 2) < 8) ldab(rho + 1, cnt >> 2);
              ++cnt;
              SB();
            }
    }
    STAMP(6);
    ctl_add(C_PAIR + ch * 4); ctl_spin(C_PAIR + ch * 4, 2);
    const unsigned ex0 = base + half * 4096, ex1 = base + (half ^ 1) * 4096;
#pragma unroll
    for (int n = 0; n < 4; ++n) {
      if (half) { const f4 t = y[n]; y[n] = y[4 + n]; y[4 + n] = t; }
      sts128f(ex0 + n * 1024 + lane * 16, y[4 + n]);
    }
    u2 uu[4];
#pragma unroll
    for (int n = 0; n < 4; ++n) uu[n] = *(const LAS u2*)(size_t)(base + CV_UR + 2 * (2044 - (256 * (4 * half + n) + 16 * l15 + 4 * g)));
    ctl_add(C_PAIR + ch * 4);
    const float dc = ch ? bfhi(dcr) : bflo(dcr);
#pragma unroll
    for (int n = 0; n < 4; ++n) {
      y[n][0] = ffma(bfhi(uu[n][1]), dc, y[n][0]); y[n][1] = ffma(bflo(uu[n][1]), dc, y[n][1]);
      y[n][2] = ffma(bfhi(uu[n][0]), dc, y[n][2]); y[n][3] = ffma(bflo(uu[n][0]), dc, y[n][3]);
    }
    ctl_spin(C_PAIR + ch * 4, 4);
    STAMP(11);
    u2 pk[4];
#pragma unroll
    for (int n = 0; n < 4; ++n) {
      const f4 t = lds128f(ex1 + n * 1024 + lane * 16);
      pk[n] = (u2){pk2(fadd(t[0], y[n][0]), fadd(t[1], y[n][1])), pk2(fadd(t[2], y[n][2]), fadd(t[3], y[n][3]))};
    }
#pragma unroll
    for (int n = 0; n < 4; n += 2) {
      const auto s0 = __builtin_amdgcn_permlane16_swap(pk[n][0], pk[n + 1][0], false, false);
      const auto s1 = __builtin_amdgcn_permlane16_swap(pk[n][1], pk[n + 1][1], false, false);
      const u4 o4 = {s0[0], s1[0], s0[1], s1[1]};
      __builtin_amdgcn_raw_buffer_store_b128(o4, wsr, WS_YT + (2 * b + ch) * 4096 + 2 * (256 * (4 * half + n + (g & 1)) + 16 * l15 + 8 * (g >> 1)), 0, 16);
    }
    STAMP(22);
    WAITV(0);
    ctl_add(C_BSYNC);
    ctl_spin(C_BSYNC, 4);
    if (tid == 256) flag_set(ws, WS_FY + b * 128, tag);
    STAMP(7);
    tail_load();
    if (gw == 3) {
      poll(ws, WS_FY + (ct2 * 32 + (lane & 31)) * 128, tag);
      ctl_add(C_GO);
    } else ctl_wait(C_GO, 1);
    STAMP(8);
    u4 yq[2];
#pragma unroll
    for (int z = 0; z < 2; ++z) {
      const int id = t8 + 256 * z;
      yq[z] = __builtin_amdgcn_raw_buffer_load_b128(wsr, WS_YT + (ct2 * 64 + (id >> 3)) * 4096 + rt * 128 + (id & 7) * 16, 0, 16);
    }
#pragma unroll
    for (int z = 0; z < 2; ++z) {
      const int id = t8 + 256 * z, c = id >> 3;
      sts128(L_YI + c * 128 + (((id & 7) ^ (c >> 3)) << 4), yq[z]);
    }
    __builtin_amdgcn_s_setprio(0);
    STAMP(9);
  }
  f2 xn[4], xf[4];
#pragma unroll
  for (int j = 0; j < 4; ++j) {
    xf[j] = (f2){bflo(xq[j]), bfhi(xq[j])};
    OPAQUE(xf[j]);
    const float m = __uint_as_float(st2[0]), rr = __uint_as_float(st2[1]);
    const unsigned d = pk2(xf[j][0] - m, xf[j][1] - m);
    const unsigned n = pk2(bflo(d) * rr, bfhi(d) * rr);
    xn[j] = (f2){bflo(n), bfhi(n)};
    OPAQUE(xn[j]);
  }
  int ln3 = __builtin_amdgcn_mbcnt_hi(-1, __builtin_amdgcn_mbcnt_lo(-1, 0));
  OPAQUE(ln3);
  const int tk = w * 8 + (ln3 >> 3), c8 = ln3 & 7;
  unsigned p0a = (w >> 2) * SLOT + (tk & 31) * 256 + (((2 * c8) ^ (tk & 7)) << 4), p1a = p0a ^ 16, ya = L_YI + c8 * 1024 + ((w ^ c8) << 4) + (tk & 7) * 2;
  unsigned oo = (rt * 64 + tk) * 1024 + ct2 * 128 + c8 * 16;
  OPAQUE(p0a); OPAQUE(p1a); OPAQUE(ya); OPAQUE(oo);
  BAR();
  if (w < 4) STAMP(10);
  {
    f4 o0 = lds128f(p0a), o1 = lds128f(p1a);
#pragma unroll
    for (int k4 = 1; k4 < 4; ++k4) {
      o0 += lds128f(p0a + k4 * 12288);
      o1 += lds128f(p1a + k4 * 12288);
    }
    unsigned yv[8];
#pragma unroll
    for (int j = 0; j < 8; ++j) yv[j] = *(const LAS unsigned short*)(size_t)(ya + j * 128);
    f2 o[4] = {{o0[0], o0[1]}, {o0[2], o0[3]}, {o1[0], o1[1]}, {o1[2], o1[3]}}, e[4], sg[4];
#pragma unroll
    for (int j = 0; j < 4; ++j) {
      const f2 z = o[j] * -1.4426950408889634f;
      e[j] = (f2){__builtin_amdgcn_exp2f(z[0]), __builtin_amdgcn_exp2f(z[1])} + 1.0f;
      sg[j] = xn[j] * (f2){bflo(yv[2 * j]), bflo(yv[2 * j + 1])} * o[j];
    }
    u4 res;
#pragma unroll
    for (int j = 0; j < 4; ++j) {
      const f2 t = __builtin_elementwise_fma(sg[j], (f2){__builtin_amdgcn_rcpf(e[j][0]), __builtin_amdgcn_rcpf(e[j][1])}, xf[j]);
      res[j] = pk2(t[0], t[1]);
    }
    *(GAS u4*)(out + oo) = res;
    asm volatile(".Lh3_end:");
  }
  if (w < 4) STAMP(11);
}
}

extern "C" void kernel_launch(void* const* d_in, const int* in_sizes, int n_in, void* d_out, int out_size, void* d_ws, size_t ws_size,
                              hipStream_t stream) {
  (void)in_sizes; (void)n_in; (void)out_size; (void)ws_size;
  (void)hipFuncSetAttribute((const void*)h3_geglu_fused, hipFuncAttributeMaxDynamicSharedMemorySize, LDS_BYTES);
  h3_geglu_fused<<<256, 512, LDS_BYTES, stream>>>((const char*)d_in[0], (const char*)d_in[1], (const char*)d_in[2], (const char*)d_in[3],
                                                  (const char*)d_in[4], (const char*)d_in[5], (const char*)d_in[7], (const char*)d_in[8],
                                                  (const char*)d_in[9], (char*)d_out, (char*)d_ws);
}
```

```cpp
#define PROF
#define PROBE_B 9
#define PROBE_W 4
#define PROBE_I 23
#include <hip/hip_runtime.h>
#include <stdint.h>

#define LAS __attribute__((address_space(3)))
#define GAS __attribute__((address_space(1)))
#define DI __device__ __forceinline__
typedef __bf16 bf8 __attribute__((ext_vector_type(8)));
typedef __bf16 bf2 __attribute__((ext_vector_type(2)));
typedef short s4 __attribute__((ext_vector_type(4)));
typedef float f4 __attribute__((ext_vector_type(4)));
typedef float f2 __attribute__((ext_vector_type(2)));
typedef unsigned u4 __attribute__((ext_vector_type(4)));
typedef unsigned u2 __attribute__((ext_vector_type(2)));
typedef unsigned u2a __attribute__((ext_vector_type(2), aligned(4)));
typedef unsigned long long u64;

namespace {
constexpr unsigned WS_EPOCH = 0;
constexpr unsigned WS_FK = 4096;
constexpr unsigned WS_FS = WS_FK + 256 * 128;
constexpr unsigned WS_FH = WS_FS + 256 * 128;
constexpr unsigned WS_FY = WS_FH + 256 * 128;
constexpr unsigned WS_ROW = WS_FY + 256 * 128;
constexpr unsigned WS_COL = WS_ROW + 2048 * 8;
constexpr unsigned WS_KVT = 1u << 20;
constexpr unsigned WS_YT = 3u << 20;
constexpr unsigned WS_H = 5u << 20;
constexpr unsigned WS_PROF = 32u << 20;
constexpr unsigned SLOT = 49152;
constexpr unsigned L_MISC = 3 * SLOT;
constexpr unsigned L_RS = 159744;
constexpr unsigned L_CS = L_RS + 1024;
constexpr unsigned L_CTL = L_CS + 1024;
constexpr unsigned C_LAND = L_CTL, C_CONS = L_CTL + 40, C_BSYNC = L_CTL + 72, C_PRE = L_CTL + 76, C_WSYNC = L_CTL + 80, C_EPI = L_CTL + 84,
                   C_HPUB = L_CTL + 88, C_GO = L_CTL + 92, C_WD = L_CTL + 96, C_CVIN = L_CTL + 104, C_TAG = L_CTL + 108, C_PAIR = L_CTL + 112,
                   C_RD = L_CTL + 128, C_LD = L_CTL + 144, C_HOK = L_CTL + 160, L_TAG = L_CTL + 192;
constexpr unsigned CV_COPY = 4160, CV_UR = 2 * CV_COPY, CV_CH = CV_UR + 4608;
constexpr unsigned L_CV = L_RS - 2 * CV_CH;
static_assert(L_CV >= 131072, "W_down ring");
constexpr unsigned L_YI = L_CV;
constexpr unsigned LDS_BYTES = 163840;
constexpr int SPIN_CAP = 1 << 18;

DI float bflo(unsigned u) { return __uint_as_float(u << 16); }
DI float bfhi(unsigned u) { return __uint_as_float(u & 0xffff0000u); }
DI unsigned pk2(float a, float b) { f2 v = {a, b}; return __builtin_bit_cast(unsigned, __builtin_convertvector(v, bf2)); }
DI float dot2(unsigned a, bf2 b, float c) { return __builtin_amdgcn_fdot2_f32_bf16(__builtin_bit_cast(bf2, a), b, c, false); }
DI float rbf(float a) { return bflo(pk2(a, a)); }
DI f4 mfma(bf8 a, bf8 b, f4 c) { return __builtin_amdgcn_mfma_f32_16x16x32_bf16(a, b, c, 0, 0, 0); }
DI bf8 lds128(unsigned a) { return *(const LAS bf8*)(size_t)a; }
DI u4 lds128u(unsigned a) { return *(const LAS u4*)(size_t)a; }
DI f4 lds128f(unsigned a) { return *(const LAS f4*)(size_t)a; }
DI void sts128(unsigned a, u4 v) { *(LAS u4*)(size_t)a = v; }
DI void sts128f(unsigned a, f4 v) { *(LAS f4*)(size_t)a = v; }
DI bf8 ldtr(unsigned a0, unsigned a1) {
  s4 lo = __builtin_amdgcn_ds_read_tr16_b64_v4i16((LAS s4*)(size_t)a0);
  s4 hi = __builtin_amdgcn_ds_read_tr16_b64_v4i16((LAS s4*)(size_t)a1);
  return __builtin_bit_cast(bf8, __builtin_shufflevector(lo, hi, 0, 1, 2, 3, 4, 5, 6, 7));
}
#define WAITV(n) asm volatile("s_waitcnt vmcnt(%0)" ::"i"(n) : "memory")
#define WAITL(n) asm volatile("s_waitcnt lgkmcnt(%0)" ::"i"(n) : "memory")
#define SB() __builtin_amdgcn_sched_barrier(0)
#define OPAQUE(x) asm volatile("" : "+v"(x))
#define BAR() do { asm volatile("s_waitcnt lgkmcnt(0)" ::: "memory"); __builtin_amdgcn_s_barrier(); asm volatile("" ::: "memory"); } while (0)

DI void glds2(unsigned v0, unsigned v1, const char* sb, unsigned l) {
  asm volatile("s_mov_b32 m0, %3\n\ts_nop 0\n\tglobal_load_lds_dwordx4 %0, %2\n\tglobal_load_lds_dwordx4 %1, %2 offset:1024"
               ::"v"(v0), "v"(v1), "s"(sb), "s"(l) : "memory");
}
DI void glds4(unsigned v0, unsigned v1, unsigned v2, unsigned v3, const char* sb, unsigned l) {
  asm volatile("s_mov_b32 m0, %5\n\ts_nop 0\n\tglobal_load_lds_dwordx4 %0, %4\n\tglobal_load_lds_dwordx4 %1, %4 offset:1024\n\t"
               "global_load_lds_dwordx4 %2, %4 offset:2048\n\tglobal_load_lds_dwordx4 %3, %4 offset:3072"
               ::"v"(v0), "v"(v1), "v"(v2), "v"(v3), "s"(sb), "s"(l) : "memory");
}
DI void flag_set(char* ws, unsigned off, u64 tag) {
  __hip_atomic_store((GAS u64*)(ws + off), tag, __ATOMIC_RELAXED, __HIP_MEMORY_SCOPE_AGENT);
}
DI void poll(const char* ws, unsigned off, u64 tag) {
#pragma unroll 1
  for (int it = 0; it < SPIN_CAP; ++it) {
    u64 x = __hip_atomic_load((GAS u64*)(ws + off), __ATOMIC_RELAXED, __HIP_MEMORY_SCOPE_AGENT);
    if (__all(x == tag)) break;
    __builtin_amdgcn_s_sleep(4);
  }
  asm volatile("" ::: "memory");
}
DI float fadd(float a, float b) { float r; asm("v_add_f32 %0, %1, %2" : "=v"(r) : "v"(a), "v"(b)); return r; }
DI float fsub(float a, float b) { float r; asm("v_sub_f32 %0, %1, %2" : "=v"(r) : "v"(a), "v"(b)); return r; }
DI float fmul(float a, float b) { float r; asm("v_mul_f32 %0, %1, %2" : "=v"(r) : "v"(a), "v"(b)); return r; }
DI float ffma(float a, float b, float c) { float r; asm("v_fma_f32 %0, %1, %2, %3" : "=v"(r) : "v"(a), "v"(b), "v"(c)); return r; }
DI void poll2(const char* ws, unsigned off0, unsigned off1, u64 tag) {
#pragma unroll 1
  for (int it = 0; it < SPIN_CAP; ++it) {
    const u64 x = __hip_atomic_load((GAS u64*)(ws + off0), __ATOMIC_RELAXED, __HIP_MEMORY_SCOPE_AGENT);
    const u64 y = __hip_atomic_load((GAS u64*)(ws + off1), __ATOMIC_RELAXED, __HIP_MEMORY_SCOPE_AGENT);
    if (__all(x == tag && y == tag)) break;
    __builtin_amdgcn_s_sleep(8);
  }
  asm volatile("" ::: "memory");
}
template <int CTRL> DI float dpp_add(float v) {
  return v + __int_as_float(__builtin_amdgcn_update_dpp(0, __float_as_int(v), CTRL, 0xf, 0xf, true));
}
DI float wave_sum(float v) {
  v = dpp_add<0xB1>(v); v = dpp_add<0x4E>(v); v = dpp_add<0x141>(v); v = dpp_add<0x140>(v);
  const int i = __float_as_int(v);
  return (__int_as_float(__builtin_amdgcn_readlane(i, 0)) + __int_as_float(__builtin_amdgcn_readlane(i, 16))) +
         (__int_as_float(__builtin_amdgcn_readlane(i, 32)) + __int_as_float(__builtin_amdgcn_readlane(i, 48)));
}
DI void ctl_add(unsigned addr) {
  asm volatile("s_waitcnt lgkmcnt(0)\n\ts_mov_b64 exec, 1\n\tds_add_u32 %0, %1\n\ts_mov_b64 exec, -1" ::"v"(addr), "v"(1u) : "memory");
}
DI unsigned ctl_get(unsigned addr) {
  unsigned s, t;
  asm volatile("ds_read_b32 %1, %2\n\ts_waitcnt lgkmcnt(0)\n\tv_readfirstlane_b32 %0, %1\n\ts_nop 1" : "=s"(s), "=&v"(t) : "v"(addr) : "memory");
  return s;
}
DI void ctl_spin(unsigned addr, unsigned target) {
  unsigned t, s, n;
  asm volatile("s_mov_b32 %2, 0x40000\n.Lspin%=:\n\tds_read_b32 %0, %3\n\ts_waitcnt lgkmcnt(0)\n\tv_readfirstlane_b32 %1, %0\n\ts_nop 1\n\t"
               "s_cmp_ge_u32 %1, %4\n\ts_cbranch_scc1 .Lspun%=\n\ts_sub_u32 %2, %2, 1\n\ts_cmp_lg_u32 %2, 0\n\ts_cbranch_scc1 .Lspin%=\n.Lspun%=:"
               : "=&v"(t), "=&s"(s), "=&s"(n) : "v"(addr), "s"(target) : "memory", "scc");
}
DI void ctl_wait(unsigned addr, unsigned target) {
#pragma unroll 1
  for (int it = 0; it < SPIN_CAP; ++it) {
    if (ctl_get(addr) >= target) break;
    __builtin_amdgcn_s_sleep(1);
  }
}
#define CSTAMP(i) do { u64 t_; asm volatile("s_memtime %0\n\ts_waitcnt lgkmcnt(0)" : "=s"(t_)::"memory"); tt[i] = (unsigned)t_; } while (0)
#define CNOW(v) do { u64 t_; asm volatile("s_memtime %0\n\ts_waitcnt lgkmcnt(0)" : "=s"(t_)::"memory"); v = (unsigned)t_; } while (0)
#define STAMP(i) do { if ((i) != 0 && (i) != 12 && (i) != PROBE_I) break; u64 t_; asm volatile("s_memrealtime %0\n\ts_waitcnt lgkmcnt(0)" : "=s"(t_)::"memory"); tt[i] = (unsigned)t_; } while (0)

#define HWIN 4
#define WREG 6
#define SFIRST 0
#define PROGR 0
#define PRE_FIRST 1
#define CONV_DB 1
#define CPRIO 1
#define APRIO 2
#define LIGHT67 1
#define ROWS_FIRST 0
#define PEEK_AT 5
#define SIDE_SLEEP 0
#define CODE_LINES 256
#define PF_W 1
#define PF_CODE 1
#define PF_L2 14
#define S1_EARLY 1
#define BAR_AT 28
#define KBAR() do { if (g1) BAR(); } while (0)
struct Frag { bf8 x[4]; bf8 w[2][4]; };

__global__ __launch_bounds__(512, 2) void h3_geglu_fused(const char* q, const char* kk, const char* vv, const char* shk, const char* ssk,
                                                         const char* sD, const char* wg, const char* wu, const char* wd, char* out, char* ws) {
  extern __shared__ __attribute__((aligned(16))) char smem[];
  u64 kbase;
  asm volatile("s_getpc_b64 %0\n.Lh3_begin:" : "=s"(kbase));
  const int lane = threadIdx.x & 63, w0 = __builtin_amdgcn_readfirstlane(threadIdx.x >> 6) ^ (SFIRST ? 4 : 0);
  const int b = blockIdx.x, xcd = b & 7, r = b >> 3;
  const bool g1 = r < 28;
  const int l15 = lane & 15, g = lane >> 4;
  const __amdgpu_buffer_rsrc_t wsr = __builtin_amdgcn_make_buffer_rsrc(ws, 0, 16 << 20, 0x00020000);
  const int pnl = r >> 2;
  const int pm = (xcd >> 1) * 4 + (r & 3), pn = (xcd & 1) * 7 + pnl;
  const int rt = xcd * 4 + (r & 3), ct2 = r >> 2;
  unsigned tt[24] = {0};
  STAMP(0);
  auto stage_offsets = [&](unsigned (&xv)[4], unsigned (&wv)[4]) {
#pragma unroll
    for (int p = 0; p < 4; ++p) {
      xv[p] = (p * 8 + (lane >> 3)) * 1024 + (((lane & 7) ^ (((p & 1) << 2) | g)) << 4) - p * 1024;
      wv[p] = (p * 4 + g) * 3584 + ((l15 ^ ((g | ((p >> 1) << 2)) << 1)) << 4) - p * 1024;
    }
  };
  auto quarter = [&](const unsigned (&xv)[4], const unsigned (&wv)[4], int st, int qt) {
    const unsigned l = (st % 3) * SLOT + qt * 4096;
    glds4(xv[0], xv[1], xv[2], xv[3], q + pm * 131072 + qt * 32768 + st * 128, l);
    glds4(wv[0], wv[1], wv[2], wv[3], wg + pn * 256 + (st * 64 + qt * 16) * 3584, l + 16384);
    glds4(wv[0], wv[1], wv[2], wv[3], wu + pn * 256 + (st * 64 + qt * 16) * 3584, l + 32768);
  };
  if ((w0 >> 1) == 2) {
    if (g1) {
      unsigned xv[4], wv[4];
      stage_offsets(xv, wv);
#pragma unroll 1
      for (int i = 0; i < 3; ++i) { if (i == 0) STAMP(23); quarter(xv, wv, i >> 1, 2 * (w0 & 1) + (i & 1)); }
    }
  } else if (w0 < 4) {
    const unsigned kvo = (lane >> 3) * 1024 + (((lane & 7) ^ ((g & 1) << 2)) << 4);
    const char* kb = kk + (r * 64 + w0 * 16) * 1024 + xcd * 128;
    const char* vb = vv + (r * 64 + w0 * 16) * 1024 + xcd * 128;
    asm volatile("s_mov_b32 m0, %6\n\ts_nop 0\n\tglobal_load_lds_dwordx4 %0, %3\n\tglobal_load_lds_dwordx4 %0, %4 offset:1024\n\t"
                 "global_load_lds_dwordx4 %1, %4 offset:2048\n\tglobal_load_lds_dwordx4 %0, %5 offset:3072\n\ts_add_u32 m0, m0, 4096\n\ts_nop 0\n\t"
                 "global_load_lds_dwordx4 %1, %8\n\tglobal_load_lds_dwordx4 %2, %7 offset:1024"
                 ::"v"(kvo), "v"(kvo + 7168), "v"((lane & 31) * 16), "s"(r == 0 && w0 == 0 ? kb : kb - 8192), "s"(kb - 1024), "s"(vb - 3072),
                   "s"(2 * SLOT + w0 * 6144), "s"(shk + xcd * 512 - 1024), "s"(vb + 1024)
                 : "memory", "scc");
  } else if (w0 == 7 && lane < 8) {
    unsigned len;
    asm volatile("s_mov_b32 %0, .Lh3_end-.Lh3_begin" : "=s"(len));
    if ((unsigned)(r * 8 + lane) * 128 < len)
      asm volatile("s_mov_b32 m0, %2\n\ts_nop 0\n\tglobal_load_lds_dword %0, %1" ::"v"((r * 8 + lane) * 128), "s"(kbase), "s"(L_RS) : "memory");
  }
  STAMP(21);
  int w_ = w0;
  asm volatile("" : "+s"(w_));
  const int w = w_, gw = w & 3, tid = w * 64 + lane, t8 = tid & 255;
  auto wslot = [](int i) -> unsigned { const int s = i & 7; return s < 2 ? 2 * SLOT + s * 4096 : s < 5 ? (s - 2) * 4096 : SLOT + (s - 5) * 4096; };
  auto wdstage = [&](unsigned dv0, int kq, int i) {
    glds4(dv0, dv0 + 7168, dv0 + 2 * 7168, dv0 + 3 * 7168, wd + ct2 * 128 + (kq * 14 + i) * 32768, wslot(i) + kq * ((i & 7) < 2 ? 8192 : 12288));
  };
  u64 tag;
  auto tag_get = [&]() {
    const u2 t = *(const volatile LAS u2*)(size_t)L_TAG;
    tag = ((u64)(unsigned)__builtin_amdgcn_readfirstlane(t[1]) << 32) | (unsigned)__builtin_amdgcn_readfirstlane(t[0]);
  };

  u4 xq; u2 st2;
  auto tail_load = [&]() {
    int ln = __builtin_amdgcn_mbcnt_hi(-1, __builtin_amdgcn_mbcnt_lo(-1, 0));
    OPAQUE(ln);
    const int tk = w * 8 + (ln >> 3), c8 = ln & 7;
    xq = *(const GAS u4*)(q + (rt * 64 + tk) * 1024 + ct2 * 128 + c8 * 16);
    st2 = __builtin_amdgcn_raw_buffer_load_b64(wsr, WS_ROW + (rt * 64 + tk) * 8, 0, 16);
  };

  if (w < 4) {
    {
      const unsigned lk = 2 * SLOT + gw * 6144;
      const bool top = r == 0 && gw == 0;
      const int qq = l15 >> 2, pp = lane & 3;
      unsigned ra = lk + qq * 128 + (((2 * g + (pp >> 1)) ^ ((qq >> 1) << 2)) << 4) + (pp & 1) * 8;
      OPAQUE(ra);
      WAITV(0);
      STAMP(18);
      const u2 sw = *(const LAS u2*)(size_t)(lk + 5120 + lane * 8);
      u2 kr[5], vr[4];
#pragma unroll
      for (int i = 0; i < 5; ++i) kr[i] = __builtin_bit_cast(u2, __builtin_amdgcn_ds_read_tr16_b64_v4i16((LAS s4*)(size_t)(ra + 512 + i * 512)));
#pragma unroll
      for (int i = 0; i < 4; ++i) vr[i] = __builtin_bit_cast(u2, __builtin_amdgcn_ds_read_tr16_b64_v4i16((LAS s4*)(size_t)(ra + 3072 + i * 512)));
      if (top) kr[0] = (u2){0, 0};
      const float w0 = bflo(sw[0]), w1 = bfhi(sw[0]), w2 = bflo(sw[1]), w3 = bfhi(sw[1]);
      float kf[20], u[16];
#pragma unroll
      for (int i = 0; i < 5; ++i) { kf[4 * i] = bflo(kr[i][0]); kf[4 * i + 1] = bfhi(kr[i][0]); kf[4 * i + 2] = bflo(kr[i][1]); kf[4 * i + 3] = bfhi(kr[i][1]); }
      SB();
#pragma unroll
      for (int i = 0; i < 16; ++i) u[i] = fmul(kf[i + 1], w3);
      SB();
#pragma unroll
      for (int i = 0; i < 16; ++i) u[i] = ffma(kf[i + 2], w2, u[i]);
      SB();
#pragma unroll
      for (int i = 0; i < 16; ++i) u[i] = ffma(kf[i + 3], w1, u[i]);
      SB();
#pragma unroll
      for (int i = 0; i < 16; ++i) u[i] = ffma(kf[i + 4], w0, u[i]);
      SB();
#pragma unroll
      for (int i = 0; i < 16; ++i) { const unsigned vw = vr[i >> 2][(i >> 1) & 1]; u[i] = fmul(u[i], (i & 1) ? bfhi(vw) : bflo(vw)); }
      SB();
      const unsigned ad = L_MISC + lane * 128;
      sts128(ad + (((7 - 2 * gw) ^ (lane & 7)) << 4), (u4){pk2(u[7], u[6]), pk2(u[5], u[4]), pk2(u[3], u[2]), pk2(u[1], u[0])});
      sts128(ad + (((6 - 2 * gw) ^ (lane & 7)) << 4), (u4){pk2(u[15], u[14]), pk2(u[13], u[12]), pk2(u[11], u[10]), pk2(u[9], u[8])});
      STAMP(1);
      BAR();
    }
    if (g1) {
      const int qq = l15 >> 2, pp = lane & 3;
      const int wm = w >> 1, wn = w & 1;
      f4 acc[2][4][4];
#pragma unroll
      for (int s = 0; s < 2; ++s)
#pragma unroll
        for (int e = 0; e < 4; ++e)
#pragma unroll
          for (int i = 0; i < 4; ++i) acc[s][e][i] = (f4){0.f, 0.f, 0.f, 0.f};
      unsigned xa[2], wa[4];
      xa[0] = (wm * 64 + l15) * 128 + ((g ^ (l15 >> 1)) << 4); xa[1] = xa[0] ^ 64;
#pragma unroll
      for (int e = 0; e < 4; ++e) wa[e] = (8 * g + qq) * 256 + (((4 * wn + e) ^ (qq | ((g & 1) << 2))) << 5) + pp * 8;
      Frag f0, f1;
      s4 lo_;
      auto ldpart = [&](Frag& f, int ks, int j) {
        const unsigned o = 16384 + ks * 8192;
        if (j == 0) f.x[0] = lds128(xa[ks]);
        if (j >= 1 && j <= 16) {
          const int s = (j - 1) >> 3, e = ((j - 1) >> 1) & 3;
          if (j & 1) lo_ = __builtin_amdgcn_ds_read_tr16_b64_v4i16((LAS s4*)(size_t)(wa[e] + o + s * 16384));
          else {
            const s4 hi = __builtin_amdgcn_ds_read_tr16_b64_v4i16((LAS s4*)(size_t)(wa[e] + o + s * 16384 + 1024));
            f.w[s][e] = __builtin_bit_cast(bf8, __builtin_shufflevector(lo_, hi, 0, 1, 2, 3, 4, 5, 6, 7));
          }
        }
        if (j >= 17 && j < 20) f.x[j - 16] = lds128(xa[ks] + (j - 16) * 2048);
      };
      auto mm1 = [&](const Frag& f, int j) { acc[(j >> 2) & 1][j & 3][j >> 3] = mfma(f.w[(j >> 2) & 1][j & 3], f.x[j >> 3], acc[(j >> 2) & 1][j & 3][j >> 3]); };
      auto second = [&](int st) {
        const int d = (st == 2 || st == 5) ? -2 * (int)SLOT : (int)SLOT;
#pragma unroll
        for (int j = 0; j < 32; ++j) {
          if (j == BAR_AT) { BAR(); SB(); }
          mm1(f0, j);
          if (j < 20) ldpart(f1, 1, j);
          if (j == 20) { xa[0] += d; OPAQUE(xa[0]); xa[1] += d; OPAQUE(xa[1]); }
          if (j == 21) { wa[0] += d; OPAQUE(wa[0]); wa[1] += d; OPAQUE(wa[1]); }
          if (j == 22) { wa[2] += d; OPAQUE(wa[2]); wa[3] += d; OPAQUE(wa[3]); }
          SB();
        }
        if (BAR_AT == 32) { BAR(); SB(); }
      };
      OPAQUE(xa[0]); OPAQUE(xa[1]); OPAQUE(wa[0]); OPAQUE(wa[1]); OPAQUE(wa[2]); OPAQUE(wa[3]);
      STAMP(2);
#pragma unroll
      for (int j = 0; j < 20; ++j) ldpart(f0, 0, j);
      SB();
      second(0);
#pragma unroll 1
      for (int st = 1; st < 8; ++st) {
#pragma unroll
        for (int j = 0; j < 32; ++j) { mm1(f1, j); if (j < 20) ldpart(f0, 0, j); SB(); }
        second(st);
      }
      STAMP(16);
      constexpr float C0 = -2.0f * 0.7978845608028654f * 1.4426950408889634f, C1 = C0 * 0.044715f;
      f4 cs[2][4];
      f2 rs[4];
      int ln1 = __builtin_amdgcn_mbcnt_hi(-1, __builtin_amdgcn_mbcnt_lo(-1, 0));
      OPAQUE(ln1);
      const int lane = ln1, l15 = lane & 15, g = lane >> 4;
#pragma unroll
      for (int j = 0; j < 32; ++j) {
        mm1(f1, j);
        if (j < 8) cs[j >> 2][j & 3] = lds128f(L_CS + ((j >> 2) * 128 + wn * 64 + (j & 3) * 16 + 4 * g) * 4);
        if (j >= 8 && j < 12) rs[j - 8] = *(const LAS f2*)(size_t)(L_RS + (wm * 64 + (j - 8) * 16 + l15) * 8);
        SB();
      }
      STAMP(3);
      const unsigned hso = WS_H + ((pm * 8 + wm * 4) * 56 + pn * 4 + wn * 2) * 1024 + lane * 16;
#pragma unroll
      for (int i = 0; i < 4; ++i) {
        const float m = -rs[i][0], rr = rs[i][1];
        const float k0 = C0 * rr, k1 = C1 * rr * rr * rr, i2 = __builtin_amdgcn_rcpf(rr * rr);
        const f2 m2 = {m, m}, k02 = {k0, k0}, k12 = {k1, k1}, i22 = {i2, i2};
        f2 a[8], c[8], z[8];
        SB();
#pragma unroll
        for (int j = 0; j < 8; ++j) {
          const int e = j >> 1, h = (j & 1) * 2;
          a[j] = __builtin_elementwise_fma(m2, (f2){cs[0][e][h], cs[0][e][h + 1]}, (f2){acc[0][e][i][h], acc[0][e][i][h + 1]});
        }
        SB();
#pragma unroll
        for (int j = 0; j < 8; ++j) z[j] = a[j] * a[j];
        SB();
#pragma unroll
        for (int j = 0; j < 8; ++j) z[j] = __builtin_elementwise_fma(z[j], k12, k02);
        SB();
#pragma unroll
        for (int j = 0; j < 8; ++j) z[j] = z[j] * a[j];
        SB();
#pragma unroll
        for (int j = 0; j < 8; ++j) {
          const int e = j >> 1, h = (j & 1) * 2;
          z[j] = (f2){__builtin_amdgcn_exp2f(z[j][0]), __builtin_amdgcn_exp2f(z[j][1])};
          c[j] = __builtin_elementwise_fma(m2, (f2){cs[1][e][h], cs[1][e][h + 1]}, (f2){acc[1][e][i][h], acc[1][e][i][h + 1]});
          SB();
        }
#pragma unroll
        for (int j = 0; j < 8; ++j) z[j] = __builtin_elementwise_fma(z[j], i22, i22);
        SB();
#pragma unroll
        for (int j = 0; j < 8; ++j) {
          z[j] = (f2){__builtin_amdgcn_rcpf(z[j][0]), __builtin_amdgcn_rcpf(z[j][1])};
          c[j] = c[j] * a[j];
          SB();
        }
#pragma unroll
        for (int j = 0; j < 8; ++j) c[j] = c[j] * z[j];
        SB();
        u4 o0, o1;
#pragma unroll
        for (int j = 0; j < 4; ++j) { o0[j] = pk2(c[j][0], c[j][1]); o1[j] = pk2(c[4 + j][0], c[4 + j][1]); }
        __builtin_amdgcn_raw_buffer_store_b128(o0, wsr, hso + i * 56 * 1024, 0, 16);
        __builtin_amdgcn_raw_buffer_store_b128(o1, wsr, hso + i * 56 * 1024 + 1024, 0, 16);
      }
      SB();
      ctl_add(C_EPI);
      tag_get();
      STAMP(4);
    } else {
      const unsigned o = ((xcd * 4 + r - 28) * 448 + gw * 112 + lane) * 128;
      if (PF_W) asm volatile("s_mov_b32 m0, %3\n\ts_nop 0\n\tglobal_load_lds_dword %0, %2\n\tglobal_load_lds_dword %1, %2"
                             ::"v"(o), "v"(o + (lane < 48 ? 8192 : 0)), "s"(wd), "s"(131072 + gw * 256) : "memory");
      if (PF_L2) {
#pragma unroll 1
        for (int j = (r - 28) * 4 + gw; j < PF_L2 * 16; j += 16) {
          const unsigned o2 = ((((j >> 2) & 3) * 14 + (j >> 4)) * 32 + (j & 3) * 8 + (lane >> 3)) * 1024 + (lane & 7) * 128;
          asm volatile("s_waitcnt vmcnt(0)\n\ts_mov_b32 m0, %2\n\ts_nop 0\n\tglobal_load_lds_dword %0, %1" ::"v"(o2), "s"(wd), "s"(131072 + gw * 256) : "memory");
        }
      }
      WAITV(0);
    }
    int ln2 = __builtin_amdgcn_mbcnt_hi(-1, __builtin_amdgcn_mbcnt_lo(-1, 0));
    OPAQUE(ln2);
    {
      const int lane = ln2, l15 = lane & 15, g = lane >> 4, qq = l15 >> 2, pp = lane & 3;
      const unsigned dv0 = (lane >> 3) * 1024 + (((lane & 7) ^ (g << 1)) * 16);
      f4 acc[4][4];
#pragma unroll
      for (int t = 0; t < 4; ++t)
#pragma unroll
        for (int i = 0; i < 4; ++i) acc[t][i] = (f4){0.f, 0.f, 0.f, 0.f};
      const unsigned da = (4 * g + qq) * 128 + (((2 * g + (qq >> 1)) & 3) << 5) + pp * 8;
      unsigned dlo[4];
#pragma unroll
      for (int t = 0; t < 4; ++t) { dlo[t] = (da ^ (t * 32)) + gw * 12288; OPAQUE(dlo[t]); }
      const unsigned dh = 2 * SLOT - gw * 4096;
      constexpr int WR = 6, HW = HWIN;
      bf8 wf[WR][4];
      auto waddr = [&](int i, int t) -> unsigned {
        if ((i & 7) >= 2) return dlo[t] + wslot(i);
        unsigned d_ = dh;
        asm volatile("" : "+s"(d_));
        unsigned a_ = dlo[t] + d_;
        OPAQUE(a_);
        return a_ + (i & 1) * 4096;
      };
      __builtin_amdgcn_s_setprio(APRIO);
      auto publish = [&]() {
        if (g1) {
          WAITV(0);
          if (lane == 0 && __hip_atomic_fetch_add((LAS unsigned*)(size_t)C_HPUB, 1u, __ATOMIC_RELAXED, __HIP_MEMORY_SCOPE_WORKGROUP) == 3)
            flag_set(ws, WS_FH + (pm * 16 + pn) * 128, tag);
        }
        STAMP(5);
      };
      if (!PRE_FIRST) publish();
      ctl_wait(C_WD + (gw >> 1) * 4, 2);
      if (!g1) tag_get();
#pragma unroll
      for (int t = 0; t < WR * 4; ++t) { const unsigned a_ = waddr(t >> 2, t & 3); wf[t >> 2][t & 3] = ldtr(a_, a_ + 2048); }
      SB();
      if (PRE_FIRST) publish();
      WAITL(0);
      STAMP(17);
#pragma unroll
      for (int i = 8; i < 14; ++i) wdstage(dv0, gw, i);
      __builtin_amdgcn_s_setprio(0);
      STAMP(9);
      const unsigned fh = WS_FH + ((rt >> 1) * 16 + ((gw * 7) >> 1) + (lane & 3)) * 128;
      unsigned have = 0;
      auto need = [&](int i) {
        const unsigned m = PROGR ? 1u << (((gw & 1) * 2 + i) >> 2) : 15u;
        u64 x_;
        unsigned t_, n_;
        asm volatile("s_and_b32 %2, %0, %4\n\ts_cmp_eq_u32 %2, %4\n\ts_cbranch_scc1 .Ldone%=\n\ts_mov_b32 %3, 0x40000\n"
                     ".Lpoll%=:\n\tglobal_load_dwordx2 %1, %5, %6 sc1\n\ts_waitcnt vmcnt(0)\n\tv_cmp_eq_u64 vcc, %7, %1\n\ts_nop 4\n\t"
                     "s_and_b32 %0, vcc_lo, 15\n\ts_and_b32 %2, %0, %4\n\ts_cmp_eq_u32 %2, %4\n\ts_cbranch_scc1 .Ldone%=\n\ts_sleep 2\n\t"
                     "s_sub_u32 %3, %3, 1\n\ts_cmp_lg_u32 %3, 0\n\ts_cbranch_scc1 .Lpoll%=\n.Ldone%=:"
                     : "+s"(have), "=&v"(x_), "=&s"(t_), "=&s"(n_) : "s"(m), "v"(fh), "s"(ws), "s"(tag) : "memory", "vcc", "scc");
        SB();
      };
      need(0);
      STAMP(6);
      u4 hf[HW][4];
      unsigned ho[4];
#pragma unroll
      for (int t = 0; t < 4; ++t) ho[t] = WS_H + ((rt * 4 + t) * 56 + gw * 14) * 1024 + lane * 16;
      auto ldh1 = [&](int i, int t) { hf[i % HW][t] = __builtin_amdgcn_raw_buffer_load_b128(wsr, ho[t] + (i & 3) * 1024, (i >> 2) * 4096, 16); };
#pragma unroll
      for (int i = 0; i < HW * 4; ++i) { if (i == 8) need(2); ldh1(i >> 2, i & 3); SB(); }
      s4 lo_;
      unsigned wa_;
#pragma unroll
      for (int i = 0; i < 14; ++i) {
        const int nw = i + WR - 1;
        if (i == 11) { tail_load(); SB(); }
        if (i + HW < 14 && ((i + HW) & 1) == 0) need(i + HW);
#pragma unroll
        for (int j = 0; j < 16; ++j) {
          const int ti = j >> 2, t = j & 3;
          acc[t][ti] = mfma(wf[i % WR][t], __builtin_bit_cast(bf8, hf[i % HW][ti]), acc[t][ti]);
          if (i >= 1 && nw < 14 && t != 3 && (j - ti) < 8) {
            const int e = j - ti;
            if (!(e & 1)) wa_ = waddr(nw, e >> 1);
            const s4 v_ = __builtin_amdgcn_ds_read_tr16_b64_v4i16((LAS s4*)(size_t)(wa_ + (e & 1) * 2048));
            if (e & 1) wf[nw % WR][e >> 1] = __builtin_bit_cast(bf8, __builtin_shufflevector(lo_, v_, 0, 1, 2, 3, 4, 5, 6, 7)); else lo_ = v_;
          }
          if (t == 3 && i + HW < 14) ldh1(i + HW, ti);
          SB();
        }
        if (i == 0) STAMP(19);
      }
      STAMP(7);
#pragma unroll
      for (int ti = 0; ti < 4; ++ti)
#pragma unroll
        for (int t = 0; t < 4; ++t)
          sts128f((ti >> 1) * SLOT + gw * 12288 + ((ti & 1) * 16 + l15) * 256 + (((4 * t + g) ^ (l15 & 7)) << 4), acc[t][ti]);
    }
    STAMP(8);
  } else {
    const int t7 = tid & 127, bw = w & 1;
    unsigned dcr;
    if (w < 6) {
      unsigned xv[4], wv[4];
      stage_offsets(xv, wv);
      auto stage = [&](int st) { quarter(xv, wv, st, 2 * bw); quarter(xv, wv, st, 2 * bw + 1); };
      const unsigned dv0 = (lane >> 3) * 1024 + (((lane & 7) ^ (g << 1)) * 16);
      WAITV(12);
      STAMP(1);
      BAR();
      if (g1) { quarter(xv, wv, 1, 2 * bw + 1); stage(2); WAITV(24); }
      KBAR();
#pragma unroll 1
      for (int st = 3; st < 8; ++st) {
        if (g1) { stage(st); WAITV(24); }
        KBAR();
      }
      STAMP(13);
      if (!g1) ctl_wait(C_TAG, 2);
      asm volatile("global_load_dword %0, %1, %2" : "=&v"(dcr) : "v"(0u), "s"(sD + b * 4) : "memory");
#pragma unroll
      for (int i = 0; i < 4; ++i) wdstage(dv0, bw * 2 + (i & 1), i >> 1);
      WAITV(17);
      KBAR();
      STAMP(3);
#pragma unroll
      for (int i = 0; i < 6; ++i) wdstage(dv0, bw * 2 + (i & 1), 2 + (i >> 1));
      KBAR();
      STAMP(19);
      tag_get();
      asm volatile("s_waitcnt vmcnt(32)" : "+v"(dcr)::"memory");
#pragma unroll
      for (int i = 0; i < 6; ++i) wdstage(dv0, bw * 2 + (i & 1), 5 + (i >> 1));
      WAITV(24);
      ctl_add(C_WD + bw * 4);
      STAMP(2);
      WAITV(0);
      ctl_add(C_WD + bw * 4);
      STAMP(4);
    } else {
      if (t7 < 48) *(LAS unsigned*)(size_t)(L_CTL + t7 * 4) = 0;
      const int ptt = b >> 3, pct = b & 7;
      const int rgrp = (xcd >> 1) * 64 + (xcd & 1) * 32 + r;
      const int cst = 2 * ((r + pnl) & 3) + ((xcd >> 1) & 1);
      const unsigned hoff = (2 * b + bw) * 4096 + lane * 16;
      u4 xr[4];
      auto rows_load = [&]() {
        asm volatile("global_load_dwordx4 %0, %4, %5\n\tglobal_load_dwordx4 %1, %4, %5 offset:1024\n\tglobal_load_dwordx4 %2, %4, %5 offset:2048\n\t"
                     "global_load_dwordx4 %3, %4, %5 offset:3072"
                     : "=&v"(xr[0]), "=&v"(xr[1]), "=&v"(xr[2]), "=&v"(xr[3]) : "v"(lane * 16), "s"(q + (rgrp * 8 + (w & 1) * 4) * 1024) : "memory");
      };
      auto rows_stat = [&]() {
        asm volatile("" : "+v"(xr[0]), "+v"(xr[1]), "+v"(xr[2]), "+v"(xr[3])::"memory");
        float e[4][8], mu[4], va[4];
#pragma unroll
        for (int i = 0; i < 4; ++i) {
#pragma unroll
          for (int j = 0; j < 4; ++j) { e[i][2 * j] = bflo(xr[i][j]); e[i][2 * j + 1] = bfhi(xr[i][j]); }
          mu[i] = fadd(fadd(fadd(e[i][0], e[i][1]), fadd(e[i][2], e[i][3])), fadd(fadd(e[i][4], e[i][5]), fadd(e[i][6], e[i][7])));
        }
        auto fold4 = [&](const float (&v)[4]) {
          const auto s0 = __builtin_amdgcn_permlane16_swap(__float_as_uint(v[0]), __float_as_uint(v[1]), false, false);
          const auto s1 = __builtin_amdgcn_permlane16_swap(__float_as_uint(v[2]), __float_as_uint(v[3]), false, false);
          const float p0 = fadd(__uint_as_float(s0[0]), __uint_as_float(s0[1])), p1 = fadd(__uint_as_float(s1[0]), __uint_as_float(s1[1]));
          const auto s2 = __builtin_amdgcn_permlane32_swap(__float_as_uint(p0), __float_as_uint(p1), false, false);
          float t = fadd(__uint_as_float(s2[0]), __uint_as_float(s2[1]));
          t = dpp_add<0xB1>(t); t = dpp_add<0x4E>(t); t = dpp_add<0x141>(t); t = dpp_add<0x140>(t);
          return t * (1.0f / 512.0f);
        };
        const float mg = fold4(mu);
#pragma unroll
        for (int i = 0; i < 4; ++i) {
          const float m = __int_as_float(__builtin_amdgcn_readlane(__float_as_int(mg), 16 * i));
          float s = 0.f;
#pragma unroll
          for (int j = 0; j < 8; ++j) { const float d = e[i][j] - m; s = ffma(d, d, s); }
          va[i] = s;
        }
        const float vg = fold4(va);
        const u2 o = {__float_as_uint(rbf(mg)), __float_as_uint(__builtin_amdgcn_rcpf(rbf(__builtin_amdgcn_sqrtf(rbf(rbf(vg) + 1e-5f)))))};
        if (l15 == 0) __builtin_amdgcn_raw_buffer_store_b64(o, wsr, WS_ROW + (rgrp * 8 + (w & 1) * 4 + g) * 8, 0, 16);
        asm volatile("" ::: "memory");
      };

      u4 wsh[8], hc[4], uc[4], c1[4]; unsigned hp[4]; u2 ep;
      {
        const char* wb = ((xcd >> 2) ? wu : wg) + (g1 ? pn * 256 + (cst * 64 + bw * 32) * 3584 : 0);
        const unsigned o = g * 3584 + l15 * 16;
        if (SIDE_SLEEP) __builtin_amdgcn_s_sleep(SIDE_SLEEP);
        asm volatile("global_load_dwordx4 %0, %8, %12\n\tglobal_load_dwordx4 %1, %9, %12\n\tglobal_load_dwordx4 %2, %10, %12\n\tglobal_load_dwordx4 %3, %11, %12\n\t"
                     "global_load_dwordx4 %4, %8, %13\n\tglobal_load_dwordx4 %5, %9, %13\n\tglobal_load_dwordx4 %6, %10, %13\n\tglobal_load_dwordx4 %7, %11, %13"
                     : "=&v"(wsh[0]), "=&v"(wsh[1]), "=&v"(wsh[2]), "=&v"(wsh[3]), "=&v"(wsh[4]), "=&v"(wsh[5]), "=&v"(wsh[6]), "=&v"(wsh[7])
                     : "v"(o), "v"(o + 4 * 3584), "v"(o + 8 * 3584), "v"(o + 12 * 3584), "s"(wb), "s"(wb + 16 * 3584) : "memory");
        asm volatile("global_load_dwordx2 %0, %1, %2 sc1" : "=&v"(ep) : "v"(WS_EPOCH), "s"(ws) : "memory");
        rows_load();
      }
      BAR();
#pragma unroll
      for (int z = 0; z < 4; ++z) {
        const int id = t7 + 128 * z, c2 = id >> 3, ch = id & 7;
        const u4 t = lds128u(L_MISC + c2 * 128 + ((ch ^ (c2 & 7)) << 4));
        __builtin_amdgcn_raw_buffer_store_b128(t, wsr, WS_KVT + (pct * 64 + c2) * 4096 + 3968 - ptt * 128 + ch * 16, 0, 16);
      }
      asm volatile("s_waitcnt vmcnt(4)" : "+v"(wsh[0]), "+v"(wsh[1]), "+v"(wsh[2]), "+v"(wsh[3]), "+v"(wsh[4]), "+v"(wsh[5]), "+v"(wsh[6]), "+v"(wsh[7]), "+v"(ep)::"memory");
      tag = (((u64)(unsigned)__builtin_amdgcn_readfirstlane(ep[1]) << 32) | (unsigned)__builtin_amdgcn_readfirstlane(ep[0])) + 1;
      *(LAS u64*)(size_t)L_TAG = tag;
      STAMP(17);
      if (g1) {
        float cacc[8];
#pragma unroll
        for (int j = 0; j < 4; ++j) {
          cacc[2 * j] = fadd(fadd(fadd(bflo(wsh[0][j]), bflo(wsh[1][j])), fadd(bflo(wsh[2][j]), bflo(wsh[3][j]))),
                             fadd(fadd(bflo(wsh[4][j]), bflo(wsh[5][j])), fadd(bflo(wsh[6][j]), bflo(wsh[7][j]))));
          cacc[2 * j + 1] = fadd(fadd(fadd(bfhi(wsh[0][j]), bfhi(wsh[1][j])), fadd(bfhi(wsh[2][j]), bfhi(wsh[3][j]))),
                                 fadd(fadd(bfhi(wsh[4][j]), bfhi(wsh[5][j])), fadd(bfhi(wsh[6][j]), bfhi(wsh[7][j]))));
        }
        float c4[4], c2[2];
#pragma unroll
        for (int j = 0; j < 4; ++j) {
          const auto s2 = __builtin_amdgcn_permlane16_swap(__float_as_uint(cacc[j]), __float_as_uint(cacc[4 + j]), false, false);
          c4[j] = fadd(__uint_as_float(s2[0]), __uint_as_float(s2[1]));
        }
#pragma unroll
        for (int j = 0; j < 2; ++j) {
          const auto s2 = __builtin_amdgcn_permlane32_swap(__float_as_uint(c4[j]), __float_as_uint(c4[2 + j]), false, false);
          c2[j] = fadd(__uint_as_float(s2[0]), __uint_as_float(s2[1]));
        }
        __builtin_amdgcn_raw_buffer_store_b64((u2){__float_as_uint(c2[0]), __float_as_uint(c2[1])}, wsr,
                                              WS_COL + ((pn * 16 + cst * 2 + bw) * 256 + (xcd >> 2) * 128 + l15 * 8 + (g & 1) * 4 + (g >> 1) * 2) * 4, 0, 16);
      }
      STAMP(10);
      KBAR();
      rows_stat();
      STAMP(18);
      asm volatile("global_load_dwordx4 %0, %9, %11\n\tglobal_load_dwordx4 %1, %9, %11 offset:1024\n\tglobal_load_dwordx4 %2, %9, %11 offset:2048\n\t"
                   "global_load_dwordx4 %3, %9, %11 offset:3072\n\tglobal_load_dword %4, %10, %11\n\tglobal_load_dword %5, %9, %11 offset:1020\n\t"
                   "global_load_dword %6, %9, %11 offset:2044\n\tglobal_load_dword %7, %9, %11 offset:3068\n\tglobal_load_dword %8, %12, %13"
                   : "=&v"(hc[0]), "=&v"(hc[1]), "=&v"(hc[2]), "=&v"(hc[3]), "=&v"(hp[0]), "=&v"(hp[1]), "=&v"(hp[2]), "=&v"(hp[3]), "=&v"(dcr)
                   : "v"(hoff), "v"(lane ? hoff - 4 : hoff), "s"(ssk), "v"(0u), "s"(sD + b * 4) : "memory");
      KBAR();
      WAITV(9);
      KBAR();
      if (!g1) { ctl_add(C_WSYNC); ctl_spin(C_WSYNC, 2); }
      if (tid == 384 || tid == 385) flag_set(ws, tid == 384 ? WS_FS + rgrp * 128 : WS_FK + (pct * 32 + ptt) * 128, tag);
      STAMP(20);
      unsigned fo = lane < 32 || !g1 ? WS_FK + ((b >> 5) * 32 + (lane & 31)) * 128
                                     : WS_FS + (lane < 48 ? pm * 16 + l15 : (l15 >> 2) * 64 + (xcd & 1) * 32 + (l15 & 3) + 4 * pnl) * 128;
      unsigned fo2 = WS_FS + (rt * 8 + (lane & 7)) * 128;
      OPAQUE(fo); OPAQUE(fo2);
      KBAR();
      asm volatile("s_waitcnt vmcnt(0)" : "+v"(hc[0]), "+v"(hc[1]), "+v"(hc[2]), "+v"(hc[3]), "+v"(hp[0]), "+v"(hp[1]), "+v"(hp[2]), "+v"(hp[3]), "+v"(dcr)::"memory");
      if (lane == 0) hp[0] = 0;
#pragma unroll
      for (int z = 0; z < 4; ++z) {
        c1[z] = (u4){__builtin_amdgcn_alignbit(hc[z][0], hp[z], 16), __builtin_amdgcn_alignbit(hc[z][1], hc[z][0], 16),
                     __builtin_amdgcn_alignbit(hc[z][2], hc[z][1], 16), __builtin_amdgcn_alignbit(hc[z][3], hc[z][2], 16)};
        OPAQUE(c1[z]);
      }
      u64 p0, p1;
      if (!g1) __builtin_amdgcn_s_sleep(70);
      if (PEEK_AT == 5) KBAR();
      asm volatile("global_load_dwordx2 %0, %2, %4 sc1\n\tglobal_load_dwordx2 %1, %3, %4 sc1" : "=&v"(p0), "=&v"(p1) : "v"(fo), "v"(fo2), "s"(ws) : "memory");
      KBAR();
      asm volatile("s_waitcnt vmcnt(0)" : "+v"(p0), "+v"(p1)::"memory");
      if (!__all(p0 == tag && p1 == tag)) poll2(ws, fo, fo2, tag);
      if (!g1) ctl_add(C_TAG);
      u2 rsv, csv[16];
      {
        const unsigned co = WS_COL + (g1 ? pn : 0) * 16384 + t7 * 8;
#define LD4(o, vreg) asm volatile("global_load_dwordx2 %0, %4, %5 sc1\n\tglobal_load_dwordx2 %1, %4, %5 offset:1024 sc1\n\t" \
                                  "global_load_dwordx2 %2, %4, %5 offset:2048 sc1\n\tglobal_load_dwordx2 %3, %4, %5 offset:3072 sc1" \
                                  : "=&v"(csv[o]), "=&v"(csv[o + 1]), "=&v"(csv[o + 2]), "=&v"(csv[o + 3]) : "v"(vreg), "s"(ws) : "memory")
        LD4(0, co); LD4(4, co + 4096); LD4(8, co + 8192); LD4(12, co + 12288);
        asm volatile("global_load_dwordx2 %0, %1, %2 sc1" : "=&v"(rsv) : "v"(WS_ROW + (g1 ? pm : 0) * 1024 + t7 * 8), "s"(ws) : "memory");
      }
      if (PEEK_AT == 4) KBAR();
      STAMP(14);
      KBAR();
      asm volatile("global_load_dwordx4 %0, %4, %5 sc1\n\tglobal_load_dwordx4 %1, %4, %5 offset:1024 sc1\n\tglobal_load_dwordx4 %2, %4, %5 offset:2048 sc1\n\t"
                   "global_load_dwordx4 %3, %4, %5 offset:3072 sc1" : "=&v"(uc[0]), "=&v"(uc[1]), "=&v"(uc[2]), "=&v"(uc[3]) : "v"(WS_KVT + hoff), "s"(ws) : "memory");
      const unsigned base = L_CV + bw * CV_CH;
#pragma unroll
      for (int z = 0; z < 4; ++z) {
        sts128(base + 32 + lane * 16 + z * 1024, hc[z]);
        sts128(base + 32 + lane * 16 + z * 1024 + CV_COPY, c1[z]);
      }
      if (lane < 4) sts128(base + (lane >> 1) * CV_COPY + (lane & 1) * 16, (u4){0, 0, 0, 0});
      if (lane >= 32) sts128(base + CV_UR + 4096 + (lane - 32) * 16, (u4){0, 0, 0, 0});
      asm volatile("s_waitcnt vmcnt(4)" : "+v"(rsv), "+v"(csv[0]), "+v"(csv[1]), "+v"(csv[2]), "+v"(csv[3]), "+v"(csv[4]), "+v"(csv[5]), "+v"(csv[6]), "+v"(csv[7]),
                   "+v"(csv[8]), "+v"(csv[9]), "+v"(csv[10]), "+v"(csv[11]), "+v"(csv[12]), "+v"(csv[13]), "+v"(csv[14]), "+v"(csv[15])::"memory");
      {
        *(LAS u2*)(size_t)(L_RS + t7 * 8) = rsv;
        float s0[4], s1[4];
#pragma unroll
        for (int i = 0; i < 4; ++i) {
          s0[i] = fadd(fadd(__uint_as_float(csv[i][0]), __uint_as_float(csv[4 + i][0])), fadd(__uint_as_float(csv[8 + i][0]), __uint_as_float(csv[12 + i][0])));
          s1[i] = fadd(fadd(__uint_as_float(csv[i][1]), __uint_as_float(csv[4 + i][1])), fadd(__uint_as_float(csv[8 + i][1]), __uint_as_float(csv[12 + i][1])));
        }
        *(LAS u2*)(size_t)(L_CS + t7 * 8) = (u2){__float_as_uint(fadd(fadd(s0[0], s0[1]), fadd(s0[2], s0[3]))), __float_as_uint(fadd(fadd(s1[0], s1[1]), fadd(s1[2], s1[3])))};
      }
      STAMP(16);
      KBAR();
      if (b == 255 && bw == 1) {
#pragma unroll 1
        for (int z = 0; z < 4; ++z) poll(ws, WS_FS + (z * 64 + lane) * 128, tag);
        if (lane == 0) flag_set(ws, WS_EPOCH, tag);
      }
      asm volatile("s_waitcnt vmcnt(0)" : "+v"(uc[0]), "+v"(uc[1]), "+v"(uc[2]), "+v"(uc[3])::"memory");
#pragma unroll
      for (int z = 0; z < 4; ++z) sts128(base + CV_UR + lane * 16 + z * 1024, uc[z]);
      ctl_add(C_CVIN);
      STAMP(15);
    }
    ctl_wait(C_CVIN, 2);
    const int ch = gw >> 1, half = gw & 1;
    const unsigned base = L_CV + ch * CV_CH;
    const unsigned o = 8 * g + l15 + 1, kap = o & 1;
    unsigned aa = base + kap * CV_COPY + 2 * (o + kap) + half * 256;
    unsigned ba = base + CV_UR + 2 * (2032 - 16 * l15 + 8 * g) - 3584 + half * 256;
    unsigned ab = aa + 8;
    OPAQUE(aa); OPAQUE(ab); OPAQUE(ba);
    f4 y[8];
#pragma unroll
    for (int n = 0; n < 8; ++n) y[n] = (f4){0.f, 0.f, 0.f, 0.f};
    bf8 A[2][8], B[2][8];
    auto ldab = [&](int rho, int i) {
      const u2a lo = *(const LAS u2a*)(size_t)(aa + rho * 64 + i * 512), hi = *(const LAS u2a*)(size_t)(ab + rho * 64 + i * 512);
      A[rho & 1][i] = __builtin_bit_cast(bf8, (u4){lo[0], lo[1], hi[0], hi[1]});
      B[rho & 1][i] = lds128(ba + rho * 64 + (7 - i) * 512);
    };
#pragma unroll
    for (int i = 0; i < 8; ++i) ldab(0, i);
    SB();
    if (g1) ctl_spin(C_EPI, 4);
    STAMP(5);
    __builtin_amdgcn_s_setprio(CPRIO);
#pragma unroll
    for (int rho = 0; rho < 4; ++rho) {
      int cnt = 0;
#pragma unroll
      for (int lv = 0; lv < 8; ++lv)
#pragma unroll
        for (int qi = 0; qi <= lv; ++qi)
#pragma unroll
          for (int p = 0; p <= lv; ++p)
            if ((qi == lv || p == lv) && qi + p < 8) {
              y[qi + p] = mfma(A[rho & 1][qi], B[rho & 1][p], y[qi + p]);
              if (rho < 3 && (cnt & 3) == 0 && (cnt >> 2) < 8) ldab(rho + 1, cnt >> 2);
              ++cnt;
              SB();
            }
    }
    STAMP(6);
    ctl_add(C_PAIR + ch * 4); ctl_spin(C_PAIR + ch * 4, 2);
    const unsigned ex0 = base + half * 4096, ex1 = base + (half ^ 1) * 4096;
#pragma unroll
    for (int n = 0; n < 4; ++n) {
      if (half) { const f4 t = y[n]; y[n] = y[4 + n]; y[4 + n] = t; }
      sts128f(ex0 + n * 1024 + lane * 16, y[4 + n]);
    }
    u2 uu[4];
#pragma unroll
    for (int n = 0; n < 4; ++n) uu[n] = *(const LAS u2*)(size_t)(base + CV_UR + 2 * (2044 - (256 * (4 * half + n) + 16 * l15 + 4 * g)));
    ctl_add(C_PAIR + ch * 4);
    const float dc = ch ? bfhi(dcr) : bflo(dcr);
#pragma unroll
    for (int n = 0; n < 4; ++n) {
      y[n][0] = ffma(bfhi(uu[n][1]), dc, y[n][0]); y[n][1] = ffma(bflo(uu[n][1]), dc, y[n][1]);
      y[n][2] = ffma(bfhi(uu[n][0]), dc, y[n][2]); y[n][3] = ffma(bflo(uu[n][0]), dc, y[n][3]);
    }
    ctl_spin(C_PAIR + ch * 4, 4);
    STAMP(11);
    u2 pk[4];
#pragma unroll
    for (int n = 0; n < 4; ++n) {
      const f4 t = lds128f(ex1 + n * 1024 + lane * 16);
      pk[n] = (u2){pk2(fadd(t[0], y[n][0]), fadd(t[1], y[n][1])), pk2(fadd(t[2], y[n][2]), fadd(t[3], y[n][3]))};
    }
#pragma unroll
    for (int n = 0; n < 4; n += 2) {
      const auto s0 = __builtin_amdgcn_permlane16_swap(pk[n][0], pk[n + 1][0], false, false);
      const auto s1 = __builtin_amdgcn_permlane16_swap(pk[n][1], pk[n + 1][1], false, false);
      const u4 o4 = {s0[0], s1[0], s0[1], s1[1]};
      __builtin_amdgcn_raw_buffer_store_b128(o4, wsr, WS_YT + (2 * b + ch) * 4096 + 2 * (256 * (4 * half + n + (g & 1)) + 16 * l15 + 8 * (g >> 1)), 0, 16);
    }
    STAMP(22);
    WAITV(0);
    ctl_add(C_BSYNC);
    ctl_spin(C_BSYNC, 4);
    if (tid == 256) flag_set(ws, WS_FY + b * 128, tag);
    STAMP(7);
    tail_load();
    if (gw == 3) {
      poll(ws, WS_FY + (ct2 * 32 + (lane & 31)) * 128, tag);
      ctl_add(C_GO);
    } else ctl_wait(C_GO, 1);
    STAMP(8);
    u4 yq[2];
#pragma unroll
    for (int z = 0; z < 2; ++z) {
      const int id = t8 + 256 * z;
      yq[z] = __builtin_amdgcn_raw_buffer_load_b128(wsr, WS_YT + (ct2 * 64 + (id >> 3)) * 4096 + rt * 128 + (id & 7) * 16, 0, 16);
    }
#pragma unroll
    for (int z = 0; z < 2; ++z) {
      const int id = t8 + 256 * z, c = id >> 3;
      sts128(L_YI + c * 128 + (((id & 7) ^ (c >> 3)) << 4), yq[z]);
    }
    __builtin_amdgcn_s_setprio(0);
    STAMP(9);
  }
  f2 xn[4], xf[4];
#pragma unroll
  for (int j = 0; j < 4; ++j) {
    xf[j] = (f2){bflo(xq[j]), bfhi(xq[j])};
    OPAQUE(xf[j]);
    const float m = __uint_as_float(st2[0]), rr = __uint_as_float(st2[1]);
    const unsigned d = pk2(xf[j][0] - m, xf[j][1] - m);
    const unsigned n = pk2(bflo(d) * rr, bfhi(d) * rr);
    xn[j] = (f2){bflo(n), bfhi(n)};
    OPAQUE(xn[j]);
  }
  int ln3 = __builtin_amdgcn_mbcnt_hi(-1, __builtin_amdgcn_mbcnt_lo(-1, 0));
  OPAQUE(ln3);
  const int tk = w * 8 + (ln3 >> 3), c8 = ln3 & 7;
  unsigned p0a = (w >> 2) * SLOT + (tk & 31) * 256 + (((2 * c8) ^ (tk & 7)) << 4), p1a = p0a ^ 16, ya = L_YI + c8 * 1024 + ((w ^ c8) << 4) + (tk & 7) * 2;
  unsigned oo = (rt * 64 + tk) * 1024 + ct2 * 128 + c8 * 16;
  OPAQUE(p0a); OPAQUE(p1a); OPAQUE(ya); OPAQUE(oo);
  BAR();
  if (w < 4) STAMP(10);
  {
    f4 o0 = lds128f(p0a), o1 = lds128f(p1a);
#pragma unroll
    for (int k4 = 1; k4 < 4; ++k4) {
      o0 += lds128f(p0a + k4 * 12288);
      o1 += lds128f(p1a + k4 * 12288);
    }
    unsigned yv[8];
#pragma unroll
    for (int j = 0; j < 8; ++j) yv[j] = *(const LAS unsigned short*)(size_t)(ya + j * 128);
    f2 o[4] = {{o0[0], o0[1]}, {o0[2], o0[3]}, {o1[0], o1[1]}, {o1[2], o1[3]}}, e[4], sg[4];
#pragma unroll
    for (int j = 0; j < 4; ++j) {
      const f2 z = o[j] * -1.4426950408889634f;
      e[j] = (f2){__builtin_amdgcn_exp2f(z[0]), __builtin_amdgcn_exp2f(z[1])} + 1.0f;
      sg[j] = xn[j] * (f2){bflo(yv[2 * j]), bflo(yv[2 * j + 1])} * o[j];
    }
    u4 res;
#pragma unroll
    for (int j = 0; j < 4; ++j) {
      const f2 t = __builtin_elementwise_fma(sg[j], (f2){__builtin_amdgcn_rcpf(e[j][0]), __builtin_amdgcn_rcpf(e[j][1])}, xf[j]);
      res[j] = pk2(t[0], t[1]);
    }
    *(GAS u4*)(out + oo) = res;
    asm volatile(".Lh3_end:");
  }
  if (w < 4) STAMP(11);
  asm volatile("s_waitcnt vmcnt(0)" ::: "memory");
  STAMP(12);
  if (b == PROBE_B && w == PROBE_W) {
    const unsigned until = tt[12] + 8u * (tt[PROBE_I] - tt[0]);
    for (int it = 0; it < (1 << 20); ++it) { u64 t_; asm volatile("s_memrealtime %0\n\ts_waitcnt lgkmcnt(0)" : "=s"(t_)::"memory"); if ((int)((unsigned)t_ - until) >= 0) break; }
  }
}
}

extern "C" void kernel_launch(void* const* d_in, const int* in_sizes, int n_in, void* d_out, int out_size, void* d_ws, size_t ws_size,
                              hipStream_t stream) {
  (void)in_sizes; (void)n_in; (void)out_size; (void)ws_size;
  (void)hipFuncSetAttribute((const void*)h3_geglu_fused, hipFuncAttributeMaxDynamicSharedMemorySize, LDS_BYTES);
  h3_geglu_fused<<<256, 512, LDS_BYTES, stream>>>((const char*)d_in[0], (const char*)d_in[1], (const char*)d_in[2], (const char*)d_in[3],
                                                  (const char*)d_in[4], (const char*)d_in[5], (const char*)d_in[7], (const char*)d_in[8],
                                                  (const char*)d_in[9], (char*)d_out, (char*)d_ws);
}
```

```cpp
#include <hip/hip_runtime.h>
#include <stdint.h>

#define LAS __attribute__((address_space(3)))
#define GAS __attribute__((address_space(1)))
#define DI __device__ __forceinline__
typedef __bf16 bf8 __attribute__((ext_vector_type(8)));
typedef __bf16 bf2 __attribute__((ext_vector_type(2)));
typedef short s4 __attribute__((ext_vector_type(4)));
typedef float f4 __attribute__((ext_vector_type(4)));
typedef float f2 __attribute__((ext_vector_type(2)));
typedef unsigned u4 __attribute__((ext_vector_type(4)));
typedef unsigned u2 __attribute__((ext_vector_type(2)));
typedef unsigned u2a __attribute__((ext_vector_type(2), aligned(4)));
typedef unsigned long long u64;

namespace {
constexpr unsigned WS_EPOCH = 0;
constexpr unsigned WS_FK = 4096;
constexpr unsigned WS_FS = WS_FK + 256 * 128;
constexpr unsigned WS_FH = WS_FS + 256 * 128;
constexpr unsigned WS_FY = WS_FH + 256 * 128;
constexpr unsigned WS_ROW = WS_FY + 256 * 128;
constexpr unsigned WS_COL = WS_ROW + 2048 * 8;
constexpr unsigned WS_KVT = 1u << 20;
constexpr unsigned WS_YT = 3u << 20;
constexpr unsigned WS_H = 5u << 20;
constexpr unsigned WS_PROF = 32u << 20;
constexpr unsigned SLOT = 49152;
constexpr unsigned L_MISC = 3 * SLOT;
constexpr unsigned L_RS = 159744;
constexpr unsigned L_CS = L_RS + 1024;
constexpr unsigned L_CTL = L_CS + 1024;
constexpr unsigned C_LAND = L_CTL, C_CONS = L_CTL + 40, C_BSYNC = L_CTL + 72, C_PRE = L_CTL + 76, C_WSYNC = L_CTL + 80, C_EPI = L_CTL + 84,
                   C_HPUB = L_CTL + 88, C_GO = L_CTL + 92, C_WD = L_CTL + 96, C_CVIN = L_CTL + 104, C_TAG = L_CTL + 108, C_PAIR = L_CTL + 112,
                   C_RD = L_CTL + 128, C_LD = L_CTL + 144, C_HOK = L_CTL + 160, L_TAG = L_CTL + 192;
constexpr unsigned CV_COPY = 4160, CV_UR = 2 * CV_COPY, CV_CH = CV_UR + 4608;
constexpr unsigned L_CV = L_RS - 2 * CV_CH;
static_assert(L_CV >= 131072, "W_down ring");
constexpr unsigned L_YI = L_CV;
constexpr unsigned LDS_BYTES = 163840;
constexpr int SPIN_CAP = 1 << 18;

DI float bflo(unsigned u) { return __uint_as_float(u << 16); }
DI float bfhi(unsigned u) { return __uint_as_float(u & 0xffff0000u); }
DI unsigned pk2(float a, float b) { f2 v = {a, b}; return __builtin_bit_cast(unsigned, __builtin_convertvector(v, bf2)); }
DI float dot2(unsigned a, bf2 b, float c) { return __builtin_amdgcn_fdot2_f32_bf16(__builtin_bit_cast(bf2, a), b, c, false); }
DI float rbf(float a) { return bflo(pk2(a, a)); }
DI f4 mfma(bf8 a, bf8 b, f4 c) { return __builtin_amdgcn_mfma_f32_16x16x32_bf16(a, b, c, 0, 0, 0); }
DI bf8 lds128(unsigned a) { return *(const LAS bf8*)(size_t)a; }
DI u4 lds128u(unsigned a) { return *(const LAS u4*)(size_t)a; }
DI f4 lds128f(unsigned a) { return *(const LAS f4*)(size_t)a; }
DI void sts128(unsigned a, u4 v) { *(LAS u4*)(size_t)a = v; }
DI void sts128f(unsigned a, f4 v) { *(LAS f4*)(size_t)a = v; }
DI bf8 ldtr(unsigned a0, unsigned a1) {
  s4 lo = __builtin_amdgcn_ds_read_tr16_b64_v4i16((LAS s4*)(size_t)a0);
  s4 hi = __builtin_amdgcn_ds_read_tr16_b64_v4i16((LAS s4*)(size_t)a1);
  return __builtin_bit_cast(bf8, __builtin_shufflevector(lo, hi, 0, 1, 2, 3, 4, 5, 6, 7));
}
#define WAITV(n) asm volatile("s_waitcnt vmcnt(%0)" ::"i"(n) : "memory")
#define WAITL(n) asm volatile("s_waitcnt lgkmcnt(%0)" ::"i"(n) : "memory")
#define SB() __builtin_amdgcn_sched_barrier(0)
#define OPAQUE(x) asm volatile("" : "+v"(x))
#define BAR() do { asm volatile("s_waitcnt lgkmcnt(0)" ::: "memory"); __builtin_amdgcn_s_barrier(); asm volatile("" ::: "memory"); } while (0)

DI void glds2(unsigned v0, unsigned v1, const char* sb, unsigned l) {
  asm volatile("s_mov_b32 m0, %3\n\ts_nop 0\n\tglobal_load_lds_dwordx4 %0, %2\n\tglobal_load_lds_dwordx4 %1, %2 offset:1024"
               ::"v"(v0), "v"(v1), "s"(sb), "s"(l) : "memory");
}
DI void glds4(unsigned v0, unsigned v1, unsigned v2, unsigned v3, const char* sb, unsigned l) {
  asm volatile("s_mov_b32 m0, %5\n\ts_nop 0\n\tglobal_load_lds_dwordx4 %0, %4\n\tglobal_load_lds_dwordx4 %1, %4 offset:1024\n\t"
               "global_load_lds_dwordx4 %2, %4 offset:2048\n\tglobal_load_lds_dwordx4 %3, %4 offset:3072"
               ::"v"(v0), "v"(v1), "v"(v2), "v"(v3), "s"(sb), "s"(l) : "memory");
}
DI void flag_set(char* ws, unsigned off, u64 tag) {
  __hip_atomic_store((GAS u64*)(ws + off), tag, __ATOMIC_RELAXED, __HIP_MEMORY_SCOPE_AGENT);
}
DI void poll(const char* ws, unsigned off, u64 tag) {
#pragma unroll 1
  for (int it = 0; it < SPIN_CAP; ++it) {
    u64 x = __hip_atomic_load((GAS u64*)(ws + off), __ATOMIC_RELAXED, __HIP_MEMORY_SCOPE_AGENT);
    if (__all(x == tag)) break;
    __builtin_amdgcn_s_sleep(4);
  }
  asm volatile("" ::: "memory");
}
DI float fadd(float a, float b) { float r; asm("v_add_f32 %0, %1, %2" : "=v"(r) : "v"(a), "v"(b)); return r; }
DI float fsub(float a, float b) { float r; asm("v_sub_f32 %0, %1, %2" : "=v"(r) : "v"(a), "v"(b)); return r; }
DI float fmul(float a, float b) { float r; asm("v_mul_f32 %0, %1, %2" : "=v"(r) : "v"(a), "v"(b)); return r; }
DI float ffma(float a, float b, float c) { float r; asm("v_fma_f32 %0, %1, %2, %3" : "=v"(r) : "v"(a), "v"(b), "v"(c)); return r; }
DI void poll2(const char* ws, unsigned off0, unsigned off1, u64 tag) {
#pragma unroll 1
  for (int it = 0; it < SPIN_CAP; ++it) {
    const u64 x = __hip_atomic_load((GAS u64*)(ws + off0), __ATOMIC_RELAXED, __HIP_MEMORY_SCOPE_AGENT);
    const u64 y = __hip_atomic_load((GAS u64*)(ws + off1), __ATOMIC_RELAXED, __HIP_MEMORY_SCOPE_AGENT);
    if (__all(x == tag && y == tag)) break;
    __builtin_amdgcn_s_sleep(8);
  }
  asm volatile("" ::: "memory");
}
template <int CTRL> DI float dpp_add(float v) {
  return v + __int_as_float(__builtin_amdgcn_update_dpp(0, __float_as_int(v), CTRL, 0xf, 0xf, true));
}
DI float wave_sum(float v) {
  v = dpp_add<0xB1>(v); v = dpp_add<0x4E>(v); v = dpp_add<0x141>(v); v = dpp_add<0x140>(v);
  const int i = __float_as_int(v);
  return (__int_as_float(__builtin_amdgcn_readlane(i, 0)) + __int_as_float(__builtin_amdgcn_readlane(i, 16))) +
         (__int_as_float(__builtin_amdgcn_readlane(i, 32)) + __int_as_float(__builtin_amdgcn_readlane(i, 48)));
}
DI void ctl_add(unsigned addr) {
  asm volatile("s_waitcnt lgkmcnt(0)\n\ts_mov_b64 exec, 1\n\tds_add_u32 %0, %1\n\ts_mov_b64 exec, -1" ::"v"(addr), "v"(1u) : "memory");
}
DI unsigned ctl_get(unsigned addr) {
  unsigned s, t;
  asm volatile("ds_read_b32 %1, %2\n\ts_waitcnt lgkmcnt(0)\n\tv_readfirstlane_b32 %0, %1\n\ts_nop 1" : "=s"(s), "=&v"(t) : "v"(addr) : "memory");
  return s;
}
DI void ctl_spin(unsigned addr, unsigned target) {
  unsigned t, s, n;
  asm volatile("s_mov_b32 %2, 0x40000\n.Lspin%=:\n\tds_read_b32 %0, %3\n\ts_waitcnt lgkmcnt(0)\n\tv_readfirstlane_b32 %1, %0\n\ts_nop 1\n\t"
               "s_cmp_ge_u32 %1, %4\n\ts_cbranch_scc1 .Lspun%=\n\ts_sub_u32 %2, %2, 1\n\ts_cmp_lg_u32 %2, 0\n\ts_cbranch_scc1 .Lspin%=\n.Lspun%=:"
               : "=&v"(t), "=&s"(s), "=&s"(n) : "v"(addr), "s"(target) : "memory", "scc");
}
DI void ctl_wait(unsigned addr, unsigned target) {
#pragma unroll 1
  for (int it = 0; it < SPIN_CAP; ++it) {
    if (ctl_get(addr) >= target) break;
    __builtin_amdgcn_s_sleep(1);
  }
}
#define STAMP(i)
#define CNOW(v)
#define CSTAMP(i)

#define HWIN 4
#define WREG 6
#define SFIRST 0
#define PROGR 0
#define PRE_FIRST 1
#define CONV_DB 1
#define CPRIO 1
#define APRIO 2
#define PEEK_AT 5
#define SIDE_SLEEP 0
#define CODE_LINES 256
#define PF_W 1
#define PF_CODE 1
#define PF_L2 14
#define S1_EARLY 1
#define BAR_AT 28
#define KBAR() do { if (g1) BAR(); } while (0)
struct Frag { bf8 x[4]; bf8 w[2][4]; };

__global__ __launch_bounds__(512, 2) void h3_geglu_fused(const char* q, const char* kk, const char* vv, const char* shk, const char* ssk,
                                                         const char* sD, const char* wg, const char* wu, const char* wd, char* out, char* ws) {
  extern __shared__ __attribute__((aligned(16))) char smem[];
  u64 kbase;
  asm volatile("s_getpc_b64 %0" : "=s"(kbase));
  const int lane = threadIdx.x & 63, w0 = __builtin_amdgcn_readfirstlane(threadIdx.x >> 6) ^ (SFIRST ? 4 : 0);
  const int b = blockIdx.x, xcd = b & 7, r = b >> 3;
  const bool g1 = r < 28;
  const int l15 = lane & 15, g = lane >> 4;
  const __amdgpu_buffer_rsrc_t wsr = __builtin_amdgcn_make_buffer_rsrc(ws, 0, 16 << 20, 0x00020000);
  const int pnl = r >> 2;
  const int pm = (xcd >> 1) * 4 + (r & 3), pn = (xcd & 1) * 7 + pnl;
  const int rt = xcd * 4 + (r & 3), ct2 = r >> 2;
  auto stage_offsets = [&](unsigned (&xv)[4], unsigned (&wv)[4]) {
#pragma unroll
    for (int p = 0; p < 4; ++p) {
      xv[p] = (p * 8 + (lane >> 3)) * 1024 + (((lane & 7) ^ (((p & 1) << 2) | g)) << 4) - p * 1024;
      wv[p] = (p * 4 + g) * 3584 + ((l15 ^ ((g | ((p >> 1) << 2)) << 1)) << 4) - p * 1024;
    }
  };
  auto quarter = [&](const unsigned (&xv)[4], const unsigned (&wv)[4], int st, int qt) {
    const unsigned l = (st % 3) * SLOT + qt * 4096;
    glds4(xv[0], xv[1], xv[2], xv[3], q + pm * 131072 + qt * 32768 + st * 128, l);
    glds4(wv[0], wv[1], wv[2], wv[3], wg + pn * 256 + (st * 64 + qt * 16) * 3584, l + 16384);
    glds4(wv[0], wv[1], wv[2], wv[3], wu + pn * 256 + (st * 64 + qt * 16) * 3584, l + 32768);
  };
  if (w0 < 4) {
    const unsigned kvo = (lane >> 3) * 1024 + (((lane & 7) ^ ((g & 1) << 2)) << 4);
    const char* kb = kk + (r * 64 + w0 * 16) * 1024 + xcd * 128;
    const char* vb = vv + (r * 64 + w0 * 16) * 1024 + xcd * 128;
    asm volatile("s_mov_b32 m0, %6\n\ts_nop 0\n\tglobal_load_lds_dwordx4 %0, %3\n\tglobal_load_lds_dwordx4 %0, %4 offset:1024\n\t"
                 "global_load_lds_dwordx4 %1, %4 offset:2048\n\tglobal_load_lds_dwordx4 %0, %5 offset:3072\n\ts_add_u32 m0, m0, 4096\n\ts_nop 0\n\t"
                 "global_load_lds_dwordx4 %1, %8\n\tglobal_load_lds_dwordx4 %2, %7 offset:1024"
                 ::"v"(kvo), "v"(kvo + 7168), "v"((lane & 31) * 16), "s"(r == 0 && w0 == 0 ? kb : kb - 8192), "s"(kb - 1024), "s"(vb - 3072),
                   "s"(2 * SLOT + w0 * 6144), "s"(shk + xcd * 512 - 1024), "s"(vb + 1024)
                 : "memory", "scc");
  } else if (w0 < 6) {
    if (g1) {
      unsigned xv[4], wv[4];
      stage_offsets(xv, wv);
      quarter(xv, wv, 0, 2 * (w0 & 1)); quarter(xv, wv, 0, 2 * (w0 & 1) + 1); quarter(xv, wv, 1, 2 * (w0 & 1));
    }
  } else if (PF_CODE && w0 == 7 && lane < 8 && r * 8 + lane < CODE_LINES) {
    asm volatile("s_mov_b32 m0, %2\n\ts_nop 0\n\tglobal_load_lds_dword %0, %1" ::"v"((r * 8 + lane) * 128), "s"(kbase), "s"(L_RS) : "memory");
  }
  int w_ = w0;
  asm volatile("" : "+s"(w_));
  const int w = w_, gw = w & 3, tid = w * 64 + lane, t8 = tid & 255;
  auto wslot = [](int i) -> unsigned { const int s = i & 7; return s < 2 ? 2 * SLOT + s * 4096 : s < 5 ? (s - 2) * 4096 : SLOT + (s - 5) * 4096; };
  auto wdstage = [&](unsigned dv0, int kq, int i) {
    glds4(dv0, dv0 + 7168, dv0 + 2 * 7168, dv0 + 3 * 7168, wd + ct2 * 128 + (kq * 14 + i) * 32768, wslot(i) + kq * ((i & 7) < 2 ? 8192 : 12288));
  };
  u64 tag;
  auto tag_get = [&]() {
    const u2 t = *(const volatile LAS u2*)(size_t)L_TAG;
    tag = ((u64)(unsigned)__builtin_amdgcn_readfirstlane(t[1]) << 32) | (unsigned)__builtin_amdgcn_readfirstlane(t[0]);
  };

  u4 xq; u2 st2;
  auto tail_load = [&]() {
    int ln = __builtin_amdgcn_mbcnt_hi(-1, __builtin_amdgcn_mbcnt_lo(-1, 0));
    OPAQUE(ln);
    const int tk = w * 8 + (ln >> 3), c8 = ln & 7;
    xq = *(const GAS u4*)(q + (rt * 64 + tk) * 1024 + ct2 * 128 + c8 * 16);
    st2 = __builtin_amdgcn_raw_buffer_load_b64(wsr, WS_ROW + (rt * 64 + tk) * 8, 0, 16);
  };

  if (w < 4) {
    {
      const unsigned lk = 2 * SLOT + gw * 6144;
      const bool top = r == 0 && gw == 0;
      const int qq = l15 >> 2, pp = lane & 3;
      unsigned ra = lk + qq * 128 + (((2 * g + (pp >> 1)) ^ ((qq >> 1) << 2)) << 4) + (pp & 1) * 8;
      OPAQUE(ra);
      WAITV(0);
      STAMP(18);
      const u2 sw = *(const LAS u2*)(size_t)(lk + 5120 + lane * 8);
      u2 kr[5], vr[4];
#pragma unroll
      for (int i = 0; i < 5; ++i) kr[i] = __builtin_bit_cast(u2, __builtin_amdgcn_ds_read_tr16_b64_v4i16((LAS s4*)(size_t)(ra + 512 + i * 512)));
#pragma unroll
      for (int i = 0; i < 4; ++i) vr[i] = __builtin_bit_cast(u2, __builtin_amdgcn_ds_read_tr16_b64_v4i16((LAS s4*)(size_t)(ra + 3072 + i * 512)));
      if (top) kr[0] = (u2){0, 0};
      const float w0 = bflo(sw[0]), w1 = bfhi(sw[0]), w2 = bflo(sw[1]), w3 = bfhi(sw[1]);
      float kf[20], u[16];
#pragma unroll
      for (int i = 0; i < 5; ++i) { kf[4 * i] = bflo(kr[i][0]); kf[4 * i + 1] = bfhi(kr[i][0]); kf[4 * i + 2] = bflo(kr[i][1]); kf[4 * i + 3] = bfhi(kr[i][1]); }
      SB();
#pragma unroll
      for (int i = 0; i < 16; ++i) u[i] = fmul(kf[i + 1], w3);
      SB();
#pragma unroll
      for (int i = 0; i < 16; ++i) u[i] = ffma(kf[i + 2], w2, u[i]);
      SB();
#pragma unroll
      for (int i = 0; i < 16; ++i) u[i] = ffma(kf[i + 3], w1, u[i]);
      SB();
#pragma unroll
      for (int i = 0; i < 16; ++i) u[i] = ffma(kf[i + 4], w0, u[i]);
      SB();
#pragma unroll
      for (int i = 0; i < 16; ++i) { const unsigned vw = vr[i >> 2][(i >> 1) & 1]; u[i] = fmul(u[i], (i & 1) ? bfhi(vw) : bflo(vw)); }
      SB();
      const unsigned ad = L_MISC + lane * 128;
      sts128(ad + (((7 - 2 * gw) ^ (lane & 7)) << 4), (u4){pk2(u[7], u[6]), pk2(u[5], u[4]), pk2(u[3], u[2]), pk2(u[1], u[0])});
      sts128(ad + (((6 - 2 * gw) ^ (lane & 7)) << 4), (u4){pk2(u[15], u[14]), pk2(u[13], u[12]), pk2(u[11], u[10]), pk2(u[9], u[8])});
      STAMP(1);
      BAR();
    }
    if (g1) {
      const int qq = l15 >> 2, pp = lane & 3;
      const int wm = w >> 1, wn = w & 1;
      f4 acc[2][4][4];
#pragma unroll
      for (int s = 0; s < 2; ++s)
#pragma unroll
        for (int e = 0; e < 4; ++e)
#pragma unroll
          for (int i = 0; i < 4; ++i) acc[s][e][i] = (f4){0.f, 0.f, 0.f, 0.f};
      unsigned xa[2], wa[4];
      xa[0] = (wm * 64 + l15) * 128 + ((g ^ (l15 >> 1)) << 4); xa[1] = xa[0] ^ 64;
#pragma unroll
      for (int e = 0; e < 4; ++e) wa[e] = (8 * g + qq) * 256 + (((4 * wn + e) ^ (qq | ((g & 1) << 2))) << 5) + pp * 8;
      Frag f0, f1;
      s4 lo_;
      auto ldpart = [&](Frag& f, int ks, int j) {
        const unsigned o = 16384 + ks * 8192;
        if (j == 0) f.x[0] = lds128(xa[ks]);
        if (j >= 1 && j <= 16) {
          const int s = (j - 1) >> 3, e = ((j - 1) >> 1) & 3;
          if (j & 1) lo_ = __builtin_amdgcn_ds_read_tr16_b64_v4i16((LAS s4*)(size_t)(wa[e] + o + s * 16384));
          else {
            const s4 hi = __builtin_amdgcn_ds_read_tr16_b64_v4i16((LAS s4*)(size_t)(wa[e] + o + s * 16384 + 1024));
            f.w[s][e] = __builtin_bit_cast(bf8, __builtin_shufflevector(lo_, hi, 0, 1, 2, 3, 4, 5, 6, 7));
          }
        }
        if (j >= 17 && j < 20) f.x[j - 16] = lds128(xa[ks] + (j - 16) * 2048);
      };
      auto mm1 = [&](const Frag& f, int j) { acc[(j >> 2) & 1][j & 3][j >> 3] = mfma(f.w[(j >> 2) & 1][j & 3], f.x[j >> 3], acc[(j >> 2) & 1][j & 3][j >> 3]); };
      auto second = [&](int st) {
        const int d = (st == 2 || st == 5) ? -2 * (int)SLOT : (int)SLOT;
#pragma unroll
        for (int j = 0; j < 32; ++j) {
          if (j == BAR_AT) { BAR(); SB(); }
          mm1(f0, j);
          if (j < 20) ldpart(f1, 1, j);
          if (j == 20) { xa[0] += d; OPAQUE(xa[0]); xa[1] += d; OPAQUE(xa[1]); }
          if (j == 21) { wa[0] += d; OPAQUE(wa[0]); wa[1] += d; OPAQUE(wa[1]); }
          if (j == 22) { wa[2] += d; OPAQUE(wa[2]); wa[3] += d; OPAQUE(wa[3]); }
          SB();
        }
        if (BAR_AT == 32) { BAR(); SB(); }
      };
      OPAQUE(xa[0]); OPAQUE(xa[1]); OPAQUE(wa[0]); OPAQUE(wa[1]); OPAQUE(wa[2]); OPAQUE(wa[3]);
      STAMP(2); CSTAMP(20);
#pragma unroll
      for (int j = 0; j < 20; ++j) ldpart(f0, 0, j);
      SB();
      second(0);
#pragma unroll 1
      for (int st = 1; st < 8; ++st) {
#pragma unroll
        for (int j = 0; j < 32; ++j) { mm1(f1, j); if (j < 20) ldpart(f0, 0, j); SB(); }
        second(st);
      }
      STAMP(16); CSTAMP(21);
      constexpr float C0 = -2.0f * 0.7978845608028654f * 1.4426950408889634f, C1 = C0 * 0.044715f;
      f4 cs[2][4];
      f2 rs[4];
      int ln1 = __builtin_amdgcn_mbcnt_hi(-1, __builtin_amdgcn_mbcnt_lo(-1, 0));
      OPAQUE(ln1);
      const int lane = ln1, l15 = lane & 15, g = lane >> 4;
#pragma unroll
      for (int j = 0; j < 32; ++j) {
        mm1(f1, j);
        if (j < 8) cs[j >> 2][j & 3] = lds128f(L_CS + ((j >> 2) * 128 + wn * 64 + (j & 3) * 16 + 4 * g) * 4);
        if (j >= 8 && j < 12) rs[j - 8] = *(const LAS f2*)(size_t)(L_RS + (wm * 64 + (j - 8) * 16 + l15) * 8);
        SB();
      }
      STAMP(3);
      const unsigned hso = WS_H + ((pm * 8 + wm * 4) * 56 + pn * 4 + wn * 2) * 1024 + lane * 16;
#pragma unroll
      for (int i = 0; i < 4; ++i) {
        const float m = -rs[i][0], rr = rs[i][1];
        const float k0 = C0 * rr, k1 = C1 * rr * rr * rr, i2 = __builtin_amdgcn_rcpf(rr * rr);
        const f2 m2 = {m, m}, k02 = {k0, k0}, k12 = {k1, k1}, i22 = {i2, i2};
        f2 a[8], c[8], z[8];
        SB();
#pragma unroll
        for (int j = 0; j < 8; ++j) {
          const int e = j >> 1, h = (j & 1) * 2;
          a[j] = __builtin_elementwise_fma(m2, (f2){cs[0][e][h], cs[0][e][h + 1]}, (f2){acc[0][e][i][h], acc[0][e][i][h + 1]});
        }
        SB();
#pragma unroll
        for (int j = 0; j < 8; ++j) z[j] = a[j] * a[j];
        SB();
#pragma unroll
        for (int j = 0; j < 8; ++j) z[j] = __builtin_elementwise_fma(z[j], k12, k02);
        SB();
#pragma unroll
        for (int j = 0; j < 8; ++j) z[j] = z[j] * a[j];
        SB();
#pragma unroll
        for (int j = 0; j < 8; ++j) {
          const int e = j >> 1, h = (j & 1) * 2;
          z[j] = (f2){__builtin_amdgcn_exp2f(z[j][0]), __builtin_amdgcn_exp2f(z[j][1])};
          c[j] = __builtin_elementwise_fma(m2, (f2){cs[1][e][h], cs[1][e][h + 1]}, (f2){acc[1][e][i][h], acc[1][e][i][h + 1]});
          SB();
        }
#pragma unroll
        for (int j = 0; j < 8; ++j) z[j] = __builtin_elementwise_fma(z[j], i22, i22);
        SB();
#pragma unroll
        for (int j = 0; j < 8; ++j) {
          z[j] = (f2){__builtin_amdgcn_rcpf(z[j][0]), __builtin_amdgcn_rcpf(z[j][1])};
          c[j] = c[j] * a[j];
          SB();
        }
#pragma unroll
        for (int j = 0; j < 8; ++j) c[j] = c[j] * z[j];
        SB();
        u4 o0, o1;
#pragma unroll
        for (int j = 0; j < 4; ++j) { o0[j] = pk2(c[j][0], c[j][1]); o1[j] = pk2(c[4 + j][0], c[4 + j][1]); }
        __builtin_amdgcn_raw_buffer_store_b128(o0, wsr, hso + i * 56 * 1024, 0, 16);
        __builtin_amdgcn_raw_buffer_store_b128(o1, wsr, hso + i * 56 * 1024 + 1024, 0, 16);
      }
      SB();
      ctl_add(C_EPI);
      tag_get();
      STAMP(4);
    } else {
      const unsigned o = ((xcd * 4 + r - 28) * 448 + gw * 112 + lane) * 128;
      if (PF_W) asm volatile("s_mov_b32 m0, %3\n\ts_nop 0\n\tglobal_load_lds_dword %0, %2\n\tglobal_load_lds_dword %1, %2"
                             ::"v"(o), "v"(o + (lane < 48 ? 8192 : 0)), "s"(wd), "s"(131072 + gw * 256) : "memory");
      if (PF_L2) {
#pragma unroll 1
        for (int j = (r - 28) * 4 + gw; j < PF_L2 * 16; j += 16) {
          const unsigned o2 = ((((j >> 2) & 3) * 14 + (j >> 4)) * 32 + (j & 3) * 8 + (lane >> 3)) * 1024 + (lane & 7) * 128;
          asm volatile("s_waitcnt vmcnt(0)\n\ts_mov_b32 m0, %2\n\ts_nop 0\n\tglobal_load_lds_dword %0, %1" ::"v"(o2), "s"(wd), "s"(131072 + gw * 256) : "memory");
        }
      }
      WAITV(0);
    }
    int ln2 = __builtin_amdgcn_mbcnt_hi(-1, __builtin_amdgcn_mbcnt_lo(-1, 0));
    OPAQUE(ln2);
    {
      const int lane = ln2, l15 = lane & 15, g = lane >> 4, qq = l15 >> 2, pp = lane & 3;
      const unsigned dv0 = (lane >> 3) * 1024 + (((lane & 7) ^ (g << 1)) * 16);
      f4 acc[4][4];
#pragma unroll
      for (int t = 0; t < 4; ++t)
#pragma unroll
        for (int i = 0; i < 4; ++i) acc[t][i] = (f4){0.f, 0.f, 0.f, 0.f};
      const unsigned da = (4 * g + qq) * 128 + (((2 * g + (qq >> 1)) & 3) << 5) + pp * 8;
      unsigned dlo[4];
#pragma unroll
      for (int t = 0; t < 4; ++t) { dlo[t] = (da ^ (t * 32)) + gw * 12288; OPAQUE(dlo[t]); }
      const unsigned dh = 2 * SLOT - gw * 4096;
      constexpr int WR = 6, HW = HWIN;
      bf8 wf[WR][4];
      auto waddr = [&](int i, int t) -> unsigned {
        if ((i & 7) >= 2) return dlo[t] + wslot(i);
        unsigned d_ = dh;
        asm volatile("" : "+s"(d_));
        unsigned a_ = dlo[t] + d_;
        OPAQUE(a_);
        return a_ + (i & 1) * 4096;
      };
      __builtin_amdgcn_s_setprio(APRIO);
      auto publish = [&]() {
        if (g1) {
          WAITV(0);
          if (lane == 0 && __hip_atomic_fetch_add((LAS unsigned*)(size_t)C_HPUB, 1u, __ATOMIC_RELAXED, __HIP_MEMORY_SCOPE_WORKGROUP) == 3)
            flag_set(ws, WS_FH + (pm * 16 + pn) * 128, tag);
        }
        STAMP(5);
      };
      if (!PRE_FIRST) publish();
      ctl_wait(C_WD + (gw >> 1) * 4, 2);
      if (!g1) tag_get();
#pragma unroll
      for (int t = 0; t < WR * 4; ++t) { const unsigned a_ = waddr(t >> 2, t & 3); wf[t >> 2][t & 3] = ldtr(a_, a_ + 2048); }
      SB();
      if (PRE_FIRST) publish();
      WAITL(0);
      STAMP(17);
#pragma unroll
      for (int i = 8; i < 14; ++i) wdstage(dv0, gw, i);
      __builtin_amdgcn_s_setprio(0);
      STAMP(9);
      const unsigned fh = WS_FH + ((rt >> 1) * 16 + ((gw * 7) >> 1) + (lane & 3)) * 128;
      unsigned have = 0;
      auto need = [&](int i) {
        const unsigned m = PROGR ? 1u << (((gw & 1) * 2 + i) >> 2) : 15u;
        u64 x_;
        unsigned t_, n_;
        asm volatile("s_and_b32 %2, %0, %4\n\ts_cmp_eq_u32 %2, %4\n\ts_cbranch_scc1 .Ldone%=\n\ts_mov_b32 %3, 0x40000\n"
                     ".Lpoll%=:\n\tglobal_load_dwordx2 %1, %5, %6 sc1\n\ts_waitcnt vmcnt(0)\n\tv_cmp_eq_u64 vcc, %7, %1\n\ts_nop 4\n\t"
                     "s_and_b32 %0, vcc_lo, 15\n\ts_and_b32 %2, %0, %4\n\ts_cmp_eq_u32 %2, %4\n\ts_cbranch_scc1 .Ldone%=\n\ts_sleep 2\n\t"
                     "s_sub_u32 %3, %3, 1\n\ts_cmp_lg_u32 %3, 0\n\ts_cbranch_scc1 .Lpoll%=\n.Ldone%=:"
                     : "+s"(have), "=&v"(x_), "=&s"(t_), "=&s"(n_) : "s"(m), "v"(fh), "s"(ws), "s"(tag) : "memory", "vcc", "scc");
        SB();
      };
      need(0);
      STAMP(6);
      u4 hf[HW][4];
      unsigned ho[4];
#pragma unroll
      for (int t = 0; t < 4; ++t) ho[t] = WS_H + ((rt * 4 + t) * 56 + gw * 14) * 1024 + lane * 16;
      auto ldh1 = [&](int i, int t) { hf[i % HW][t] = __builtin_amdgcn_raw_buffer_load_b128(wsr, ho[t] + (i & 3) * 1024, (i >> 2) * 4096, 16); };
#pragma unroll
      for (int i = 0; i < HW * 4; ++i) { if (i == 8) need(2); ldh1(i >> 2, i & 3); SB(); }
      s4 lo_;
      unsigned wa_;
#pragma unroll
      for (int i = 0; i < 14; ++i) {
        const int nw = i + WR - 1;
        if (i == 11) { tail_load(); SB(); }
        if (i + HW < 14 && ((i + HW) & 1) == 0) need(i + HW);
#pragma unroll
        for (int j = 0; j < 16; ++j) {
          const int ti = j >> 2, t = j & 3;
          acc[t][ti] = mfma(wf[i % WR][t], __builtin_bit_cast(bf8, hf[i % HW][ti]), acc[t][ti]);
          if (i >= 1 && nw < 14 && t != 3 && (j - ti) < 8) {
            const int e = j - ti;
            if (!(e & 1)) wa_ = waddr(nw, e >> 1);
            const s4 v_ = __builtin_amdgcn_ds_read_tr16_b64_v4i16((LAS s4*)(size_t)(wa_ + (e & 1) * 2048));
            if (e & 1) wf[nw % WR][e >> 1] = __builtin_bit_cast(bf8, __builtin_shufflevector(lo_, v_, 0, 1, 2, 3, 4, 5, 6, 7)); else lo_ = v_;
          }
          if (t == 3 && i + HW < 14) ldh1(i + HW, ti);
          SB();
        }
        if (i == 0) STAMP(19);
      }
      STAMP(7);
#pragma unroll
      for (int ti = 0; ti < 4; ++ti)
#pragma unroll
        for (int t = 0; t < 4; ++t)
          sts128f((ti >> 1) * SLOT + gw * 12288 + ((ti & 1) * 16 + l15) * 256 + (((4 * t + g) ^ (l15 & 7)) << 4), acc[t][ti]);
    }
    STAMP(8);
  } else {
    const int t7 = tid & 127, bw = w & 1;
    unsigned dcr;
    if (w < 6) {
      unsigned xv[4], wv[4];
      stage_offsets(xv, wv);
      auto stage = [&](int st) { quarter(xv, wv, st, 2 * bw); quarter(xv, wv, st, 2 * bw + 1); };
      const unsigned dv0 = (lane >> 3) * 1024 + (((lane & 7) ^ (g << 1)) * 16);
      WAITV(12);
      STAMP(1);
      BAR();
      if (g1) { quarter(xv, wv, 1, 2 * bw + 1); stage(2); WAITV(24); }
      KBAR();
#pragma unroll 1
      for (int st = 3; st < 8; ++st) {
        if (g1) { stage(st); WAITV(24); }
        KBAR();
      }
      STAMP(13);
      if (!g1) ctl_wait(C_TAG, 2);
      asm volatile("global_load_dword %0, %1, %2" : "=&v"(dcr) : "v"(0u), "s"(sD + b * 4) : "memory");
#pragma unroll
      for (int i = 0; i < 4; ++i) wdstage(dv0, bw * 2 + (i & 1), i >> 1);
      WAITV(17);
      KBAR();
      STAMP(3);
#pragma unroll
      for (int i = 0; i < 6; ++i) wdstage(dv0, bw * 2 + (i & 1), 2 + (i >> 1));
      KBAR();
      STAMP(19);
      tag_get();
      asm volatile("s_waitcnt vmcnt(32)" : "+v"(dcr)::"memory");
#pragma unroll
      for (int i = 0; i < 6; ++i) wdstage(dv0, bw * 2 + (i & 1), 5 + (i >> 1));
      WAITV(24);
      ctl_add(C_WD + bw * 4);
      STAMP(2);
      WAITV(0);
      ctl_add(C_WD + bw * 4);
      STAMP(4);
    } else {
      if (t7 < 48) *(LAS unsigned*)(size_t)(L_CTL + t7 * 4) = 0;
      const int ptt = b >> 3, pct = b & 7;
      const int rgrp = (xcd >> 1) * 64 + (xcd & 1) * 32 + r;
      const int cst = 2 * ((r + pnl) & 3) + ((xcd >> 1) & 1);
      const unsigned hoff = (2 * b + bw) * 4096 + lane * 16;
      u4 xr[4];
      auto rows_load = [&]() {
        asm volatile("global_load_dwordx4 %0, %4, %5\n\tglobal_load_dwordx4 %1, %4, %5 offset:1024\n\tglobal_load_dwordx4 %2, %4, %5 offset:2048\n\t"
                     "global_load_dwordx4 %3, %4, %5 offset:3072"
                     : "=&v"(xr[0]), "=&v"(xr[1]), "=&v"(xr[2]), "=&v"(xr[3]) : "v"(lane * 16), "s"(q + (rgrp * 8 + (w & 1) * 4) * 1024) : "memory");
      };
      auto rows_stat = [&]() {
        asm volatile("" : "+v"(xr[0]), "+v"(xr[1]), "+v"(xr[2]), "+v"(xr[3])::"memory");
        float e[4][8], mu[4], va[4];
#pragma unroll
        for (int i = 0; i < 4; ++i) {
#pragma unroll
          for (int j = 0; j < 4; ++j) { e[i][2 * j] = bflo(xr[i][j]); e[i][2 * j + 1] = bfhi(xr[i][j]); }
          mu[i] = fadd(fadd(fadd(e[i][0], e[i][1]), fadd(e[i][2], e[i][3])), fadd(fadd(e[i][4], e[i][5]), fadd(e[i][6], e[i][7])));
        }
        auto fold4 = [&](const float (&v)[4]) {
          const auto s0 = __builtin_amdgcn_permlane16_swap(__float_as_uint(v[0]), __float_as_uint(v[1]), false, false);
          const auto s1 = __builtin_amdgcn_permlane16_swap(__float_as_uint(v[2]), __float_as_uint(v[3]), false, false);
          const float p0 = fadd(__uint_as_float(s0[0]), __uint_as_float(s0[1])), p1 = fadd(__uint_as_float(s1[0]), __uint_as_float(s1[1]));
          const auto s2 = __builtin_amdgcn_permlane32_swap(__float_as_uint(p0), __float_as_uint(p1), false, false);
          float t = fadd(__uint_as_float(s2[0]), __uint_as_float(s2[1]));
          t = dpp_add<0xB1>(t); t = dpp_add<0x4E>(t); t = dpp_add<0x141>(t); t = dpp_add<0x140>(t);
          return t * (1.0f / 512.0f);
        };
        const float mg = fold4(mu);
#pragma unroll
        for (int i = 0; i < 4; ++i) {
          const float m = __int_as_float(__builtin_amdgcn_readlane(__float_as_int(mg), 16 * i));
          float s = 0.f;
#pragma unroll
          for (int j = 0; j < 8; ++j) { const float d = e[i][j] - m; s = ffma(d, d, s); }
          va[i] = s;
        }
        const float vg = fold4(va);
        const u2 o = {__float_as_uint(rbf(mg)), __float_as_uint(__builtin_amdgcn_rcpf(rbf(__builtin_amdgcn_sqrtf(rbf(rbf(vg) + 1e-5f)))))};
        if (l15 == 0) __builtin_amdgcn_raw_buffer_store_b64(o, wsr, WS_ROW + (rgrp * 8 + (w & 1) * 4 + g) * 8, 0, 16);
        asm volatile("" ::: "memory");
      };

      u4 wsh[8], hc[4], uc[4], c1[4]; unsigned hp[4];
      {
        const char* wb = ((xcd >> 2) ? wu : wg) + (g1 ? pn * 256 + (cst * 64 + bw * 32) * 3584 : 0);
        const unsigned o = g * 3584 + l15 * 16;
        if (SIDE_SLEEP) __builtin_amdgcn_s_sleep(SIDE_SLEEP);
        asm volatile("global_load_dwordx4 %0, %8, %12\n\tglobal_load_dwordx4 %1, %9, %12\n\tglobal_load_dwordx4 %2, %10, %12\n\tglobal_load_dwordx4 %3, %11, %12\n\t"
                     "global_load_dwordx4 %4, %8, %13\n\tglobal_load_dwordx4 %5, %9, %13\n\tglobal_load_dwordx4 %6, %10, %13\n\tglobal_load_dwordx4 %7, %11, %13"
                     : "=&v"(wsh[0]), "=&v"(wsh[1]), "=&v"(wsh[2]), "=&v"(wsh[3]), "=&v"(wsh[4]), "=&v"(wsh[5]), "=&v"(wsh[6]), "=&v"(wsh[7])
                     : "v"(o), "v"(o + 4 * 3584), "v"(o + 8 * 3584), "v"(o + 12 * 3584), "s"(wb), "s"(wb + 16 * 3584) : "memory");
        rows_load();
      }
      asm volatile("s_load_dwordx2 %0, %1, 0x0\n\ts_waitcnt lgkmcnt(0)" : "=s"(tag) : "s"(ws) : "memory");
      tag += 1;
      *(LAS u64*)(size_t)L_TAG = tag;
      BAR();
#pragma unroll
      for (int z = 0; z < 4; ++z) {
        const int id = t7 + 128 * z, c2 = id >> 3, ch = id & 7;
        const u4 t = lds128u(L_MISC + c2 * 128 + ((ch ^ (c2 & 7)) << 4));
        __builtin_amdgcn_raw_buffer_store_b128(t, wsr, WS_KVT + (pct * 64 + c2) * 4096 + 3968 - ptt * 128 + ch * 16, 0, 16);
      }
      asm volatile("s_waitcnt vmcnt(4)" : "+v"(wsh[0]), "+v"(wsh[1]), "+v"(wsh[2]), "+v"(wsh[3]), "+v"(wsh[4]), "+v"(wsh[5]), "+v"(wsh[6]), "+v"(wsh[7])::"memory");
      STAMP(17);
      if (g1) {
        float cacc[8];
#pragma unroll
        for (int j = 0; j < 4; ++j) {
          cacc[2 * j] = fadd(fadd(fadd(bflo(wsh[0][j]), bflo(wsh[1][j])), fadd(bflo(wsh[2][j]), bflo(wsh[3][j]))),
                             fadd(fadd(bflo(wsh[4][j]), bflo(wsh[5][j])), fadd(bflo(wsh[6][j]), bflo(wsh[7][j]))));
          cacc[2 * j + 1] = fadd(fadd(fadd(bfhi(wsh[0][j]), bfhi(wsh[1][j])), fadd(bfhi(wsh[2][j]), bfhi(wsh[3][j]))),
                                 fadd(fadd(bfhi(wsh[4][j]), bfhi(wsh[5][j])), fadd(bfhi(wsh[6][j]), bfhi(wsh[7][j]))));
        }
        float c4[4], c2[2];
#pragma unroll
        for (int j = 0; j < 4; ++j) {
          const auto s2 = __builtin_amdgcn_permlane16_swap(__float_as_uint(cacc[j]), __float_as_uint(cacc[4 + j]), false, false);
          c4[j] = fadd(__uint_as_float(s2[0]), __uint_as_float(s2[1]));
        }
#pragma unroll
        for (int j = 0; j < 2; ++j) {
          const auto s2 = __builtin_amdgcn_permlane32_swap(__float_as_uint(c4[j]), __float_as_uint(c4[2 + j]), false, false);
          c2[j] = fadd(__uint_as_float(s2[0]), __uint_as_float(s2[1]));
        }
        __builtin_amdgcn_raw_buffer_store_b64((u2){__float_as_uint(c2[0]), __float_as_uint(c2[1])}, wsr,
                                              WS_COL + ((pn * 16 + cst * 2 + bw) * 256 + (xcd >> 2) * 128 + l15 * 8 + (g & 1) * 4 + (g >> 1) * 2) * 4, 0, 16);
      }
      STAMP(10);
      KBAR();
      rows_stat();
      STAMP(18);
      asm volatile("global_load_dwordx4 %0, %9, %11\n\tglobal_load_dwordx4 %1, %9, %11 offset:1024\n\tglobal_load_dwordx4 %2, %9, %11 offset:2048\n\t"
                   "global_load_dwordx4 %3, %9, %11 offset:3072\n\tglobal_load_dword %4, %10, %11\n\tglobal_load_dword %5, %9, %11 offset:1020\n\t"
                   "global_load_dword %6, %9, %11 offset:2044\n\tglobal_load_dword %7, %9, %11 offset:3068\n\tglobal_load_dword %8, %12, %13"
                   : "=&v"(hc[0]), "=&v"(hc[1]), "=&v"(hc[2]), "=&v"(hc[3]), "=&v"(hp[0]), "=&v"(hp[1]), "=&v"(hp[2]), "=&v"(hp[3]), "=&v"(dcr)
                   : "v"(hoff), "v"(lane ? hoff - 4 : hoff), "s"(ssk), "v"(0u), "s"(sD + b * 4) : "memory");
      KBAR();
      WAITV(9);
      KBAR();
      if (!g1) { ctl_add(C_WSYNC); ctl_spin(C_WSYNC, 2); }
      if (tid == 384 || tid == 385) flag_set(ws, tid == 384 ? WS_FS + rgrp * 128 : WS_FK + (pct * 32 + ptt) * 128, tag);
      STAMP(20);
      unsigned fo = lane < 32 || !g1 ? WS_FK + ((b >> 5) * 32 + (lane & 31)) * 128
                                     : WS_FS + (lane < 48 ? pm * 16 + l15 : (l15 >> 2) * 64 + (xcd & 1) * 32 + (l15 & 3) + 4 * pnl) * 128;
      unsigned fo2 = WS_FS + (rt * 8 + (lane & 7)) * 128;
      OPAQUE(fo); OPAQUE(fo2);
      KBAR();
      asm volatile("s_waitcnt vmcnt(0)" : "+v"(hc[0]), "+v"(hc[1]), "+v"(hc[2]), "+v"(hc[3]), "+v"(hp[0]), "+v"(hp[1]), "+v"(hp[2]), "+v"(hp[3]), "+v"(dcr)::"memory");
      if (lane == 0) hp[0] = 0;
#pragma unroll
      for (int z = 0; z < 4; ++z) {
        c1[z] = (u4){__builtin_amdgcn_alignbit(hc[z][0], hp[z], 16), __builtin_amdgcn_alignbit(hc[z][1], hc[z][0], 16),
                     __builtin_amdgcn_alignbit(hc[z][2], hc[z][1], 16), __builtin_amdgcn_alignbit(hc[z][3], hc[z][2], 16)};
        OPAQUE(c1[z]);
      }
      u64 p0, p1;
      if (!g1) __builtin_amdgcn_s_sleep(70);
      if (PEEK_AT == 5) KBAR();
      asm volatile("global_load_dwordx2 %0, %2, %4 sc1\n\tglobal_load_dwordx2 %1, %3, %4 sc1" : "=&v"(p0), "=&v"(p1) : "v"(fo), "v"(fo2), "s"(ws) : "memory");
      KBAR();
      STAMP(23);
      asm volatile("s_waitcnt vmcnt(0)" : "+v"(p0), "+v"(p1)::"memory");
      if (!__all(p0 == tag && p1 == tag)) poll2(ws, fo, fo2, tag);
      if (!g1) ctl_add(C_TAG);
      STAMP(21);
      u2 rsv, csv[16];
      {
        const unsigned co = WS_COL + (g1 ? pn : 0) * 16384 + t7 * 8;
#define LD4(o, vreg) asm volatile("global_load_dwordx2 %0, %4, %5 sc1\n\tglobal_load_dwordx2 %1, %4, %5 offset:1024 sc1\n\t" \
                                  "global_load_dwordx2 %2, %4, %5 offset:2048 sc1\n\tglobal_load_dwordx2 %3, %4, %5 offset:3072 sc1" \
                                  : "=&v"(csv[o]), "=&v"(csv[o + 1]), "=&v"(csv[o + 2]), "=&v"(csv[o + 3]) : "v"(vreg), "s"(ws) : "memory")
        LD4(0, co); LD4(4, co + 4096); LD4(8, co + 8192); LD4(12, co + 12288);
        asm volatile("global_load_dwordx2 %0, %1, %2 sc1" : "=&v"(rsv) : "v"(WS_ROW + (g1 ? pm : 0) * 1024 + t7 * 8), "s"(ws) : "memory");
      }
      asm volatile("global_load_dwordx4 %0, %4, %5 sc1\n\tglobal_load_dwordx4 %1, %4, %5 offset:1024 sc1\n\tglobal_load_dwordx4 %2, %4, %5 offset:2048 sc1\n\t"
                   "global_load_dwordx4 %3, %4, %5 offset:3072 sc1" : "=&v"(uc[0]), "=&v"(uc[1]), "=&v"(uc[2]), "=&v"(uc[3]) : "v"(WS_KVT + hoff), "s"(ws) : "memory");
      if (PEEK_AT == 4) KBAR();
      const unsigned base = L_CV + bw * CV_CH;
#pragma unroll
      for (int z = 0; z < 4; ++z) {
        sts128(base + 32 + lane * 16 + z * 1024, hc[z]);
        sts128(base + 32 + lane * 16 + z * 1024 + CV_COPY, c1[z]);
      }
      if (lane < 4) sts128(base + (lane >> 1) * CV_COPY + (lane & 1) * 16, (u4){0, 0, 0, 0});
      if (lane >= 32) sts128(base + CV_UR + 4096 + (lane - 32) * 16, (u4){0, 0, 0, 0});
      STAMP(14);
      KBAR();
      asm volatile("s_waitcnt vmcnt(4)" : "+v"(rsv), "+v"(csv[0]), "+v"(csv[1]), "+v"(csv[2]), "+v"(csv[3]), "+v"(csv[4]), "+v"(csv[5]), "+v"(csv[6]), "+v"(csv[7]),
                   "+v"(csv[8]), "+v"(csv[9]), "+v"(csv[10]), "+v"(csv[11]), "+v"(csv[12]), "+v"(csv[13]), "+v"(csv[14]), "+v"(csv[15])::"memory");
      {
        *(LAS u2*)(size_t)(L_RS + t7 * 8) = rsv;
        float s0[4], s1[4];
#pragma unroll
        for (int i = 0; i < 4; ++i) {
          s0[i] = fadd(fadd(__uint_as_float(csv[i][0]), __uint_as_float(csv[4 + i][0])), fadd(__uint_as_float(csv[8 + i][0]), __uint_as_float(csv[12 + i][0])));
          s1[i] = fadd(fadd(__uint_as_float(csv[i][1]), __uint_as_float(csv[4 + i][1])), fadd(__uint_as_float(csv[8 + i][1]), __uint_as_float(csv[12 + i][1])));
        }
        *(LAS u2*)(size_t)(L_CS + t7 * 8) = (u2){__float_as_uint(fadd(fadd(s0[0], s0[1]), fadd(s0[2], s0[3]))), __float_as_uint(fadd(fadd(s1[0], s1[1]), fadd(s1[2], s1[3])))};
      }
      STAMP(16);
      KBAR();
      if (b == 255 && bw == 1) {
#pragma unroll 1
        for (int z = 0; z < 4; ++z) poll(ws, WS_FS + (z * 64 + lane) * 128, tag);
        if (lane == 0) flag_set(ws, WS_EPOCH, tag);
      }
      asm volatile("s_waitcnt vmcnt(0)" : "+v"(uc[0]), "+v"(uc[1]), "+v"(uc[2]), "+v"(uc[3])::"memory");
#pragma unroll
      for (int z = 0; z < 4; ++z) sts128(base + CV_UR + lane * 16 + z * 1024, uc[z]);
      ctl_add(C_CVIN);
      STAMP(15);
    }
    ctl_wait(C_CVIN, 2);
    const int ch = gw >> 1, half = gw & 1;
    const unsigned base = L_CV + ch * CV_CH;
    const unsigned o = 8 * g + l15 + 1, kap = o & 1;
    unsigned aa = base + kap * CV_COPY + 2 * (o + kap) + half * 256;
    unsigned ba = base + CV_UR + 2 * (2032 - 16 * l15 + 8 * g) - 3584 + half * 256;
    unsigned ab = aa + 8;
    OPAQUE(aa); OPAQUE(ab); OPAQUE(ba);
    f4 y[8];
#pragma unroll
    for (int n = 0; n < 8; ++n) y[n] = (f4){0.f, 0.f, 0.f, 0.f};
    bf8 A[2][8], B[2][8];
    auto ldab = [&](int rho, int i) {
      const u2a lo = *(const LAS u2a*)(size_t)(aa + rho * 64 + i * 512), hi = *(const LAS u2a*)(size_t)(ab + rho * 64 + i * 512);
      A[rho & 1][i] = __builtin_bit_cast(bf8, (u4){lo[0], lo[1], hi[0], hi[1]});
      B[rho & 1][i] = lds128(ba + rho * 64 + (7 - i) * 512);
    };
#pragma unroll
    for (int i = 0; i < 8; ++i) ldab(0, i);
    SB();
    if (g1) ctl_spin(C_EPI, 4);
    STAMP(5);
    __builtin_amdgcn_s_setprio(CPRIO);
#pragma unroll
    for (int rho = 0; rho < 4; ++rho) {
      int cnt = 0;
#pragma unroll
      for (int lv = 0; lv < 8; ++lv)
#pragma unroll
        for (int qi = 0; qi <= lv; ++qi)
#pragma unroll
          for (int p = 0; p <= lv; ++p)
            if ((qi == lv || p == lv) && qi + p < 8) {
              y[qi + p] = mfma(A[rho & 1][qi], B[rho & 1][p], y[qi + p]);
              if (rho < 3 && (cnt & 3) == 0 && (cnt >> 2) < 8) ldab(rho + 1, cnt >> 2);
              ++cnt;
              SB();
            }
    }
    STAMP(6);
    ctl_add(C_PAIR + ch * 4); ctl_spin(C_PAIR + ch * 4, 2);
    const unsigned ex0 = base + half * 4096, ex1 = base + (half ^ 1) * 4096;
#pragma unroll
    for (int n = 0; n < 4; ++n) {
      if (half) { const f4 t = y[n]; y[n] = y[4 + n]; y[4 + n] = t; }
      sts128f(ex0 + n * 1024 + lane * 16, y[4 + n]);
    }
    u2 uu[4];
#pragma unroll
    for (int n = 0; n < 4; ++n) uu[n] = *(const LAS u2*)(size_t)(base + CV_UR + 2 * (2044 - (256 * (4 * half + n) + 16 * l15 + 4 * g)));
    ctl_add(C_PAIR + ch * 4);
    const float dc = ch ? bfhi(dcr) : bflo(dcr);
#pragma unroll
    for (int n = 0; n < 4; ++n) {
      y[n][0] = ffma(bfhi(uu[n][1]), dc, y[n][0]); y[n][1] = ffma(bflo(uu[n][1]), dc, y[n][1]);
      y[n][2] = ffma(bfhi(uu[n][0]), dc, y[n][2]); y[n][3] = ffma(bflo(uu[n][0]), dc, y[n][3]);
    }
    ctl_spin(C_PAIR + ch * 4, 4);
    STAMP(11);
    u2 pk[4];
#pragma unroll
    for (int n = 0; n < 4; ++n) {
      const f4 t = lds128f(ex1 + n * 1024 + lane * 16);
      pk[n] = (u2){pk2(fadd(t[0], y[n][0]), fadd(t[1], y[n][1])), pk2(fadd(t[2], y[n][2]), fadd(t[3], y[n][3]))};
    }
#pragma unroll
    for (int n = 0; n < 4; n += 2) {
      const auto s0 = __builtin_amdgcn_permlane16_swap(pk[n][0], pk[n + 1][0], false, false);
      const auto s1 = __builtin_amdgcn_permlane16_swap(pk[n][1], pk[n + 1][1], false, false);
      const u4 o4 = {s0[0], s1[0], s0[1], s1[1]};
      __builtin_amdgcn_raw_buffer_store_b128(o4, wsr, WS_YT + (2 * b + ch) * 4096 + 2 * (256 * (4 * half + n + (g & 1)) + 16 * l15 + 8 * (g >> 1)), 0, 16);
    }
    STAMP(22);
    WAITV(0);
    ctl_add(C_BSYNC);
    ctl_spin(C_BSYNC, 4);
    if (tid == 256) flag_set(ws, WS_FY + b * 128, tag);
    STAMP(7);
    tail_load();
    if (gw == 3) {
      poll(ws, WS_FY + (ct2 * 32 + (lane & 31)) * 128, tag);
      ctl_add(C_GO);
    } else ctl_wait(C_GO, 1);
    STAMP(8);
    u4 yq[2];
#pragma unroll
    for (int z = 0; z < 2; ++z) {
      const int id = t8 + 256 * z;
      yq[z] = __builtin_amdgcn_raw_buffer_load_b128(wsr, WS_YT + (ct2 * 64 + (id >> 3)) * 4096 + rt * 128 + (id & 7) * 16, 0, 16);
    }
#pragma unroll
    for (int z = 0; z < 2; ++z) {
      const int id = t8 + 256 * z, c = id >> 3;
      sts128(L_YI + c * 128 + (((id & 7) ^ (c >> 3)) << 4), yq[z]);
    }
    __builtin_amdgcn_s_setprio(0);
    STAMP(9);
  }
  f2 xn[4];
#pragma unroll
  for (int j = 0; j < 4; ++j) {
    const float m = __uint_as_float(st2[0]), rr = __uint_as_float(st2[1]);
    const unsigned d = pk2(bflo(xq[j]) - m, bfhi(xq[j]) - m);
    const unsigned n = pk2(bflo(d) * rr, bfhi(d) * rr);
    xn[j] = (f2){bflo(n), bfhi(n)};
    OPAQUE(xn[j]);
  }
  BAR();
  if (w < 4) STAMP(10);
  {
    int ln3 = __builtin_amdgcn_mbcnt_hi(-1, __builtin_amdgcn_mbcnt_lo(-1, 0));
    OPAQUE(ln3);
    const int tk = w * 8 + (ln3 >> 3), c8 = ln3 & 7;
    const unsigned pb = (w >> 2) * SLOT + (tk & 31) * 256;
    f4 o0 = lds128f(pb + (((2 * c8) ^ (tk & 7)) << 4)), o1 = lds128f(pb + (((2 * c8 + 1) ^ (tk & 7)) << 4));
#pragma unroll
    for (int k4 = 1; k4 < 4; ++k4) {
      o0 += lds128f(pb + k4 * 12288 + (((2 * c8) ^ (tk & 7)) << 4));
      o1 += lds128f(pb + k4 * 12288 + (((2 * c8 + 1) ^ (tk & 7)) << 4));
    }
    const unsigned ya = L_YI + c8 * 1024 + ((w ^ c8) << 4) + (tk & 7) * 2;
    unsigned yv[8];
#pragma unroll
    for (int j = 0; j < 8; ++j) yv[j] = *(const LAS unsigned short*)(size_t)(ya + j * 128);
    f2 o[4] = {{o0[0], o0[1]}, {o0[2], o0[3]}, {o1[0], o1[1]}, {o1[2], o1[3]}}, e[4], sg[4];
#pragma unroll
    for (int j = 0; j < 4; ++j) {
      const f2 z = o[j] * -1.4426950408889634f;
      e[j] = (f2){__builtin_amdgcn_exp2f(z[0]), __builtin_amdgcn_exp2f(z[1])} + 1.0f;
      sg[j] = xn[j] * (f2){bflo(yv[2 * j]), bflo(yv[2 * j + 1])} * o[j];
    }
    u4 res;
#pragma unroll
    for (int j = 0; j < 4; ++j) {
      const f2 t = __builtin_elementwise_fma(sg[j], (f2){__builtin_amdgcn_rcpf(e[j][0]), __builtin_amdgcn_rcpf(e[j][1])}, (f2){bflo(xq[j]), bfhi(xq[j])});
      res[j] = pk2(t[0], t[1]);
    }
    *(GAS u4*)(out + (rt * 64 + tk) * 1024 + ct2 * 128 + c8 * 16) = res;
  }
  if (w < 4) STAMP(11);
}
}

extern "C" void kernel_launch(void* const* d_in, const int* in_sizes, int n_in, void* d_out, int out_size, void* d_ws, size_t ws_size,
                              hipStream_t stream) {
  (void)in_sizes; (void)n_in; (void)out_size; (void)ws_size;
  (void)hipFuncSetAttribute((const void*)h3_geglu_fused, hipFuncAttributeMaxDynamicSharedMemorySize, LDS_BYTES);
  h3_geglu_fused<<<256, 512, LDS_BYTES, stream>>>((const char*)d_in[0], (const char*)d_in[1], (const char*)d_in[2], (const char*)d_in[3],
                                                  (const char*)d_in[4], (const char*)d_in[5], (const char*)d_in[7], (const char*)d_in[8],
                                                  (const char*)d_in[9], (char*)d_out, (char*)d_ws);
}
```
